```python
import torch

def _softmax(x, axis=-1):
    return torch.softmax(x, dim=axis)

def _gelu(x):
    return torch.nn.functional.gelu(x, approximate='tanh')

def _silu(x):
    return torch.nn.functional.silu(x)

def _sigmoid(x):
    return torch.sigmoid(x)

def _layernorm(x, axis=-1, eps=1e-05):
    m = x.mean(dim=axis, keepdim=True)
    v = x.var(dim=axis, keepdim=True, unbiased=False)
    return (x - m) / torch.sqrt(v + eps)

def _scatter_add(tgt, idx, src, axis=0):
    return tgt.clone().index_add_(axis, idx, src)

def _segment_sum(src, seg, n):
    out = torch.zeros((n,) + src.shape[1:], dtype=src.dtype)
    return out.index_add_(0, seg, src)

def _pad(x, pw):
    flat = tuple((v for pair in reversed(pw) for v in pair))
    return torch.nn.functional.pad(x, flat)
kernel_name = 'adaLN_cond__linear_kernel_hedgehog__geglu_ffn__prompt'

def setup_inputs():

    _w = lambda a: (lambda t: t.to(torch.bfloat16) if t.is_floating_point() else t)(torch.as_tensor(a))

    def _A_setup():
        B, S, D, H = (1, 2048, 448, 16)
        Dh = D // H
        return {'x': _w((lambda *s: torch.randn(*s, dtype=torch.bfloat16) * 0.05)(B, S, D)), 'wqkv': _w((lambda *s: torch.randn(*s, dtype=torch.bfloat16) * 0.4)(D, 3 * D)), 'w_phi': _w((lambda *s: torch.randn(*s, dtype=torch.bfloat16) * 0.4)(H, Dh, Dh) * Dh ** (-0.5)), 'wo': _w((lambda *s: torch.randn(*s, dtype=torch.bfloat16) * 0.05)(D, D))}
    _A_in = _A_setup()

    def _B_setup():
        B, S, D, F = (1, 2048, 448, 1792)
        return {'x': _w((lambda *s: torch.randn(*s, dtype=torch.bfloat16) * 0.05)(B, S, D)), 'w_gate': _w((lambda *s: torch.randn(*s, dtype=torch.bfloat16) * 0.05)(D, F)), 'w_up': _w((lambda *s: torch.randn(*s, dtype=torch.bfloat16) * 0.05)(D, F)), 'w_down': _w((lambda *s: torch.randn(*s, dtype=torch.bfloat16) * 0.05)(F, D))}
    _B_in = _B_setup()
    return {'A_x': _A_in['x'], 'A_wqkv': _A_in['wqkv'], 'A_w_phi': _A_in['w_phi'], 'A_wo': _A_in['wo'], 'B_x': _B_in['x'], 'B_w_gate': _B_in['w_gate'], 'B_w_up': _B_in['w_up'], 'B_w_down': _B_in['w_down'], 'c': _w((lambda *s: torch.randn(*s, dtype=torch.bfloat16) * 0.1)(1, 448) * 8.0), 'w_mod': _w((lambda *s: torch.randn(*s, dtype=torch.bfloat16) * 0.1)(448, 6 * 448) * 448 ** (-0.5) * 8.0)}

def reference(A_x, A_wqkv, A_w_phi, A_wo, B_x, B_w_gate, B_w_up, B_w_down, c, w_mod):

    def _A_ref(x, wqkv, w_phi, wo):
        B, S, D = x.shape
        H = 16
        Dh = D // H
        qkv = (lambda x, *d: x.permute(*d))((x @ wqkv).reshape(B, S, 3, H, Dh), 2, 0, 3, 1, 4)
        q, k, v = (qkv[0], qkv[1], qkv[2])
        pq = torch.einsum('bhsd,hdf->bhsf', q, w_phi)
        pk = torch.einsum('bhsd,hdf->bhsf', k, w_phi)
        phi_q = torch.concatenate([_softmax(pq, axis=-1), _softmax(-pq, axis=-1)], axis=-1)
        phi_k = torch.concatenate([_softmax(pk, axis=-1), _softmax(-pk, axis=-1)], axis=-1)
        kv = torch.einsum('bhsf,bhsd->bhfd', phi_k, v)
        z = torch.einsum('bhsf->bhf', phi_k)
        num = torch.einsum('bhqf,bhfd->bhqd', phi_q, kv)
        den = torch.einsum('bhqf,bhf->bhq', phi_q, z)[..., None] + 1e-06
        o = num / den
        return (lambda x, *d: x.permute(*d))(o, 0, 2, 1, 3).reshape(B, S, D) @ wo

    def _B_ref(x, w_gate, w_up, w_down):
        return _gelu(x @ w_gate) * (x @ w_up) @ w_down
    x = A_x
    mod = (_silu(c) @ w_mod).reshape(c.shape[0], 6, 1, -1)
    sa, ca, ga, sb, cb, gb = (mod[:, 0].clone(), mod[:, 1].clone(), mod[:, 2].clone(), mod[:, 3].clone(), mod[:, 4].clone(), mod[:, 5].clone())
    h = _layernorm(x, axis=-1) * (1 + ca) + sa
    x = x + ga * _A_ref(h, A_wqkv, A_w_phi, A_wo)
    h = _layernorm(x, axis=-1) * (1 + cb) + sb
    x = x + gb * _B_ref(h, B_w_gate, B_w_up, B_w_down)
    return x
```

```cpp
#include <hip/hip_runtime.h>
#include <hip/hip_bf16.h>
typedef unsigned short u16;
#define S 2048
#define D 448
#define H 16
#define DH 28
#define F 1792
__device__ inline float b2f(u16 b){ return __uint_as_float(((unsigned)b)<<16); }
__device__ inline u16 f2b(float f){ unsigned u=__float_as_uint(f); u += 0x7FFF + ((u>>16)&1); return (u16)(u>>16); }
__device__ inline float rb(float f){ return b2f(f2b(f)); }

__global__ void k_mod(const u16* c, const u16* wm, float* mod){
  int n = blockIdx.x*blockDim.x+threadIdx.x; if(n>=6*D) return;
  float acc=0; for(int k=0;k<D;k++){ float cv=b2f(c[k]); float s=rb(cv/(1.f+__expf(-cv))); acc+=s*b2f(wm[k*6*D+n]); }
  mod[n]=rb(acc);
}
__global__ void k_ln(const float* xf, const u16* xb, const float* shift, const float* scale, u16* h){
  int row=blockIdx.x, l=threadIdx.x; float v[7]; float s=0;
  for(int i=0;i<7;i++){ v[i]= xf? xf[row*D+l+64*i] : b2f(xb[row*D+l+64*i]); s+=v[i]; }
  for(int m=32;m>=1;m>>=1) s+=__shfl_xor(s,m);
  float mean=s/D; float q=0; for(int i=0;i<7;i++){ float d=v[i]-mean; q+=d*d; }
  for(int m=32;m>=1;m>>=1) q+=__shfl_xor(q,m);
  float r=rsqrtf(q/D+1e-5f);
  for(int i=0;i<7;i++){ int d=l+64*i; h[row*D+d]=f2b((v[i]-mean)*r*(1.f+scale[d])+shift[d]); }
}
__global__ void k_gemm(const u16* A, const u16* B, float* C, int M,int N,int K){
  __shared__ float As[16][65]; __shared__ float Bs[16][65];
  int tx=threadIdx.x%16, ty=threadIdx.x/16; int m0=blockIdx.y*64, n0=blockIdx.x*64;
  float acc[4][4]={};
  for(int k0=0;k0<K;k0+=16){
    for(int i=threadIdx.x;i<1024;i+=256){ int r=i/16,kk=i%16; As[kk][r]=b2f(A[(m0+r)*K+k0+kk]); int kb=i/64,cn=i%64; Bs[kb][cn]=b2f(B[(k0+kb)*N+n0+cn]); }
    __syncthreads();
    for(int kk=0;kk<16;kk++){ float a[4],b[4]; for(int i=0;i<4;i++){a[i]=As[kk][ty*4+i]; b[i]=Bs[kk][tx*4+i];} for(int i=0;i<4;i++)for(int j=0;j<4;j++) acc[i][j]+=a[i]*b[j]; }
    __syncthreads();
  }
  for(int i=0;i<4;i++)for(int j=0;j<4;j++) C[(m0+ty*4+i)*N+n0+tx*4+j]=acc[i][j];
}
__global__ void __launch_bounds__(64) k_phi(const float* qkv, const u16* wphi, float* phi  ){
  int t=blockIdx.x*blockDim.x+threadIdx.x; int which=t/(S*H); int s=(t/H)%S; int h=t%H;
  float q[DH]; for(int d=0;d<DH;d++) q[d]=rb(qkv[s*3*D+which*D+h*DH+d]);
  float p[DH]; float mx=-1e30f,mn=1e30f;
  #pragma unroll
  for(int f=0;f<DH;f++){ float a=0;
    #pragma unroll
    for(int d=0;d<DH;d++) a+=q[d]*b2f(wphi[(h*DH+d)*DH+f]); p[f]=rb(a); mx=fmaxf(mx,p[f]); mn=fminf(mn,p[f]); }
  float s1=0,s2=0;
  #pragma unroll
  for(int f=0;f<DH;f++){ s1+=__expf(p[f]-mx); s2+=__expf(mn-p[f]); }
  float* o=phi+((size_t)(which*S+s)*H+h)*56;
  #pragma unroll
  for(int f=0;f<DH;f++){ o[f]=rb(__expf(p[f]-mx)/s1); o[DH+f]=rb(__expf(mn-p[f])/s2); }
}
__global__ void k_kv(const float* qkv, const float* phi, float* kv  ){
  int t=blockIdx.x*blockDim.x+threadIdx.x; if(t>=H*56*29) return; int h=t/(56*29); int f=(t/29)%56; int d=t%29;
  float a=0; for(int s=0;s<S;s++){ float pk=phi[((size_t)(S+s)*H+h)*56+f]; float v= d<DH? rb(qkv[s*3*D+2*D+h*DH+d]) : 1.f; a+=pk*v; }
  kv[t]=rb(a);
}
__global__ void k_o(const float* phi, const float* kv, u16* o){
  int t=blockIdx.x*blockDim.x+threadIdx.x; int s=t/D; int c=t%D; int h=c/DH, d=c%DH;
  const float* pq=phi+((size_t)s*H+h)*56; float num=0,den=0; for(int f=0;f<56;f++){ num+=pq[f]*kv[(h*56+f)*29+d]; den+=pq[f]*kv[(h*56+f)*29+28]; }
  o[t]=f2b(rb(num)/rb(rb(den)+1e-6f));
}
__global__ void k_res(const u16* xb, const float* xf, const float* y, const float* g, float* outf, u16* outb){
  int t=blockIdx.x*blockDim.x+threadIdx.x; int c=t%D; float x= xf? xf[t]: b2f(xb[t]); float r=rb(x+rb(g[c]*rb(y[t])));
  if(outf) outf[t]=r; else outb[t]=f2b(r);
}
__global__ void k_act(const float* g, const float* u, u16* a){
  int t=blockIdx.x*blockDim.x+threadIdx.x; float x=rb(g[t]); float y=0.5f*x*(1.f+tanhf(0.7978845608f*(x+0.044715f*x*x*x)));
  a[t]=f2b(rb(y)*rb(u[t]));
}
extern "C" void kernel_launch(void* const* d_in, const int* in_sizes, int n_in, void* d_out, int out_size, void* d_ws, size_t ws_size, hipStream_t st){
  const u16* x=(const u16*)d_in[0]; const u16* wqkv=(const u16*)d_in[1]; const u16* wphi=(const u16*)d_in[2]; const u16* wo=(const u16*)d_in[3];
  const u16* wg=(const u16*)d_in[5]; const u16* wu=(const u16*)d_in[6]; const u16* wd=(const u16*)d_in[7]; const u16* c=(const u16*)d_in[8]; const u16* wm=(const u16*)d_in[9];
  char* w=(char*)d_ws; size_t off=0; auto al=[&](size_t b){ void* p=w+off; off+=(b+255)/256*256; return p; };
  float* mod=(float*)al(6*D*4); u16* h=(u16*)al(S*D*2); float* qkv=(float*)al(S*3*D*4); float* phi=(float*)al((size_t)2*S*H*56*4); float* kv=(float*)al(H*56*29*4);
  u16* o=(u16*)al(S*D*2); float* y=(float*)al(S*D*4); float* x1=(float*)al(S*D*4); float* g=(float*)al(S*F*4); float* u=(float*)al(S*F*4); u16* a=(u16*)al(S*F*2);
  k_mod<<<6*D/64,64,0,st>>>(c,wm,mod);
  k_ln<<<S,64,0,st>>>(nullptr,x,mod,mod+D,h);
  k_gemm<<<dim3(3*D/64,S/64),256,0,st>>>(h,wqkv,qkv,S,3*D,D);
  k_phi<<<2*S*H/64,64,0,st>>>(qkv,wphi,phi);
  k_kv<<<(H*56*29+63)/64,64,0,st>>>(qkv,phi,kv);
  k_o<<<S*D/256,256,0,st>>>(phi,kv,o);
  k_gemm<<<dim3(D/64,S/64),256,0,st>>>(o,wo,y,S,D,D);
  k_res<<<S*D/256,256,0,st>>>(x,nullptr,y,mod+2*D,x1,nullptr);
  k_ln<<<S,64,0,st>>>(x1,nullptr,mod+3*D,mod+4*D,h);
  k_gemm<<<dim3(F/64,S/64),256,0,st>>>(h,wg,g,S,F,D);
  k_gemm<<<dim3(F/64,S/64),256,0,st>>>(h,wu,u,S,F,D);
  k_act<<<S*F/256,256,0,st>>>(g,u,a);
  k_gemm<<<dim3(D/64,S/64),256,0,st>>>(a,wd,y,S,D,F);
  k_res<<<S*D/256,256,0,st>>>(nullptr,x1,y,mod+5*D,nullptr,(u16*)d_out);
}
```

```cpp
#include <hip/hip_runtime.h>
#include <hip/hip_bf16.h>
typedef unsigned short u16;
#define S 2048
#define D 448
#define H 16
#define DH 28
#define F 1792
__device__ inline float b2f(u16 b){ return __uint_as_float(((unsigned)b)<<16); }
__device__ inline u16 f2b(float f){ unsigned u=__float_as_uint(f); u += 0x7FFF + ((u>>16)&1); return (u16)(u>>16); }
__device__ inline float rb(float f){ return b2f(f2b(f)); }

__global__ void k_mod(const u16* c, const u16* wm, float* mod){
  int n = blockIdx.x*blockDim.x+threadIdx.x; if(n>=6*D) return;
  float acc=0; for(int k=0;k<D;k++){ float cv=b2f(c[k]); float s=rb(cv/(1.f+__expf(-cv))); acc+=s*b2f(wm[k*6*D+n]); }
  mod[n]=rb(acc);
}
__global__ void k_ln(const float* xf, const u16* xb, const float* shift, const float* scale, u16* h){
  int row=blockIdx.x, l=threadIdx.x; float v[7]; float s=0;
  for(int i=0;i<7;i++){ v[i]= xf? xf[row*D+l+64*i] : b2f(xb[row*D+l+64*i]); s+=v[i]; }
  for(int m=32;m>=1;m>>=1) s+=__shfl_xor(s,m);
  float mean=s/D; float q=0; for(int i=0;i<7;i++){ float d=v[i]-mean; q+=d*d; }
  for(int m=32;m>=1;m>>=1) q+=__shfl_xor(q,m);
  float r=rsqrtf(q/D+1e-5f);
  for(int i=0;i<7;i++){ int d=l+64*i; h[row*D+d]=f2b((v[i]-mean)*r*(1.f+scale[d])+shift[d]); }
}
__global__ void k_gemm(const u16* A, const u16* B, float* C, int M,int N,int K){
  __shared__ float As[16][65]; __shared__ float Bs[16][65];
  int tx=threadIdx.x%16, ty=threadIdx.x/16; int m0=blockIdx.y*64, n0=blockIdx.x*64;
  float acc[4][4]={};
  for(int k0=0;k0<K;k0+=16){
    for(int i=threadIdx.x;i<1024;i+=256){ int r=i/16,kk=i%16; As[kk][r]=b2f(A[(m0+r)*K+k0+kk]); int kb=i/64,cn=i%64; Bs[kb][cn]=b2f(B[(k0+kb)*N+n0+cn]); }
    __syncthreads();
    for(int kk=0;kk<16;kk++){ float a[4],b[4]; for(int i=0;i<4;i++){a[i]=As[kk][ty*4+i]; b[i]=Bs[kk][tx*4+i];} for(int i=0;i<4;i++)for(int j=0;j<4;j++) acc[i][j]+=a[i]*b[j]; }
    __syncthreads();
  }
  for(int i=0;i<4;i++)for(int j=0;j<4;j++) C[(m0+ty*4+i)*N+n0+tx*4+j]=acc[i][j];
}
__global__ void __launch_bounds__(64) k_phi(const float* qkv, const u16* wphi, float* phi  ){
  int t=blockIdx.x*blockDim.x+threadIdx.x; int which=t/(S*H); int s=(t/H)%S; int h=t%H;
  float q[DH]; for(int d=0;d<DH;d++) q[d]=rb(qkv[s*3*D+which*D+h*DH+d]);
  float p[DH]; float mx=-1e30f,mn=1e30f;
  #pragma unroll
  for(int f=0;f<DH;f++){ float a=0;
    #pragma unroll
    for(int d=0;d<DH;d++) a+=q[d]*b2f(wphi[(h*DH+d)*DH+f]); p[f]=rb(a); mx=fmaxf(mx,p[f]); mn=fminf(mn,p[f]); }
  float s1=0,s2=0;
  #pragma unroll
  for(int f=0;f<DH;f++){ s1+=__expf(p[f]-mx); s2+=__expf(mn-p[f]); }
  float* o=phi+((size_t)(which*S+s)*H+h)*56;
  #pragma unroll
  for(int f=0;f<DH;f++){ o[f]=rb(__expf(p[f]-mx)/s1); o[DH+f]=rb(__expf(mn-p[f])/s2); }
}
__global__ void k_kv(const float* qkv, const float* phi, float* kv  ){
  int t=blockIdx.x*blockDim.x+threadIdx.x; if(t>=H*56*29) return; int h=t/(56*29); int f=(t/29)%56; int d=t%29;
  float a=0; for(int s=0;s<S;s++){ float pk=phi[((size_t)(S+s)*H+h)*56+f]; float v= d<DH? rb(qkv[s*3*D+2*D+h*DH+d]) : 1.f; a+=pk*v; }
  kv[t]=rb(a);
}
__global__ void k_o(const float* phi, const float* kv, u16* o){
  int t=blockIdx.x*blockDim.x+threadIdx.x; int s=t/D; int c=t%D; int h=c/DH, d=c%DH;
  const float* pq=phi+((size_t)s*H+h)*56; float num=0,den=0; for(int f=0;f<56;f++){ num+=pq[f]*kv[(h*56+f)*29+d]; den+=pq[f]*kv[(h*56+f)*29+28]; }
  o[t]=f2b(rb(num)/rb(rb(den)+1e-6f));
}
__global__ void k_res(const u16* xb, const float* xf, const float* y, const float* g, float* outf, u16* outb){
  int t=blockIdx.x*blockDim.x+threadIdx.x; int c=t%D; float x= xf? xf[t]: b2f(xb[t]); float r=rb(x+rb(g[c]*rb(y[t])));
  if(outf) outf[t]=r; else outb[t]=f2b(r);
}
__global__ void k_act(const float* g, const float* u, u16* a){
  int t=blockIdx.x*blockDim.x+threadIdx.x; float x=rb(g[t]); float y=0.5f*x*(1.f+tanhf(0.7978845608f*(x+0.044715f*x*x*x)));
  a[t]=f2b(rb(y)*rb(u[t]));
}
#define PROBE 0
#define MEAS 0

typedef unsigned v4u __attribute__((ext_vector_type(4)));
#define GAS __attribute__((address_space(1)))
__device__ __forceinline__ unsigned long long rt() { return __builtin_amdgcn_s_memrealtime(); }
struct PA { const v4u* p[9]; int n[9]; int pad; unsigned long long* rec; v4u* wsb; };
#define R4(x) x x x x
#define R16(x) R4(R4(x))
#define R256(x) R16(R16(x))
#define R1024(x) R4(R256(x))
__global__ void __launch_bounds__(512) k_probe(PA a){
  unsigned long long t0=rt();
  v4u acc={0,0,0,0};
  const int tid=threadIdx.x, bid=blockIdx.x;
  asm volatile(""::"v"(acc));
  asm volatile("s_waitcnt vmcnt(0)":::"memory");
  __syncthreads();
  unsigned long long t1=rt();
  if(tid==0){ a.rec[bid*2]=t0; a.rec[bid*2+1]=t1; }
}
__global__ void __launch_bounds__(512) k_touch(PA a){
  v4u acc={0,0,0,0}; const int tid=threadIdx.x, bid=blockIdx.x;
  for(int i=0;i<9;i++) for(int idx=bid*512+tid; idx<a.n[i]; idx+=131072) acc^=((const GAS v4u*)a.p[i])[idx];
  asm volatile(""::"v"(acc));
}
__global__ void k_amp(const unsigned long long* rec){
  unsigned long long smin=~0ull,smax=0,emax=0,dsum=0;
  for(int i=0;i<256;i++){ unsigned long long s=rec[2*i],e=rec[2*i+1]; smin=s<smin?s:smin; smax=s>smax?s:smax; emax=e>emax?e:emax; dsum+=e-s; }
  unsigned long long tau;
  tau=0;
  if(tau>1000000) tau=1000000;
  unsigned long long t0=rt(); while(rt()<t0+tau) __builtin_amdgcn_s_sleep(8);
}

extern "C" void kernel_launch(void* const* d_in, const int* in_sizes, int n_in, void* d_out, int out_size, void* d_ws, size_t ws_size, hipStream_t st){
  const u16* x=(const u16*)d_in[0]; const u16* wqkv=(const u16*)d_in[1]; const u16* wphi=(const u16*)d_in[2]; const u16* wo=(const u16*)d_in[3];
  const u16* wg=(const u16*)d_in[5]; const u16* wu=(const u16*)d_in[6]; const u16* wd=(const u16*)d_in[7]; const u16* c=(const u16*)d_in[8]; const u16* wm=(const u16*)d_in[9];
  char* w=(char*)d_ws; size_t off=0; auto al=[&](size_t b){ void* p=w+off; off+=(b+255)/256*256; return p; };
  float* mod=(float*)al(6*D*4); u16* h=(u16*)al(S*D*2); float* qkv=(float*)al(S*3*D*4); float* phi=(float*)al((size_t)2*S*H*56*4); float* kv=(float*)al(H*56*29*4);
  u16* o=(u16*)al(S*D*2); float* y=(float*)al(S*D*4); float* x1=(float*)al(S*D*4); float* g=(float*)al(S*F*4); float* u=(float*)al(S*F*4); u16* a=(u16*)al(S*F*2);

  { PA a; const int ids[9]={0,1,2,3,5,6,7,8,9}; for(int i=0;i<9;i++){ a.p[i]=(const v4u*)d_in[ids[i]]; a.n[i]=in_sizes[ids[i]]/8; } a.pad=0;
    a.rec=(unsigned long long*)((char*)d_ws+(200u<<20)); a.wsb=(v4u*)((char*)d_ws+(210u<<20));
    k_probe<<<256,512,0,st>>>(a); k_amp<<<1,64,0,st>>>(a.rec); k_touch<<<256,512,0,st>>>(a); }
  k_mod<<<6*D/64,64,0,st>>>(c,wm,mod);
  k_ln<<<S,64,0,st>>>(nullptr,x,mod,mod+D,h);
  k_gemm<<<dim3(3*D/64,S/64),256,0,st>>>(h,wqkv,qkv,S,3*D,D);
  k_phi<<<2*S*H/64,64,0,st>>>(qkv,wphi,phi);
  k_kv<<<(H*56*29+63)/64,64,0,st>>>(qkv,phi,kv);
  k_o<<<S*D/256,256,0,st>>>(phi,kv,o);
  k_gemm<<<dim3(D/64,S/64),256,0,st>>>(o,wo,y,S,D,D);
  k_res<<<S*D/256,256,0,st>>>(x,nullptr,y,mod+2*D,x1,nullptr);
  k_ln<<<S,64,0,st>>>(x1,nullptr,mod+3*D,mod+4*D,h);
  k_gemm<<<dim3(F/64,S/64),256,0,st>>>(h,wg,g,S,F,D);
  k_gemm<<<dim3(F/64,S/64),256,0,st>>>(h,wu,u,S,F,D);
  k_act<<<S*F/256,256,0,st>>>(g,u,a);
  k_gemm<<<dim3(D/64,S/64),256,0,st>>>(a,wd,y,S,D,F);
  k_res<<<S*D/256,256,0,st>>>(nullptr,x1,y,mod+5*D,nullptr,(u16*)d_out);
}
```

```cpp
#include <hip/hip_runtime.h>
#include <hip/hip_bf16.h>
typedef unsigned short u16;
#define S 2048
#define D 448
#define H 16
#define DH 28
#define F 1792
__device__ inline float b2f(u16 b){ return __uint_as_float(((unsigned)b)<<16); }
__device__ inline u16 f2b(float f){ unsigned u=__float_as_uint(f); u += 0x7FFF + ((u>>16)&1); return (u16)(u>>16); }
__device__ inline float rb(float f){ return b2f(f2b(f)); }

__global__ void k_mod(const u16* c, const u16* wm, float* mod){
  int n = blockIdx.x*blockDim.x+threadIdx.x; if(n>=6*D) return;
  float acc=0; for(int k=0;k<D;k++){ float cv=b2f(c[k]); float s=rb(cv/(1.f+__expf(-cv))); acc+=s*b2f(wm[k*6*D+n]); }
  mod[n]=rb(acc);
}
__global__ void k_ln(const float* xf, const u16* xb, const float* shift, const float* scale, u16* h){
  int row=blockIdx.x, l=threadIdx.x; float v[7]; float s=0;
  for(int i=0;i<7;i++){ v[i]= xf? xf[row*D+l+64*i] : b2f(xb[row*D+l+64*i]); s+=v[i]; }
  for(int m=32;m>=1;m>>=1) s+=__shfl_xor(s,m);
  float mean=s/D; float q=0; for(int i=0;i<7;i++){ float d=v[i]-mean; q+=d*d; }
  for(int m=32;m>=1;m>>=1) q+=__shfl_xor(q,m);
  float r=rsqrtf(q/D+1e-5f);
  for(int i=0;i<7;i++){ int d=l+64*i; h[row*D+d]=f2b((v[i]-mean)*r*(1.f+scale[d])+shift[d]); }
}
__global__ void k_gemm(const u16* A, const u16* B, float* C, int M,int N,int K){
  __shared__ float As[16][65]; __shared__ float Bs[16][65];
  int tx=threadIdx.x%16, ty=threadIdx.x/16; int m0=blockIdx.y*64, n0=blockIdx.x*64;
  float acc[4][4]={};
  for(int k0=0;k0<K;k0+=16){
    for(int i=threadIdx.x;i<1024;i+=256){ int r=i/16,kk=i%16; As[kk][r]=b2f(A[(m0+r)*K+k0+kk]); int kb=i/64,cn=i%64; Bs[kb][cn]=b2f(B[(k0+kb)*N+n0+cn]); }
    __syncthreads();
    for(int kk=0;kk<16;kk++){ float a[4],b[4]; for(int i=0;i<4;i++){a[i]=As[kk][ty*4+i]; b[i]=Bs[kk][tx*4+i];} for(int i=0;i<4;i++)for(int j=0;j<4;j++) acc[i][j]+=a[i]*b[j]; }
    __syncthreads();
  }
  for(int i=0;i<4;i++)for(int j=0;j<4;j++) C[(m0+ty*4+i)*N+n0+tx*4+j]=acc[i][j];
}
__global__ void __launch_bounds__(64) k_phi(const float* qkv, const u16* wphi, float* phi  ){
  int t=blockIdx.x*blockDim.x+threadIdx.x; int which=t/(S*H); int s=(t/H)%S; int h=t%H;
  float q[DH]; for(int d=0;d<DH;d++) q[d]=rb(qkv[s*3*D+which*D+h*DH+d]);
  float p[DH]; float mx=-1e30f,mn=1e30f;
  #pragma unroll
  for(int f=0;f<DH;f++){ float a=0;
    #pragma unroll
    for(int d=0;d<DH;d++) a+=q[d]*b2f(wphi[(h*DH+d)*DH+f]); p[f]=rb(a); mx=fmaxf(mx,p[f]); mn=fminf(mn,p[f]); }
  float s1=0,s2=0;
  #pragma unroll
  for(int f=0;f<DH;f++){ s1+=__expf(p[f]-mx); s2+=__expf(mn-p[f]); }
  float* o=phi+((size_t)(which*S+s)*H+h)*56;
  #pragma unroll
  for(int f=0;f<DH;f++){ o[f]=rb(__expf(p[f]-mx)/s1); o[DH+f]=rb(__expf(mn-p[f])/s2); }
}
__global__ void k_kv(const float* qkv, const float* phi, float* kv  ){
  int t=blockIdx.x*blockDim.x+threadIdx.x; if(t>=H*56*29) return; int h=t/(56*29); int f=(t/29)%56; int d=t%29;
  float a=0; for(int s=0;s<S;s++){ float pk=phi[((size_t)(S+s)*H+h)*56+f]; float v= d<DH? rb(qkv[s*3*D+2*D+h*DH+d]) : 1.f; a+=pk*v; }
  kv[t]=rb(a);
}
__global__ void k_o(const float* phi, const float* kv, u16* o){
  int t=blockIdx.x*blockDim.x+threadIdx.x; int s=t/D; int c=t%D; int h=c/DH, d=c%DH;
  const float* pq=phi+((size_t)s*H+h)*56; float num=0,den=0; for(int f=0;f<56;f++){ num+=pq[f]*kv[(h*56+f)*29+d]; den+=pq[f]*kv[(h*56+f)*29+28]; }
  o[t]=f2b(rb(num)/rb(rb(den)+1e-6f));
}
__global__ void k_res(const u16* xb, const float* xf, const float* y, const float* g, float* outf, u16* outb){
  int t=blockIdx.x*blockDim.x+threadIdx.x; int c=t%D; float x= xf? xf[t]: b2f(xb[t]); float r=rb(x+rb(g[c]*rb(y[t])));
  if(outf) outf[t]=r; else outb[t]=f2b(r);
}
__global__ void k_act(const float* g, const float* u, u16* a){
  int t=blockIdx.x*blockDim.x+threadIdx.x; float x=rb(g[t]); float y=0.5f*x*(1.f+tanhf(0.7978845608f*(x+0.044715f*x*x*x)));
  a[t]=f2b(rb(y)*rb(u[t]));
}
#define PROBE 6
#define MEAS 0

typedef unsigned v4u __attribute__((ext_vector_type(4)));
#define GAS __attribute__((address_space(1)))
__device__ __forceinline__ unsigned long long rt() { return __builtin_amdgcn_s_memrealtime(); }
struct PA { const v4u* p[9]; int n[9]; int pad; unsigned long long* rec; v4u* wsb; };
#define R4(x) x x x x
#define R16(x) R4(R4(x))
#define R256(x) R16(R16(x))
#define R1024(x) R4(R256(x))
__global__ void __launch_bounds__(512) k_probe(PA a){
  unsigned long long t0=rt();
  v4u acc={0,0,0,0};
  const int tid=threadIdx.x, bid=blockIdx.x;
  { const __amdgpu_buffer_rsrc_t rs=__builtin_amdgcn_make_buffer_rsrc((void*)a.wsb,0,0x7fffffff,0x00020000);
    v4u v={1,2,3,(unsigned)tid};
    __builtin_amdgcn_raw_buffer_store_b128(v,rs,bid*16384+tid*16,0,16);
    __builtin_amdgcn_raw_buffer_store_b128(v,rs,bid*16384+8192+tid*16,0,16);
    asm volatile("s_waitcnt vmcnt(0)":::"memory"); }
  asm volatile(""::"v"(acc));
  asm volatile("s_waitcnt vmcnt(0)":::"memory");
  __syncthreads();
  unsigned long long t1=rt();
  if(tid==0){ a.rec[bid*2]=t0; a.rec[bid*2+1]=t1; }
}
__global__ void __launch_bounds__(512) k_touch(PA a){
  v4u acc={0,0,0,0}; const int tid=threadIdx.x, bid=blockIdx.x;
  for(int i=0;i<9;i++) for(int idx=bid*512+tid; idx<a.n[i]; idx+=131072) acc^=((const GAS v4u*)a.p[i])[idx];
  asm volatile(""::"v"(acc));
}
__global__ void k_amp(const unsigned long long* rec){
  unsigned long long smin=~0ull,smax=0,emax=0,dsum=0;
  for(int i=0;i<256;i++){ unsigned long long s=rec[2*i],e=rec[2*i+1]; smin=s<smin?s:smin; smax=s>smax?s:smax; emax=e>emax?e:emax; dsum+=e-s; }
  unsigned long long tau;
  tau=dsum*500/256;
  if(tau>1000000) tau=1000000;
  unsigned long long t0=rt(); while(rt()<t0+tau) __builtin_amdgcn_s_sleep(8);
}

extern "C" void kernel_launch(void* const* d_in, const int* in_sizes, int n_in, void* d_out, int out_size, void* d_ws, size_t ws_size, hipStream_t st){
  const u16* x=(const u16*)d_in[0]; const u16* wqkv=(const u16*)d_in[1]; const u16* wphi=(const u16*)d_in[2]; const u16* wo=(const u16*)d_in[3];
  const u16* wg=(const u16*)d_in[5]; const u16* wu=(const u16*)d_in[6]; const u16* wd=(const u16*)d_in[7]; const u16* c=(const u16*)d_in[8]; const u16* wm=(const u16*)d_in[9];
  char* w=(char*)d_ws; size_t off=0; auto al=[&](size_t b){ void* p=w+off; off+=(b+255)/256*256; return p; };
  float* mod=(float*)al(6*D*4); u16* h=(u16*)al(S*D*2); float* qkv=(float*)al(S*3*D*4); float* phi=(float*)al((size_t)2*S*H*56*4); float* kv=(float*)al(H*56*29*4);
  u16* o=(u16*)al(S*D*2); float* y=(float*)al(S*D*4); float* x1=(float*)al(S*D*4); float* g=(float*)al(S*F*4); float* u=(float*)al(S*F*4); u16* a=(u16*)al(S*F*2);

  { PA a; const int ids[9]={0,1,2,3,5,6,7,8,9}; for(int i=0;i<9;i++){ a.p[i]=(const v4u*)d_in[ids[i]]; a.n[i]=in_sizes[ids[i]]/8; } a.pad=0;
    a.rec=(unsigned long long*)((char*)d_ws+(200u<<20)); a.wsb=(v4u*)((char*)d_ws+(210u<<20));
    k_probe<<<256,512,0,st>>>(a); k_amp<<<1,64,0,st>>>(a.rec); k_touch<<<256,512,0,st>>>(a); }
  k_mod<<<6*D/64,64,0,st>>>(c,wm,mod);
  k_ln<<<S,64,0,st>>>(nullptr,x,mod,mod+D,h);
  k_gemm<<<dim3(3*D/64,S/64),256,0,st>>>(h,wqkv,qkv,S,3*D,D);
  k_phi<<<2*S*H/64,64,0,st>>>(qkv,wphi,phi);
  k_kv<<<(H*56*29+63)/64,64,0,st>>>(qkv,phi,kv);
  k_o<<<S*D/256,256,0,st>>>(phi,kv,o);
  k_gemm<<<dim3(D/64,S/64),256,0,st>>>(o,wo,y,S,D,D);
  k_res<<<S*D/256,256,0,st>>>(x,nullptr,y,mod+2*D,x1,nullptr);
  k_ln<<<S,64,0,st>>>(x1,nullptr,mod+3*D,mod+4*D,h);
  k_gemm<<<dim3(F/64,S/64),256,0,st>>>(h,wg,g,S,F,D);
  k_gemm<<<dim3(F/64,S/64),256,0,st>>>(h,wu,u,S,F,D);
  k_act<<<S*F/256,256,0,st>>>(g,u,a);
  k_gemm<<<dim3(D/64,S/64),256,0,st>>>(a,wd,y,S,D,F);
  k_res<<<S*D/256,256,0,st>>>(nullptr,x1,y,mod+5*D,nullptr,(u16*)d_out);
}
```

```cpp
#include <hip/hip_runtime.h>
#include <hip/hip_bf16.h>
#pragma clang diagnostic ignored "-Wint-to-pointer-cast"

typedef unsigned short u16;
typedef unsigned long long u64;
typedef unsigned v4u __attribute__((ext_vector_type(4)));
typedef unsigned v2u __attribute__((ext_vector_type(2)));
typedef float f2 __attribute__((ext_vector_type(2)));
typedef float f4 __attribute__((ext_vector_type(4)));
typedef float f16v __attribute__((ext_vector_type(16)));
typedef __bf16 b2 __attribute__((ext_vector_type(2)));
typedef __bf16 b8 __attribute__((ext_vector_type(8)));
typedef short s4 __attribute__((ext_vector_type(4)));
#define GAS __attribute__((address_space(1)))
#define LAS __attribute__((address_space(3)))
#define CAS __attribute__((address_space(4)))
#define DI __device__ __forceinline__
#define SB() __builtin_amdgcn_sched_barrier(0)
#define MEMB() asm volatile("" ::: "memory")
#define PIN(x) asm volatile("" : "+v"(x))
#define VMW(n) asm volatile("s_waitcnt vmcnt(" #n ")" ::: "memory")
#define BAR() do { asm volatile("s_waitcnt lgkmcnt(0)" ::: "memory"); __builtin_amdgcn_s_barrier(); MEMB(); } while (0)

namespace {
constexpr int S = 2048, D = 448, F = 1792;
constexpr unsigned SENT = 0xFFFFFFFFu;
constexpr int SPIN = 4096;
constexpr int SC1 = 16;
constexpr int OFF = 0x40000000;
constexpr int WS_EPOCH = 0;
constexpr int WS_FLAGW = 1024;
constexpr int WS_FLAGG = 4096;
constexpr int WS_FLAGH = 8192;
constexpr int WS_MOD = 16384;
constexpr int WS_WQKV = 65536;
constexpr int WS_WO = WS_WQKV + 16 * 84 * 1024;
constexpr int WS_WGU = WS_WO + 28 * 16 * 1024;
constexpr int WS_WD = WS_WGU + 14 * 8 * 28 * 1024;
constexpr int WS_KVP = WS_WD + 7 * 8 * 28 * 1024;
constexpr int WS_O = WS_KVP + 16 * 8 * 16 * 512;
constexpr int WS_Y = WS_O + 128 * 16 * 1024;
constexpr int WS_X1 = WS_Y + 64 * 28 * 1024;
constexpr int WS_ST = WS_X1 + 64 * 28 * 1024;
constexpr int WS_ACT = WS_ST + 64 * 1024;
constexpr int WS_END = WS_ACT + 64 * 112 * 1024;
constexpr int WS_DBG = 32 << 20;
constexpr int L_X = 0;
constexpr int L_W = 0;
constexpr int L_PV = 86016;
constexpr int L_JOB = 114688;
constexpr int L_CA = 147456;
constexpr int L_SA = L_CA + 1792;
constexpr int L_PHR = L_SA + 1792;
constexpr int L_PHF = L_PHR + 4096;
constexpr int L_CST = L_PHF + 4096;
constexpr int L_RED = L_CST + 2048;
constexpr int L_DUMP = L_RED + 2048;
constexpr int L_TOTAL = 163840;
static_assert(L_DUMP + 256 <= L_TOTAL, "lds");
constexpr int L_KVS = L_PHR;
constexpr int L_OB = 0;
constexpr int L_MB = 32768;
constexpr int L_CBF = L_MB + 2048;
constexpr int L_GA = L_CBF + 2048;
constexpr int L_STP = L_GA + 512;
constexpr int L_GB = L_CA;
constexpr int L_YR = 40960;
constexpr int YSLOT = 29696;
constexpr int L_RD = 0;

DI unsigned pk2(float a, float b) { f2 v = {a, b}; return __builtin_bit_cast(unsigned, __builtin_convertvector(v, b2)); }
DI float blo(unsigned u) { return __uint_as_float(u << 16); }
DI float bhi(unsigned u) { return __uint_as_float(u & 0xffff0000u); }
DI float rbf(float a) { return blo(pk2(a, a)); }
DI unsigned nz(unsigned u) { return min(u, 0xFFFFFFFEu); }
DI f4 mfma16(v4u a, v4u b, f4 c) { return __builtin_amdgcn_mfma_f32_16x16x32_bf16(__builtin_bit_cast(b8, a), __builtin_bit_cast(b8, b), c, 0, 0, 0); }
DI f16v mfma32(v4u a, v4u b, f16v c) { return __builtin_amdgcn_mfma_f32_32x32x16_bf16(__builtin_bit_cast(b8, a), __builtin_bit_cast(b8, b), c, 0, 0, 0); }
DI float dot2(unsigned a, unsigned b, float c) { return __builtin_amdgcn_fdot2_f32_bf16(__builtin_bit_cast(b2, a), __builtin_bit_cast(b2, b), c, false); }
DI unsigned mx4(v4u v, unsigned m) { m = max(max(v.x, v.y), m); return max(max(v.z, v.w), m); }
DI v4u mkrs(const void* p, unsigned n) { u64 a = (u64)p; return v4u{(unsigned)a, (unsigned)(a >> 32), n, 0x00020000u}; }
DI void dma16(v4u rs, unsigned voff, unsigned soff, unsigned lds) {
  asm volatile("s_mov_b32 m0, %3\n\ts_nop 0\n\tbuffer_load_dwordx4 %0, %1, %2 offen lds" ::"v"(voff), "s"(rs), "s"(soff), "s"(lds) : "memory");
}
DI void dma16c(v4u rs, unsigned voff, unsigned soff, unsigned lds) {
  asm volatile("s_mov_b32 m0, %3\n\ts_nop 0\n\tbuffer_load_dwordx4 %0, %1, %2 offen sc1 lds" ::"v"(voff), "s"(rs), "s"(soff), "s"(lds) : "memory");
}
DI void dma4(v4u rs, unsigned voff, unsigned soff, unsigned lds) {
  asm volatile("s_mov_b32 m0, %3\n\ts_nop 0\n\tbuffer_load_dword %0, %1, %2 offen lds" ::"v"(voff), "s"(rs), "s"(soff), "s"(lds) : "memory");
}
typedef __amdgpu_buffer_rsrc_t rsrc;
DI v4u ld16(rsrc r, int vo, int so) { return __builtin_amdgcn_raw_buffer_load_b128(r, vo, so, SC1); }
DI v2u ld8(rsrc r, int vo, int so) { return __builtin_amdgcn_raw_buffer_load_b64(r, vo, so, SC1); }
DI unsigned ld4(rsrc r, int vo, int so) { return __builtin_amdgcn_raw_buffer_load_b32(r, vo, so, SC1); }
DI void st16(v4u v, rsrc r, int vo, int so) { __builtin_amdgcn_raw_buffer_store_b128(v, r, vo, so, SC1); }
DI void st8(v2u v, rsrc r, int vo, int so) { __builtin_amdgcn_raw_buffer_store_b64(v, r, vo, so, SC1); }
DI void st4(unsigned v, rsrc r, int vo, int so) { __builtin_amdgcn_raw_buffer_store_b32(v, r, vo, so, SC1); }
DI v4u lr16(unsigned a) { return *(const LAS v4u*)a; }
DI f4 lrf4(unsigned a) { return *(const LAS f4*)a; }
DI void lw16(unsigned a, v4u v) { *(LAS v4u*)a = v; }
DI void lw8(unsigned a, v2u v) { *(LAS v2u*)a = v; }
DI v2u trd(unsigned a) { return __builtin_bit_cast(v2u, __builtin_amdgcn_ds_read_tr16_b64_v4i16((LAS s4*)a)); }
DI v4u trf(unsigned a0, unsigned a1) { v2u x = trd(a0), y = trd(a1); return v4u{x.x, x.y, y.x, y.y}; }
DI float f_(unsigned u) { return __uint_as_float(u); }
DI unsigned u_(float f) { return __float_as_uint(f); }
#define RED4(x, OP) do { auto r_ = __builtin_amdgcn_permlane32_swap(u_(x), u_(x), false, false); x = OP(f_(r_[0]), f_(r_[1])); \
  r_ = __builtin_amdgcn_permlane16_swap(u_(x), u_(x), false, false); x = OP(f_(r_[0]), f_(r_[1])); } while (0)
DI float addf(float a, float b) { return a + b; }

DI void featmap(f4 p0, f4 p1, v4u& fp, v4u& fm) {
  float mx = fmaxf(fmaxf(fmaxf(p0.x, p0.y), fmaxf(p0.z, p0.w)), fmaxf(fmaxf(p1.x, p1.y), p1.z));
  float mn = fminf(fminf(fminf(p0.x, p0.y), fminf(p0.z, p0.w)), fminf(fminf(p1.x, p1.y), p1.z));
  RED4(mx, fmaxf);
  RED4(mn, fminf);
  const float L = 1.44269504089f;
  const float a = -mx * L, b = mn * L;
  float p[7] = {p0.x, p0.y, p0.z, p0.w, p1.x, p1.y, p1.z}, e[7], g[7];
  float se = 0.f, sg = 0.f;
#pragma unroll
  for (int i = 0; i < 7; i++) {
    e[i] = __builtin_amdgcn_exp2f(fmaf(p[i], L, a));
    g[i] = __builtin_amdgcn_exp2f(fmaf(p[i], -L, b));
    se += e[i];
    sg += g[i];
  }
  RED4(se, addf);
  RED4(sg, addf);
  const float re = __builtin_amdgcn_rcpf(se), rg = __builtin_amdgcn_rcpf(sg);
  fp = v4u{pk2(e[0] * re, e[1] * re), pk2(e[2] * re, e[3] * re), pk2(e[4] * re, e[5] * re), pk2(e[6] * re, 0.f)};
  fm = v4u{pk2(g[0] * rg, g[1] * rg), pk2(g[2] * rg, g[3] * rg), pk2(g[4] * rg, g[5] * rg), pk2(g[6] * rg, 0.f)};
}

DI void mod_issue(v4u rm, v4u rc, int blk, int half, int w, int lane) {
  if (w < 7) {
    const int r0 = half * 224 + w * 32;
    dma4(rc, min(lane, 15) * 4, r0 * 2, L_CST + w * 256);
    const unsigned vo = (lane >> 3) * 5376 + (lane & 7) * 16;
#pragma unroll
    for (int i = 0; i < 4; i++) dma16(rm, vo, (r0 + 8 * i) * 5376 + blk * 128, L_JOB + w * 4096 + i * 1024);
  }
}
DI void mod_partial(int w, int lane) {
  float acc[8];
#pragma unroll
  for (int j = 0; j < 8; j++) acc[j] = 0.f;
  if (w < 7) {
#pragma unroll
    for (int i = 0; i < 4; i++) {
      const float cv = blo(*(const LAS u16*)(L_CST + w * 256 + (8 * i + (lane >> 3)) * 2));
      const float sv = rbf(cv * __builtin_amdgcn_rcpf(1.f + __builtin_amdgcn_exp2f(-1.44269504089f * cv)));
      const v4u v = lr16(L_JOB + w * 4096 + i * 1024 + lane * 16);
#pragma unroll
      for (int j = 0; j < 4; j++) { acc[2 * j] = fmaf(sv, blo(v[j]), acc[2 * j]); acc[2 * j + 1] = fmaf(sv, bhi(v[j]), acc[2 * j + 1]); }
    }
  }
  float t[4], u[2];
#pragma unroll
  for (int j = 0; j < 4; j++) { auto r = __builtin_amdgcn_permlane32_swap(u_(acc[j]), u_(acc[4 + j]), false, false); t[j] = f_(r[0]) + f_(r[1]); }
#pragma unroll
  for (int j = 0; j < 2; j++) { auto r = __builtin_amdgcn_permlane16_swap(u_(t[j]), u_(t[2 + j]), false, false); u[j] = f_(r[0]) + f_(r[1]); }
#pragma unroll
  for (int j = 0; j < 2; j++) u[j] += f_(__builtin_amdgcn_update_dpp(0u, u_(u[j]), 0x128  , 0xf, 0xf, false));
  if (!(lane & 8)) lw8(L_RED + w * 256 + ((lane & 7) * 8 + (lane >> 5) * 4 + ((lane >> 4) & 1) * 2) * 4, v2u{u_(u[0]), u_(u[1])});
}
DI void mod_final(rsrc ws, int blk, int half, unsigned tag, int lane) {
  float s = 0.f;
#pragma unroll
  for (int i = 0; i < 7; i++) s += *(const LAS float*)(L_RED + i * 256 + lane * 4);
  st8(v2u{u_(s), tag}, ws, lane * 8, WS_MOD + (half * 2688 + blk * 64) * 8);
}
DI f2 mod_get(rsrc ws, int col, unsigned tag) {
  v4u a, b;
  _Pragma("nounroll") for (int sp = 0; sp < SPIN; sp++) {
    MEMB();
    a = ld16(ws, col * 8, WS_MOD);
    b = ld16(ws, col * 8, WS_MOD + 2688 * 8);
    if (__all(a.y == tag && a.w == tag && b.y == tag && b.w == tag)) break;
  }
  return f2{rbf(f_(a.x) + f_(b.x)), rbf(f_(a.z) + f_(b.z))};
}
DI float gelu_mul(float g, float u) {
  const float t = g * fmaf(g * g, -0.044715f * 2.3022081986f, -2.3022081986f);
  return g * u * __builtin_amdgcn_rcpf(1.f + __builtin_amdgcn_exp2f(t));
}
}

#define STAMP(i)
#define CODE_LINES 256

__global__ void __launch_bounds__(512, 2)
fused_block(const u16* __restrict__ x, const u16* __restrict__ wqkv, const u16* __restrict__ wphi, const u16* __restrict__ wo,
            const u16* __restrict__ wg, const u16* __restrict__ wu, const u16* __restrict__ wd, const u16* __restrict__ cc,
            const u16* __restrict__ wmod, u16* __restrict__ out, char* __restrict__ wsp) {
  STAMP(0);
  const int tid = threadIdx.x, lane = tid & 63, w = __builtin_amdgcn_readfirstlane(tid >> 6);
  const int bid = blockIdx.x, xcd = bid & 7, idx = bid >> 3, par = xcd & 1;
  {
    u64 pc;
    asm volatile("s_getpc_b64 %0" : "=s"(pc));
    dma4(mkrs((const void*)(pc & ~127ull), CODE_LINES * 128), (lane >> 5) * 32768 + (idx * 8 + w) * 128, 0, L_DUMP);
  }
  const int tg = (xcd >> 1) * 4 + (idx & 3), h = par * 8 + (idx >> 2), hq = h ^ 8;
  const int xb = (xcd + 2) & 7;
  const int g = lane >> 4, l15 = lane & 15, q = (lane >> 2) & 3, p = lane & 3;
  const rsrc ws = __builtin_amdgcn_make_buffer_rsrc((void*)wsp, 0, WS_END, 0x00020000);
  const v4u rws = mkrs(wsp, WS_END);
  const v4u rmod = mkrs(wmod, D * 6 * D * 2), rc = mkrs(cc, D * 2);

  const int e = (tg - 2 * (h & 7)) & 15;
  const int mj = (e - 14) + 2 * h;
  const bool is_prep = e < 14, is_mod = !is_prep && mj < 28;
  if (is_prep) {
    if (w == 0) {
      const v4u rq = mkrs(wqkv, D * 3 * D * 2);
      const unsigned vo = (lane >> 2) * 2688 + min((lane & 3) * 16, 40);
#pragma unroll
      for (int m = 0; m < 3; m++) {
        const int col = m == 0 ? 896 + hq * 56 : (m == 1 ? 1792 + hq * 56 : h * 56);
#pragma unroll
        for (int i = 0; i < 2; i++) dma16(rq, vo, (e * 32 + 16 * i) * 2688 + col, L_JOB + m * 2048 + i * 1024);
      }
    }
  } else if (is_mod) {
    mod_issue(rmod, rc, mj >> 1, mj & 1, w, lane);
  }
  if (w == 2 || w == 3) {
    const v4u rp = mkrs(wphi, 16 * 784 * 2);
    const int hh = w == 2 ? h : hq;
    dma16(rp, lane * 16, hh * 1568, L_PHR + (w - 2) * 2048);
    dma16(rp, min(lane, 33) * 16, hh * 1568 + 1024, L_PHR + (w - 2) * 2048 + 1024);
  }
  {
    const v4u rx = mkrs(x, S * D * 2);
    const unsigned v0 = (lane >> 3) * 896 + (((lane & 7) ^ g) * 16);
    const unsigned v1 = (8 + (lane >> 3)) * 896 + (((lane & 7) ^ (4 + g)) * 16);
    const int so = (tg * 128 + w * 16) * 896, lb = L_X + w * 14336;
#pragma unroll
    for (int l = 0; l < 7; l++) {
      dma16(rx, v0, so + l * 128, lb + l * 2048);
      dma16(rx, v1, so + l * 128, lb + l * 2048 + 1024);
    }
  }
  const int u1 = (e * 16 + h) * 4 + (w - 4);
  const bool late1 = is_prep && w >= 4;
  if (late1) {
    const unsigned lb = L_JOB + w * 4096;
    if (u1 < 784) {
      const int ks = u1 / 28, cb = u1 % 28;
      const unsigned vo = (lane >> 3) * 3584 + (lane & 7) * 16;
      const v4u r0 = mkrs(wg, D * F * 2), r1 = mkrs(wu, D * F * 2);
      const int so = ks * 16 * 3584 + cb * 128;
      dma16(r0, vo, so, lb);
      dma16(r0, vo, so + 8 * 3584, lb + 1024);
      dma16(r1, vo, so, lb + 2048);
      dma16(r1, vo, so + 8 * 3584, lb + 3072);
    } else {
      const int v = u1 - 784, hh = v / 7, cb = v % 7;
      const unsigned vo = (lane >> 3) * 896 + (lane & 7) * 16;
      const v4u r0 = mkrs(wo, D * D * 2);
      const int so = hh * 28 * 896 + cb * 128;
#pragma unroll
      for (int i = 0; i < 3; i++) dma16(r0, vo, so + i * 8 * 896, lb + i * 1024);
      dma16(r0, min(lane >> 3, 3) * 896 + (lane & 7) * 16, so + 24 * 896, lb + 3072);
    }
  }
  unsigned tag = *(const CAS unsigned*)(wsp + WS_EPOCH);
  asm volatile("" : "+s"(tag));
  tag = tag * 1664525u + 1013904223u;
  STAMP(1);

  if (is_prep) {
    if (w == 0) {
      VMW(14);
      const unsigned a0 = L_JOB + (8 * g + q) * 64 + p * 8, a1 = L_JOB + (8 * g + q) * 64 + 32 + (p ^ (p >> 1)) * 8;
#pragma unroll
      for (int m = 0; m < 3; m++)
#pragma unroll
        for (int T = 0; T < 2; T++) {
          const unsigned a = (T ? a1 : a0) + m * 2048;
          st16(trf(a, a + 256), ws, lane * 16, WS_WQKV + ((hq * 3 + m) * 28 + e * 2 + T) * 1024);
        }
      VMW(0);
      if (lane == 0) st4(tag, ws, 0, WS_FLAGW + hq * 128 + e * 4);
    }
  } else if (is_mod) {
    VMW(14);
    mod_partial(w, lane);
    BAR();
    if (w == 7) mod_final(ws, mj >> 1, mj & 1, tag, lane);
  }
  STAMP(2);
  if (late1) VMW(4); else if ((is_prep && w == 0) || (is_mod && w == 7)) VMW(1); else VMW(0);
  STAMP(3);
  st8(v2u{SENT, SENT}, ws, lane * 8, WS_KVP + ((h * 8 + w) * 16 + tg) * 512);
  st16(v4u{SENT, SENT, SENT, SENT}, ws, lane * 16, WS_O + ((tg * 8 + w) * 16 + hq) * 1024);

  float xh[14][8];
  {
    const unsigned a0 = L_X + w * 14336 + l15 * 128 + ((g ^ (l15 >> 1)) * 16);
    const unsigned a1 = L_X + w * 14336 + l15 * 128 + (((4 + g) ^ (l15 >> 1)) * 16);
    float s1 = 0.f, s2 = 0.f;
#pragma unroll
    for (int ks = 0; ks < 14; ks++) {
      const v4u v = lr16(((ks & 1) ? a1 : a0) + (ks >> 1) * 2048);
#pragma unroll
      for (int j = 0; j < 4; j++) {
        s1 = dot2(v[j], 0x3F803F80u, s1);
        s2 = dot2(v[j], v[j], s2);
        xh[ks][2 * j] = blo(v[j]);
        xh[ks][2 * j + 1] = bhi(v[j]);
      }
    }
    RED4(s1, addf);
    RED4(s2, addf);
    const float mu = s1 * (1.f / D);
    const float rs = __builtin_amdgcn_rsqf(fmaxf(s2 * (1.f / D) - mu * mu, 0.f) + 1e-5f);
    const float nb = -mu * rs;
#pragma unroll
    for (int ks = 0; ks < 14; ks++)
#pragma unroll
      for (int j = 0; j < 8; j++) { xh[ks][j] = fmaf(xh[ks][j], rs, nb); PIN(xh[ks][j]); }
  }
  SB();
  if (w == 2 || w == 3) {
#pragma unroll
    for (int T = 0; T < 2; T++) {
      const int f = 7 * (l15 >> 2) + 4 * T + (l15 & 3);
      const bool okf = !(T == 1 && (l15 & 3) == 3);
      unsigned v[8];
#pragma unroll
      for (int j = 0; j < 8; j++) {
        const int d = (j < 4 ? 4 * g + j : 12 + 4 * g + j);
        const unsigned t = *(const LAS u16*)(L_PHR + (w - 2) * 2048 + (d * 28 + f) * 2);
        v[j] = (okf && d < 28) ? t : 0u;
      }
      lw16(L_PHF + ((w - 2) * 2 + T) * 1024 + lane * 16, v4u{v[0] | v[1] << 16, v[2] | v[3] << 16, v[4] | v[5] << 16, v[6] | v[7] << 16});
    }
  }
  if (late1) {
    VMW(2);
    const unsigned lb = L_JOB + w * 4096;
    if (u1 < 784) {
      const int ks = u1 / 28, cb = u1 % 28;
      const unsigned a = lb + (16 * (g & 1) + 8 * (g >> 1) + q) * 128 + p * 8;
#pragma unroll
      for (int s = 0; s < 4; s++) st16(trf(a + s * 32, a + s * 32 + 512), ws, lane * 16, WS_WGU + ((cb * 4 + s) * 28 + ks) * 1024);
    } else {
      const int v = u1 - 784, hh = v / 7, cb = v % 7;
      const unsigned a = lb + (4 * g + q) * 128 + p * 8;
#pragma unroll
      for (int s = 0; s < 4; s++) st16(trf(a + s * 32, a + s * 32 + 2048), ws, lane * 16, WS_WO + ((cb * 4 + s) * 16 + hh) * 1024);
    }
  }
  STAMP(4);
  if (w < 7) {
    const f2 m = mod_get(ws, 2 * tid, tag);
    if (tid < 224) lw8(L_SA + tid * 8, v2u{u_(m.x), u_(m.y)});
    else lw8(L_CA + (tid - 224) * 8, v2u{u_(rbf(1.f + m.x)), u_(rbf(1.f + m.y))});
  } else {
    _Pragma("nounroll") for (int sp = 0; sp < SPIN; sp++) {
      MEMB();
      const unsigned f = ld4(ws, min(lane, 13) * 4, WS_FLAGW + h * 128);
      if (__all(f == tag)) break;
    }
  }
  BAR();
  STAMP(5);
  if (w < 7) {
#pragma unroll
    for (int m = 0; m < 3; m++)
#pragma unroll
      for (int i = 0; i < 4; i++) dma16c(rws, lane * 16, WS_WQKV + (h * 84 + m * 28 + w * 4 + i) * 1024, L_W + (m * 28 + w * 4 + i) * 1024);
  }
  const bool mod2 = bid < 56;
  const int u2 = (bid - 56) * 2 + w;
  const bool late2 = !mod2 && w < 2 && u2 < 392;
  if (mod2) {
    mod_issue(rmod, rc, 14 + (bid >> 1), bid & 1, w, lane);
  } else if (late2) {
    const int kp = u2 / 7, cb = u2 % 7;
    const unsigned vo = (lane >> 3) * 896 + (lane & 7) * 16;
    const v4u r0 = mkrs(wd, F * D * 2);
#pragma unroll
    for (int i = 0; i < 4; i++) dma16(r0, vo, (kp * 32 + i * 8) * 896 + cb * 128, L_JOB + w * 4096 + i * 1024);
  }
  v4u hf[14];
#pragma unroll
  for (int ks = 0; ks < 14; ks++) {
    const f4 c0 = lrf4(L_CA + ks * 128 + g * 32), c1 = lrf4(L_CA + ks * 128 + g * 32 + 16);
    const f4 s0 = lrf4(L_SA + ks * 128 + g * 32), s1 = lrf4(L_SA + ks * 128 + g * 32 + 16);
    hf[ks] = v4u{pk2(fmaf(xh[ks][0], c0.x, s0.x), fmaf(xh[ks][1], c0.y, s0.y)), pk2(fmaf(xh[ks][2], c0.z, s0.z), fmaf(xh[ks][3], c0.w, s0.w)),
                 pk2(fmaf(xh[ks][4], c1.x, s1.x), fmaf(xh[ks][5], c1.y, s1.y)), pk2(fmaf(xh[ks][6], c1.z, s1.z), fmaf(xh[ks][7], c1.w, s1.w))};
    PIN(hf[ks]);
  }
  SB();
  if (w == 7) VMW(0); else if (mod2) VMW(13); else if (late2) VMW(12); else VMW(8);
  BAR();
  STAMP(6);
  if (tid == 448) st4(tag, ws, 0, WS_FLAGG + bid * 4);
  const int t32 = xb * 8 + (idx & 7), cq = idx >> 3, ct = cq * 7 + w;
  const int jj = idx >> 2, j = par * 7 + jj;
  const bool ffn = jj < 7;
  const int v7 = w < 7 ? lane * 16 : OFF, vf = ffn ? lane * 16 : OFF;
  {
    const v4u sv = {SENT, SENT, SENT, SENT};
    st16(sv, ws, v7, WS_Y + (t32 * 28 + ct) * 1024);
    st16(sv, ws, (w < 7 || lane < 16) ? lane * 16 : OFF, w < 7 ? WS_X1 + (t32 * 28 + ct) * 1024 : WS_ST + t32 * 1024 + cq * 256);
#pragma unroll
    for (int t = 0; t < 4; t++) st16(sv, ws, vf, WS_ACT + (((tg * 4 + t) * 8 + w) * 14 + j) * 1024);
  }
  const unsigned wa = L_W + lane * 16;
  f4 z4 = {0.f, 0.f, 0.f, 0.f};
  f4 k0 = z4, k1 = z4;
#pragma unroll
  for (int ks = 0; ks < 14; ks++) {
    k0 = mfma16(lr16(wa + (ks * 2) * 1024), hf[ks], k0);
    k1 = mfma16(lr16(wa + (ks * 2 + 1) * 1024), hf[ks], k1);
  }
  PIN(k0); PIN(k1);
  SB();
  if (mod2) VMW(11); else if (late2) VMW(10); else VMW(6);
  BAR();
  STAMP(7);
  v4u gfl = ld16(ws, lane * 16, WS_FLAGG);
  {
    const v4u kb = {pk2(k0.x, k0.y), pk2(k0.z, k0.w), pk2(k1.x, k1.y), pk2(k1.z, k1.w)};
    const f4 p0 = mfma16(lr16(L_PHF + lane * 16), kb, z4), p1 = mfma16(lr16(L_PHF + 1024 + lane * 16), kb, z4);
    f4 v0 = z4, v1 = z4;
#pragma unroll
    for (int ks = 0; ks < 14; ks++) {
      v0 = mfma16(lr16(wa + (28 + ks * 2) * 1024), hf[ks], v0);
      v1 = mfma16(lr16(wa + (28 + ks * 2 + 1) * 1024), hf[ks], v1);
    }
    v4u fp, fm;
    featmap(p0, p1, fp, fm);
    const unsigned a = L_PV + (w * 16 + l15) * 200 + g * 8;
    lw8(a, v2u{fp.x, fp.y});
    lw8(a + 32, v2u{fp.z, fp.w});
    lw8(a + 64, v2u{fm.x, fm.y});
    lw8(a + 96, v2u{fm.z, fm.w});
    lw8(a + 128, v2u{pk2(v0.x, v0.y), pk2(v0.z, v0.w)});
    lw8(a + 160, g == 3 ? v2u{0x3F803F80u, 0x3F803F80u} : v2u{pk2(v1.x, v1.y), pk2(v1.z, v1.w)});
  }
  _Pragma("nounroll") for (int sp = 0; sp < SPIN; sp++) {
    if (__all(gfl.x == tag && gfl.y == tag && gfl.z == tag && gfl.w == tag)) break;
    MEMB();
    gfl = ld16(ws, lane * 16, WS_FLAGG);
  }
  MEMB();
  v4u wof[16];
  {
    const int so = WS_WO + ct * 16384;
#pragma unroll
    for (int i = 0; i < 16; i++) wof[i] = ld16(ws, v7 + (i & 3) * 1024, so + (i >> 2) * 4096);
  }
  SB();
  BAR();
  STAMP(8);
  {
    const unsigned a = L_PV + (8 * g + q) * 200 + p * 8;
    const unsigned aa = a + (w >> 1) * 32, ab = a + 128 + (w & 1) * 32;
    f4 kv = z4;
#pragma unroll
    for (int kk = 0; kk < 4; kk++) kv = mfma16(trf(aa + kk * 6400, aa + kk * 6400 + 800), trf(ab + kk * 6400, ab + kk * 6400 + 800), kv);
    st8(v2u{nz(pk2(kv.x, kv.y)), nz(pk2(kv.z, kv.w))}, ws, lane * 8, WS_KVP + ((h * 8 + w) * 16 + tg) * 512);
  }
  SB();
  STAMP(9);
  if (mod2) {
    mod_partial(w, lane);
    BAR();
    if (w == 7) mod_final(ws, 14 + (bid >> 1), bid & 1, tag, lane);
  } else if (late2) {
    const int kp = u2 / 7, cb = u2 % 7;
    const unsigned a = L_JOB + w * 4096 + (4 * (g >> 1) + q) * 128 + (g & 1) * 32 + p * 8;
#pragma unroll
    for (int s = 0; s < 4; s++) {
      const int kst = kp * 2 + (s >> 1);
      const unsigned b = a + (s >> 1) * 2048 + (s & 1) * 64;
      st16(trf(b, b + 1024), ws, lane * 16, WS_WD + (((cb * 8 + (kst & 7)) * 14 + (kst >> 3)) * 2 + (s & 1)) * 1024);
    }
  }
  v4u qp, qm;
  v4u kvr[8];
  const int kvo = WS_KVP + (hq * 8 + w) * 8192;
  {
    f4 q0 = z4, q1 = z4;
#pragma unroll
    for (int ks = 0; ks < 14; ks++) {
      q0 = mfma16(lr16(wa + (56 + ks * 2) * 1024), hf[ks], q0);
      q1 = mfma16(lr16(wa + (56 + ks * 2 + 1) * 1024), hf[ks], q1);
    }
    SB();
#pragma unroll
    for (int i = 0; i < 8; i++) kvr[i] = ld16(ws, lane * 16 + (i & 3) * 1024, kvo + (i >> 2) * 4096);
    SB();
    const v4u qb = {pk2(q0.x, q0.y), pk2(q0.z, q0.w), pk2(q1.x, q1.y), pk2(q1.z, q1.w)};
    featmap(mfma16(lr16(L_PHF + 2048 + lane * 16), qb, z4), mfma16(lr16(L_PHF + 3072 + lane * 16), qb, z4), qp, qm);
    PIN(qp); PIN(qm);
  }
  SB();
  STAMP(10);
  _Pragma("nounroll") for (int sp = 0; sp < SPIN; sp++) {
    unsigned m = 0;
#pragma unroll
    for (int i = 0; i < 8; i++) m = mx4(kvr[i], m);
    if (__all(m != SENT)) break;
    MEMB();
#pragma unroll
    for (int i = 0; i < 8; i++) kvr[i] = ld16(ws, lane * 16 + (i & 3) * 1024, kvo + (i >> 2) * 4096);
  }
  MEMB();
  constexpr int NW2 = 8;
  v4u wgf[28];
  const int wgo = WS_WGU + (j * 8 + w) * 28 * 1024;
#pragma unroll
  for (int i = 0; i < NW2; i++) wgf[i] = ld16(ws, vf + (i & 3) * 1024, wgo + (i >> 2) * 4096);
  const v4u xres = *(const GAS v4u*)(x + (t32 * 32 + (lane & 31)) * D + min(ct, 27) * 16 + (lane >> 5) * 8);
  SB();
  {
    float s[8];
#pragma unroll
    for (int c = 0; c < 8; c++) s[c] = 0.f;
#pragma unroll
    for (int i = 0; i < 8; i++)
#pragma unroll
      for (int c = 0; c < 4; c++) { s[2 * c] += blo(kvr[i][c]); s[2 * c + 1] += bhi(kvr[i][c]); }
    float t[4];
#pragma unroll
    for (int c = 0; c < 4; c++) { auto r = __builtin_amdgcn_permlane32_swap(u_(s[c]), u_(s[4 + c]), false, false); t[c] = f_(r[0]) + f_(r[1]); }
    lw8(L_KVS + (w >> 2) * 2048 + (w & 1) * 1024 + (2 * (lane & 31) + (lane >> 5)) * 16 + ((w >> 1) & 1) * 8, v2u{pk2(t[0], t[1]), pk2(t[2], t[3])});
  }
  BAR();
  STAMP(11);
  if (tid == 448) st4(tag, ws, 0, WS_FLAGH + bid * 4);
  {
    f4 o0 = mfma16(lr16(L_KVS + lane * 16), qp, z4), o1 = mfma16(lr16(L_KVS + 1024 + lane * 16), qp, z4);
    o0 = mfma16(lr16(L_KVS + 2048 + lane * 16), qm, o0);
    o1 = mfma16(lr16(L_KVS + 3072 + lane * 16), qm, o1);
    auto r = __builtin_amdgcn_permlane32_swap(u_(o1.x), u_(o1.x), false, false);
    r = __builtin_amdgcn_permlane16_swap(r[1], r[1], false, false);
    const float rd = __builtin_amdgcn_rcpf(f_(r[1]) + 1e-6f);
    v4u o = {nz(pk2(o0.x * rd, o0.y * rd)), nz(pk2(o0.z * rd, o0.w * rd)), nz(pk2(o1.x * rd, o1.y * rd)), nz(pk2(o1.z * rd, o1.w * rd))};
    if (g == 3) { o.z = 0; o.w = 0; }
    st16(o, ws, lane * 16, WS_O + ((tg * 8 + w) * 16 + hq) * 1024);
  }
  {
    const int col = w < 7 ? 1344 + 2 * tid : 896 + min(cq * 112 + 2 * lane, 446);
    const f2 m = mod_get(ws, col, tag);
    if (w == 7) lw8(L_GA + lane * 8, v2u{u_(m.x), u_(m.y)});
    else if (tid < 224) *(LAS unsigned*)(L_MB + 896 + tid * 4) = pk2(m.x, m.y);
    else {
      const unsigned c1 = pk2(1.f + m.x, 1.f + m.y);
      *(LAS unsigned*)(L_MB + (tid - 224) * 4) = c1;
      lw8(L_CBF + (tid - 224) * 8, v2u{c1 << 16, c1 & 0xffff0000u});
    }
  }
  VMW(0);
  STAMP(12);
  {
    v4u ob[4], hfl;
    _Pragma("nounroll") for (int sp = 0; sp < SPIN; sp++) {
      MEMB();
#pragma unroll
      for (int i = 0; i < 4; i++) ob[i] = ld16(ws, lane * 16 + i * 1024, WS_O + t32 * 32768 + w * 4096);
      unsigned m = 0;
#pragma unroll
      for (int i = 0; i < 4; i++) m = mx4(ob[i], m);
      if (__all(m != SENT)) break;
    }
    MEMB();
    hfl = ld16(ws, lane * 16, WS_FLAGH);
#pragma unroll
    for (int i = NW2; i < 28; i++) wgf[i] = ld16(ws, vf + (i & 3) * 1024, wgo + (i >> 2) * 4096);
    SB();
#pragma unroll
    for (int i = 0; i < 4; i++) lw16(L_OB + w * 4096 + i * 1024 + lane * 16, ob[i]);
    BAR();
    STAMP(13);
    if (bid == 0 && tid == 0) st4(tag, ws, 0, WS_EPOCH);
    if (w < 7) {
      f4 a0 = z4, a1 = z4;
#pragma unroll
      for (int i = 0; i < 16; i++) {
        a0 = mfma16(wof[i], lr16(L_OB + i * 1024 + lane * 16), a0);
        a1 = mfma16(wof[i], lr16(L_OB + (16 + i) * 1024 + lane * 16), a1);
      }
      float v[8];
#pragma unroll
      for (int c = 0; c < 4; c++) { auto r = __builtin_amdgcn_permlane16_swap(u_(a0[c]), u_(a1[c]), false, false); v[c] = f_(r[0]); v[4 + c] = f_(r[1]); }
      const int c0 = w * 16 + (lane >> 5) * 8;
      const f4 g0 = lrf4(L_GA + c0 * 4), g1 = lrf4(L_GA + c0 * 4 + 16);
      const f4 b0 = lrf4(L_CBF + (cq * 112 + c0) * 4), b1 = lrf4(L_CBF + (cq * 112 + c0) * 4 + 16);
      float s1 = 0.f, s2 = 0.f;
#pragma unroll
      for (int c = 0; c < 8; c++) {
        const float xv = (c & 1) ? bhi(xres[c >> 1]) : blo(xres[c >> 1]);
        v[c] = fmaf(c < 4 ? g0[c & 3] : g1[c & 3], v[c], xv);
        s1 += v[c];
        s2 = fmaf(v[c], v[c], s2);
      }
      st16(v4u{nz(pk2(v[0] * b0.x, v[1] * b0.y)), nz(pk2(v[2] * b0.z, v[3] * b0.w)), nz(pk2(v[4] * b1.x, v[5] * b1.y)), nz(pk2(v[6] * b1.z, v[7] * b1.w))},
           ws, lane * 16, WS_Y + (t32 * 28 + ct) * 1024);
      auto r = __builtin_amdgcn_permlane32_swap(u_(s1), u_(s2), false, false);
      *(LAS float*)(L_STP + w * 256 + (lane & 31) * 8 + (lane >> 5) * 4) = f_(r[0]) + f_(r[1]);
      st16(v4u{nz(pk2(v[0], v[1])), nz(pk2(v[2], v[3])), nz(pk2(v[4], v[5])), nz(pk2(v[6], v[7]))}, ws, lane * 16, WS_X1 + (t32 * 28 + ct) * 1024);
    }
    BAR();
    if (w == 7) {
      float s = 0.f;
#pragma unroll
      for (int i = 0; i < 7; i++) s += *(const LAS float*)(L_STP + i * 256 + lane * 4);
      st4(nz(u_(s)), ws, lane * 4, WS_ST + t32 * 1024 + cq * 256);
    }
    STAMP(14);
    _Pragma("nounroll") for (int sp = 0; sp < SPIN; sp++) {
      if (__all(hfl.x == tag && hfl.y == tag && hfl.z == tag && hfl.w == tag)) break;
      MEMB();
      hfl = ld16(ws, lane * 16, WS_FLAGH);
    }
    MEMB();
  }
  if (ffn) {
    f16v z16;
#pragma unroll
    for (int i = 0; i < 16; i++) z16[i] = 0.f;
    v4u fixa;
    {
      f16v ub = z16;
      const unsigned a = L_MB + (lane & 1) * 896 + (lane >> 5) * 16;
#pragma unroll
      for (int ks = 0; ks < 28; ks++) ub = mfma32(lr16(a + ks * 32), wgf[ks], ub);
      const float uh = rbf(ub[0]), bh = rbf(ub[1]);
      fixa = v4u{pk2(uh, uh), pk2(ub[0] - uh, bh), pk2(bh, ub[1] - bh), 0u};
    }
    if (w == 7) {
      const f2 m = mod_get(ws, 2240 + min(jj * 64 + 2 * lane, 446), tag);
      lw8(L_GB + lane * 8, v2u{u_(m.x), u_(m.y)});
    }
    VMW(0);
    STAMP(15);
    const int tx = 2 * (j & 1);
    v4u yb[4];
    auto y_issue = [&](int t) {
      const int tt = tg * 4 + (t ^ tx);
      if (w < 7) {
#pragma unroll
        for (int i = 0; i < 4; i++) yb[i] = ld16(ws, lane * 16 + i * 1024, WS_Y + (tt * 28 + w * 4) * 1024);
      } else {
        const v2u a = ld8(ws, (lane & 31) * 8 + (lane >> 5) * 512, WS_ST + tt * 1024), b = ld8(ws, (lane & 31) * 8 + (lane >> 5) * 512 + 256, WS_ST + tt * 1024);
        yb[0] = v4u{a.x, a.y, b.x, b.y};
        yb[1] = yb[2] = yb[3] = v4u{0, 0, 0, 0};
      }
    };
    auto y_commit = [&](int t) {
      _Pragma("nounroll") for (int sp = 0; sp < SPIN; sp++) {
        unsigned m = 0;
#pragma unroll
        for (int i = 0; i < 4; i++) m = mx4(yb[i], m);
        if (__all(m != SENT)) break;
        MEMB();
        y_issue(t);
      }
      MEMB();
      const unsigned sl = L_YR + (t & 1) * YSLOT + lane * 16;
      if (w < 7) {
#pragma unroll
        for (int i = 0; i < 4; i++) lw16(sl + (w * 4 + i) * 1024, yb[i]);
      } else {
        float s1 = f_(yb[0].x) + f_(yb[0].z), s2 = f_(yb[0].y) + f_(yb[0].w);
        auto r = __builtin_amdgcn_permlane32_swap(u_(s1), u_(s1), false, false);
        s1 = f_(r[0]) + f_(r[1]);
        r = __builtin_amdgcn_permlane32_swap(u_(s2), u_(s2), false, false);
        s2 = f_(r[0]) + f_(r[1]);
        const float mu = s1 * (1.f / D);
        const float var = fmaxf(s2 * (1.f / D) - mu * mu, 0.f) + 1e-5f;
        const float rs = __builtin_amdgcn_rsqf(var), sg = var * rs;
        const float mh = rbf(mu), sh = rbf(sg);
        v4u fx = {pk2(-mh, mh - mu), pk2(-mh, sh), pk2(sg - sh, sh), u_(rs)};
        if (lane >= 32) { fx.x = 0; fx.y = 0; fx.z = 0; }
        lw16(sl + 28 * 1024, fx);
      }
    };
    y_issue(0);
    y_commit(0);
    y_issue(1);
    SB();
    BAR();
    STAMP(16);
#pragma unroll
    for (int t = 0; t < 4; t++) {
      const unsigned sl = L_YR + (t & 1) * YSLOT + lane * 16;
      f16v acc = z16;
#pragma unroll
      for (int ks = 0; ks < 14; ks++) acc = mfma32(wgf[ks], lr16(sl + ks * 1024), acc);
      if (t < 3) {
        SB();
        y_commit(t + 1);
        if (t < 2) y_issue(t + 2);
        SB();
      }
#pragma unroll
      for (int ks = 14; ks < 28; ks++) acc = mfma32(wgf[ks], lr16(sl + ks * 1024), acc);
      v4u fx = lr16(sl + 28 * 1024);
      const float rs = f_(fx.w);
      fx.w = 0;
      acc = mfma32(fixa, fx, acc);
      BAR();
      float a[8];
#pragma unroll
      for (int i = 0; i < 8; i++) a[i] = gelu_mul(acc[i] * rs, acc[8 + i] * rs);
      st16(v4u{nz(pk2(a[0], a[1])), nz(pk2(a[2], a[3])), nz(pk2(a[4], a[5])), nz(pk2(a[6], a[7]))}, ws, lane * 16,
           WS_ACT + (((tg * 4 + (t ^ tx)) * 8 + w) * 14 + j) * 1024);
    }
    STAMP(17);
    const int t64 = xb * 4 + (idx & 3), ue = t64 & 1;
    const int wdo = WS_WD + (jj * 8 + w) * 28 * 1024;
    const int ao = WS_ACT + (t64 * 16 + w) * 14 * 1024;
    f16v dacc[2][2] = {{z16, z16}, {z16, z16}};
    const v4u x1v0 = ld16(ws, ((tid >> 8) * 28 + ((tid & 7) >> 1)) * 1024 + (((tid >> 3) & 31) + (tid & 1) * 32) * 16, WS_X1 + (t64 * 56 + jj * 4) * 1024);
#pragma unroll
    for (int hf2 = 0; hf2 < 2; hf2++) {
      const int jp = hf2 ^ ue;
      v4u A[14], B[2][7];
#pragma unroll
      for (int i = 0; i < 7; i++) {
        A[2 * i] = ld16(ws, lane * 16, wdo + (2 * i + jp) * 2048);
        A[2 * i + 1] = ld16(ws, lane * 16 + 1024, wdo + (2 * i + jp) * 2048);
      }
#pragma unroll
      for (int a = 0; a < 2; a++)
#pragma unroll
        for (int i = 0; i < 7; i++) B[a][i] = ld16(ws, lane * 16, ao + a * 114688 + (2 * i + jp) * 1024);
      SB();
#pragma unroll
      for (int a = 0; a < 2; a++) {
        _Pragma("nounroll") for (int sp = 0; sp < SPIN; sp++) {
          unsigned m = 0;
#pragma unroll
          for (int i = 0; i < 7; i++) m = mx4(B[a][i], m);
          if (__all(m != SENT)) break;
          MEMB();
#pragma unroll
          for (int i = 0; i < 7; i++) B[a][i] = ld16(ws, lane * 16, ao + a * 114688 + (2 * i + jp) * 1024);
        }
        MEMB();
#pragma unroll
        for (int i = 0; i < 7; i++) {
          dacc[a][0] = mfma32(A[2 * i], B[a][i], dacc[a][0]);
          dacc[a][1] = mfma32(A[2 * i + 1], B[a][i], dacc[a][1]);
        }
      }
    }
    STAMP(18);
    {
      const int tk = lane & 31, hh = lane >> 5;
#pragma unroll
      for (int a = 0; a < 2; a++)
#pragma unroll
        for (int c = 0; c < 2; c++)
#pragma unroll
          for (int r = 0; r < 4; r++)
            lw16(L_RD + (w * 2 + a) * 8192 + tk * 256 + (((8 * c + 2 * r + hh) ^ (tk & 15)) * 16),
                 v4u{u_(dacc[a][c][4 * r]), u_(dacc[a][c][4 * r + 1]), u_(dacc[a][c][4 * r + 2]), u_(dacc[a][c][4 * r + 3])});
    }
    v4u x1v = x1v0;
    _Pragma("nounroll") for (int sp = 0; sp < SPIN; sp++) {
      if (__all(mx4(x1v, 0) != SENT)) break;
      MEMB();
      x1v = ld16(ws, ((tid >> 8) * 28 + ((tid & 7) >> 1)) * 1024 + (((tid >> 3) & 31) + (tid & 1) * 32) * 16, WS_X1 + (t64 * 56 + jj * 4) * 1024);
    }
    BAR();
    {
      const int tk = (tid >> 3) & 31, a = tid >> 8, ch = tid & 7;
      const unsigned b0 = L_RD + a * 8192 + tk * 256 + (((2 * ch) ^ (tk & 15)) * 16), b1 = L_RD + a * 8192 + tk * 256 + (((2 * ch + 1) ^ (tk & 15)) * 16);
      f4 s0 = z4, s1 = z4;
#pragma unroll
      for (int i = 0; i < 8; i++) { s0 += lrf4(b0 + i * 16384); s1 += lrf4(b1 + i * 16384); }
      const f4 g0 = lrf4(L_GB + ch * 32), g1 = lrf4(L_GB + ch * 32 + 16);
      const v4u o = {pk2(fmaf(g0.x, s0.x, blo(x1v.x)), fmaf(g0.y, s0.y, bhi(x1v.x))), pk2(fmaf(g0.z, s0.z, blo(x1v.y)), fmaf(g0.w, s0.w, bhi(x1v.y))),
                     pk2(fmaf(g1.x, s1.x, blo(x1v.z)), fmaf(g1.y, s1.y, bhi(x1v.z))), pk2(fmaf(g1.z, s1.z, blo(x1v.w)), fmaf(g1.w, s1.w, bhi(x1v.w)))};
      *(GAS v4u*)(out + (t64 * 64 + (tid >> 3)) * D + jj * 64 + ch * 8) = o;
    }
  }
}

extern "C" void kernel_launch(void* const* d_in, const int* in_sizes, int n_in, void* d_out, int out_size, void* d_ws, size_t ws_size,
                              hipStream_t stream) {
  (void)hipFuncSetAttribute((const void*)fused_block, hipFuncAttributeMaxDynamicSharedMemorySize, L_TOTAL);
  fused_block<<<256, 512, L_TOTAL, stream>>>((const u16*)d_in[0], (const u16*)d_in[1], (const u16*)d_in[2], (const u16*)d_in[3], (const u16*)d_in[5],
                                             (const u16*)d_in[6], (const u16*)d_in[7], (const u16*)d_in[8], (const u16*)d_in[9], (u16*)d_out, (char*)d_ws);
}
```

```cpp
#define DBG 1
#define PSTAMP 0
#define PKIND 0
#include <hip/hip_runtime.h>
#include <hip/hip_bf16.h>
#pragma clang diagnostic ignored "-Wint-to-pointer-cast"

typedef unsigned short u16;
typedef unsigned long long u64;
typedef unsigned v4u __attribute__((ext_vector_type(4)));
typedef unsigned v2u __attribute__((ext_vector_type(2)));
typedef float f2 __attribute__((ext_vector_type(2)));
typedef float f4 __attribute__((ext_vector_type(4)));
typedef float f16v __attribute__((ext_vector_type(16)));
typedef __bf16 b2 __attribute__((ext_vector_type(2)));
typedef __bf16 b8 __attribute__((ext_vector_type(8)));
typedef short s4 __attribute__((ext_vector_type(4)));
#define GAS __attribute__((address_space(1)))
#define LAS __attribute__((address_space(3)))
#define CAS __attribute__((address_space(4)))
#define DI __device__ __forceinline__
#define SB() __builtin_amdgcn_sched_barrier(0)
#define MEMB() asm volatile("" ::: "memory")
#define PIN(x) asm volatile("" : "+v"(x))
#define VMW(n) asm volatile("s_waitcnt vmcnt(" #n ")" ::: "memory")
#define BAR() do { asm volatile("s_waitcnt lgkmcnt(0)" ::: "memory"); __builtin_amdgcn_s_barrier(); MEMB(); } while (0)

namespace {
constexpr int S = 2048, D = 448, F = 1792;
constexpr unsigned SENT = 0xFFFFFFFFu;
constexpr int SPIN = 4096;
constexpr int SC1 = 16;
constexpr int OFF = 0x40000000;
constexpr int WS_EPOCH = 0;
constexpr int WS_FLAGW = 1024;
constexpr int WS_FLAGG = 4096;
constexpr int WS_FLAGH = 8192;
constexpr int WS_MOD = 16384;
constexpr int WS_WQKV = 65536;
constexpr int WS_WO = WS_WQKV + 16 * 84 * 1024;
constexpr int WS_WGU = WS_WO + 28 * 16 * 1024;
constexpr int WS_WD = WS_WGU + 14 * 8 * 28 * 1024;
constexpr int WS_KVP = WS_WD + 7 * 8 * 28 * 1024;
constexpr int WS_O = WS_KVP + 16 * 8 * 16 * 512;
constexpr int WS_Y = WS_O + 128 * 16 * 1024;
constexpr int WS_X1 = WS_Y + 64 * 28 * 1024;
constexpr int WS_ST = WS_X1 + 64 * 28 * 1024;
constexpr int WS_ACT = WS_ST + 64 * 1024;
constexpr int WS_END = WS_ACT + 64 * 112 * 1024;
constexpr int WS_DBG = 32 << 20;
constexpr int L_X = 0;
constexpr int L_W = 0;
constexpr int L_PV = 86016;
constexpr int L_JOB = 114688;
constexpr int L_CA = 147456;
constexpr int L_SA = L_CA + 1792;
constexpr int L_PHR = L_SA + 1792;
constexpr int L_PHF = L_PHR + 4096;
constexpr int L_CST = L_PHF + 4096;
constexpr int L_RED = L_CST + 2048;
constexpr int L_DUMP = L_RED + 2048;
constexpr int L_TOTAL = 163840;
static_assert(L_DUMP + 256 <= L_TOTAL, "lds");
constexpr int L_KVS = L_PHR;
constexpr int L_OB = 0;
constexpr int L_MB = 32768;
constexpr int L_CBF = L_MB + 2048;
constexpr int L_GA = L_CBF + 2048;
constexpr int L_STP = L_GA + 512;
constexpr int L_GB = L_CA;
constexpr int L_YR = 40960;
constexpr int YSLOT = 29696;
constexpr int L_RD = 0;

DI unsigned pk2(float a, float b) { f2 v = {a, b}; return __builtin_bit_cast(unsigned, __builtin_convertvector(v, b2)); }
DI float blo(unsigned u) { return __uint_as_float(u << 16); }
DI float bhi(unsigned u) { return __uint_as_float(u & 0xffff0000u); }
DI float rbf(float a) { return blo(pk2(a, a)); }
DI unsigned nz(unsigned u) { return min(u, 0xFFFFFFFEu); }
DI f4 mfma16(v4u a, v4u b, f4 c) { return __builtin_amdgcn_mfma_f32_16x16x32_bf16(__builtin_bit_cast(b8, a), __builtin_bit_cast(b8, b), c, 0, 0, 0); }
DI f16v mfma32(v4u a, v4u b, f16v c) { return __builtin_amdgcn_mfma_f32_32x32x16_bf16(__builtin_bit_cast(b8, a), __builtin_bit_cast(b8, b), c, 0, 0, 0); }
DI float dot2(unsigned a, unsigned b, float c) { return __builtin_amdgcn_fdot2_f32_bf16(__builtin_bit_cast(b2, a), __builtin_bit_cast(b2, b), c, false); }
DI unsigned mx4(v4u v, unsigned m) { m = max(max(v.x, v.y), m); return max(max(v.z, v.w), m); }
DI v4u mkrs(const void* p, unsigned n) { u64 a = (u64)p; return v4u{(unsigned)a, (unsigned)(a >> 32), n, 0x00020000u}; }
DI void dma16(v4u rs, unsigned voff, unsigned soff, unsigned lds) {
  asm volatile("s_mov_b32 m0, %3\n\ts_nop 0\n\tbuffer_load_dwordx4 %0, %1, %2 offen lds" ::"v"(voff), "s"(rs), "s"(soff), "s"(lds) : "memory");
}
DI void dma16c(v4u rs, unsigned voff, unsigned soff, unsigned lds) {
  asm volatile("s_mov_b32 m0, %3\n\ts_nop 0\n\tbuffer_load_dwordx4 %0, %1, %2 offen sc1 lds" ::"v"(voff), "s"(rs), "s"(soff), "s"(lds) : "memory");
}
DI void dma4(v4u rs, unsigned voff, unsigned soff, unsigned lds) {
  asm volatile("s_mov_b32 m0, %3\n\ts_nop 0\n\tbuffer_load_dword %0, %1, %2 offen lds" ::"v"(voff), "s"(rs), "s"(soff), "s"(lds) : "memory");
}
typedef __amdgpu_buffer_rsrc_t rsrc;
DI v4u ld16(rsrc r, int vo, int so) { return __builtin_amdgcn_raw_buffer_load_b128(r, vo, so, SC1); }
DI v2u ld8(rsrc r, int vo, int so) { return __builtin_amdgcn_raw_buffer_load_b64(r, vo, so, SC1); }
DI unsigned ld4(rsrc r, int vo, int so) { return __builtin_amdgcn_raw_buffer_load_b32(r, vo, so, SC1); }
DI void st16(v4u v, rsrc r, int vo, int so) { __builtin_amdgcn_raw_buffer_store_b128(v, r, vo, so, SC1); }
DI void st8(v2u v, rsrc r, int vo, int so) { __builtin_amdgcn_raw_buffer_store_b64(v, r, vo, so, SC1); }
DI void st4(unsigned v, rsrc r, int vo, int so) { __builtin_amdgcn_raw_buffer_store_b32(v, r, vo, so, SC1); }
DI v4u lr16(unsigned a) { return *(const LAS v4u*)a; }
DI f4 lrf4(unsigned a) { return *(const LAS f4*)a; }
DI void lw16(unsigned a, v4u v) { *(LAS v4u*)a = v; }
DI void lw8(unsigned a, v2u v) { *(LAS v2u*)a = v; }
DI v2u trd(unsigned a) { return __builtin_bit_cast(v2u, __builtin_amdgcn_ds_read_tr16_b64_v4i16((LAS s4*)a)); }
DI v4u trf(unsigned a0, unsigned a1) { v2u x = trd(a0), y = trd(a1); return v4u{x.x, x.y, y.x, y.y}; }
DI float f_(unsigned u) { return __uint_as_float(u); }
DI unsigned u_(float f) { return __float_as_uint(f); }
#define RED4(x, OP) do { auto r_ = __builtin_amdgcn_permlane32_swap(u_(x), u_(x), false, false); x = OP(f_(r_[0]), f_(r_[1])); \
  r_ = __builtin_amdgcn_permlane16_swap(u_(x), u_(x), false, false); x = OP(f_(r_[0]), f_(r_[1])); } while (0)
DI float addf(float a, float b) { return a + b; }

DI void featmap(f4 p0, f4 p1, v4u& fp, v4u& fm) {
  float mx = fmaxf(fmaxf(fmaxf(p0.x, p0.y), fmaxf(p0.z, p0.w)), fmaxf(fmaxf(p1.x, p1.y), p1.z));
  float mn = fminf(fminf(fminf(p0.x, p0.y), fminf(p0.z, p0.w)), fminf(fminf(p1.x, p1.y), p1.z));
  RED4(mx, fmaxf);
  RED4(mn, fminf);
  const float L = 1.44269504089f;
  const float a = -mx * L, b = mn * L;
  float p[7] = {p0.x, p0.y, p0.z, p0.w, p1.x, p1.y, p1.z}, e[7], g[7];
  float se = 0.f, sg = 0.f;
#pragma unroll
  for (int i = 0; i < 7; i++) {
    e[i] = __builtin_amdgcn_exp2f(fmaf(p[i], L, a));
    g[i] = __builtin_amdgcn_exp2f(fmaf(p[i], -L, b));
    se += e[i];
    sg += g[i];
  }
  RED4(se, addf);
  RED4(sg, addf);
  const float re = __builtin_amdgcn_rcpf(se), rg = __builtin_amdgcn_rcpf(sg);
  fp = v4u{pk2(e[0] * re, e[1] * re), pk2(e[2] * re, e[3] * re), pk2(e[4] * re, e[5] * re), pk2(e[6] * re, 0.f)};
  fm = v4u{pk2(g[0] * rg, g[1] * rg), pk2(g[2] * rg, g[3] * rg), pk2(g[4] * rg, g[5] * rg), pk2(g[6] * rg, 0.f)};
}

DI void mod_issue(v4u rm, v4u rc, int blk, int half, int w, int lane) {
  if (w < 7) {
    const int r0 = half * 224 + w * 32;
    dma4(rc, min(lane, 15) * 4, r0 * 2, L_CST + w * 256);
    const unsigned vo = (lane >> 3) * 5376 + (lane & 7) * 16;
#pragma unroll
    for (int i = 0; i < 4; i++) dma16(rm, vo, (r0 + 8 * i) * 5376 + blk * 128, L_JOB + w * 4096 + i * 1024);
  }
}
DI void mod_partial(int w, int lane) {
  float acc[8];
#pragma unroll
  for (int j = 0; j < 8; j++) acc[j] = 0.f;
  if (w < 7) {
#pragma unroll
    for (int i = 0; i < 4; i++) {
      const float cv = blo(*(const LAS u16*)(L_CST + w * 256 + (8 * i + (lane >> 3)) * 2));
      const float sv = rbf(cv * __builtin_amdgcn_rcpf(1.f + __builtin_amdgcn_exp2f(-1.44269504089f * cv)));
      const v4u v = lr16(L_JOB + w * 4096 + i * 1024 + lane * 16);
#pragma unroll
      for (int j = 0; j < 4; j++) { acc[2 * j] = fmaf(sv, blo(v[j]), acc[2 * j]); acc[2 * j + 1] = fmaf(sv, bhi(v[j]), acc[2 * j + 1]); }
    }
  }
  float t[4], u[2];
#pragma unroll
  for (int j = 0; j < 4; j++) { auto r = __builtin_amdgcn_permlane32_swap(u_(acc[j]), u_(acc[4 + j]), false, false); t[j] = f_(r[0]) + f_(r[1]); }
#pragma unroll
  for (int j = 0; j < 2; j++) { auto r = __builtin_amdgcn_permlane16_swap(u_(t[j]), u_(t[2 + j]), false, false); u[j] = f_(r[0]) + f_(r[1]); }
#pragma unroll
  for (int j = 0; j < 2; j++) u[j] += f_(__builtin_amdgcn_update_dpp(0u, u_(u[j]), 0x128  , 0xf, 0xf, false));
  if (!(lane & 8)) lw8(L_RED + w * 256 + ((lane & 7) * 8 + (lane >> 5) * 4 + ((lane >> 4) & 1) * 2) * 4, v2u{u_(u[0]), u_(u[1])});
}
DI void mod_final(rsrc ws, int blk, int half, unsigned tag, int lane) {
  float s = 0.f;
#pragma unroll
  for (int i = 0; i < 7; i++) s += *(const LAS float*)(L_RED + i * 256 + lane * 4);
  st8(v2u{u_(s), tag}, ws, lane * 8, WS_MOD + (half * 2688 + blk * 64) * 8);
}
DI f2 mod_get(rsrc ws, int col, unsigned tag) {
  v4u a, b;
  _Pragma("nounroll") for (int sp = 0; sp < SPIN; sp++) {
    MEMB();
    a = ld16(ws, col * 8, WS_MOD);
    b = ld16(ws, col * 8, WS_MOD + 2688 * 8);
    if (__all(a.y == tag && a.w == tag && b.y == tag && b.w == tag)) break;
  }
  return f2{rbf(f_(a.x) + f_(b.x)), rbf(f_(a.z) + f_(b.z))};
}
DI float gelu_mul(float g, float u) {
  const float t = g * fmaf(g * g, -0.044715f * 2.3022081986f, -2.3022081986f);
  return g * u * __builtin_amdgcn_rcpf(1.f + __builtin_amdgcn_exp2f(t));
}
}

#define STAMP(i) do { SB(); asm volatile("s_memrealtime %0\n\ts_waitcnt lgkmcnt(0)" : "=s"(stamp[i])::"memory"); SB(); } while (0)
#define CODE_LINES 256

__global__ void __launch_bounds__(512, 2)
fused_block(const u16* __restrict__ x, const u16* __restrict__ wqkv, const u16* __restrict__ wphi, const u16* __restrict__ wo,
            const u16* __restrict__ wg, const u16* __restrict__ wu, const u16* __restrict__ wd, const u16* __restrict__ cc,
            const u16* __restrict__ wmod, u16* __restrict__ out, char* __restrict__ wsp) {
  u64 stamp[20];
  STAMP(0);
  const int tid = threadIdx.x, lane = tid & 63, w = __builtin_amdgcn_readfirstlane(tid >> 6);
  const int bid = blockIdx.x, xcd = bid & 7, idx = bid >> 3, par = xcd & 1;
  {
    u64 pc;
    asm volatile("s_getpc_b64 %0" : "=s"(pc));
    dma4(mkrs((const void*)(pc & ~127ull), CODE_LINES * 128), (lane >> 5) * 32768 + (idx * 8 + w) * 128, 0, L_DUMP);
  }
  const int tg = (xcd >> 1) * 4 + (idx & 3), h = par * 8 + (idx >> 2), hq = h ^ 8;
  const int xb = (xcd + 2) & 7;
  const int g = lane >> 4, l15 = lane & 15, q = (lane >> 2) & 3, p = lane & 3;
  const rsrc ws = __builtin_amdgcn_make_buffer_rsrc((void*)wsp, 0, WS_END, 0x00020000);
  const v4u rws = mkrs(wsp, WS_END);
  const v4u rmod = mkrs(wmod, D * 6 * D * 2), rc = mkrs(cc, D * 2);

  const int e = (tg - 2 * (h & 7)) & 15;
  const int mj = (e - 14) + 2 * h;
  const bool is_prep = e < 14, is_mod = !is_prep && mj < 28;
  if (is_prep) {
    if (w == 0) {
      const v4u rq = mkrs(wqkv, D * 3 * D * 2);
      const unsigned vo = (lane >> 2) * 2688 + min((lane & 3) * 16, 40);
#pragma unroll
      for (int m = 0; m < 3; m++) {
        const int col = m == 0 ? 896 + hq * 56 : (m == 1 ? 1792 + hq * 56 : h * 56);
#pragma unroll
        for (int i = 0; i < 2; i++) dma16(rq, vo, (e * 32 + 16 * i) * 2688 + col, L_JOB + m * 2048 + i * 1024);
      }
    }
  } else if (is_mod) {
    mod_issue(rmod, rc, mj >> 1, mj & 1, w, lane);
  }
  if (w == 2 || w == 3) {
    const v4u rp = mkrs(wphi, 16 * 784 * 2);
    const int hh = w == 2 ? h : hq;
    dma16(rp, lane * 16, hh * 1568, L_PHR + (w - 2) * 2048);
    dma16(rp, min(lane, 33) * 16, hh * 1568 + 1024, L_PHR + (w - 2) * 2048 + 1024);
  }
  {
    const v4u rx = mkrs(x, S * D * 2);
    const unsigned v0 = (lane >> 3) * 896 + (((lane & 7) ^ g) * 16);
    const unsigned v1 = (8 + (lane >> 3)) * 896 + (((lane & 7) ^ (4 + g)) * 16);
    const int so = (tg * 128 + w * 16) * 896, lb = L_X + w * 14336;
#pragma unroll
    for (int l = 0; l < 7; l++) {
      dma16(rx, v0, so + l * 128, lb + l * 2048);
      dma16(rx, v1, so + l * 128, lb + l * 2048 + 1024);
    }
  }
  const int u1 = (e * 16 + h) * 4 + (w - 4);
  const bool late1 = is_prep && w >= 4;
  if (late1) {
    const unsigned lb = L_JOB + w * 4096;
    if (u1 < 784) {
      const int ks = u1 / 28, cb = u1 % 28;
      const unsigned vo = (lane >> 3) * 3584 + (lane & 7) * 16;
      const v4u r0 = mkrs(wg, D * F * 2), r1 = mkrs(wu, D * F * 2);
      const int so = ks * 16 * 3584 + cb * 128;
      dma16(r0, vo, so, lb);
      dma16(r0, vo, so + 8 * 3584, lb + 1024);
      dma16(r1, vo, so, lb + 2048);
      dma16(r1, vo, so + 8 * 3584, lb + 3072);
    } else {
      const int v = u1 - 784, hh = v / 7, cb = v % 7;
      const unsigned vo = (lane >> 3) * 896 + (lane & 7) * 16;
      const v4u r0 = mkrs(wo, D * D * 2);
      const int so = hh * 28 * 896 + cb * 128;
#pragma unroll
      for (int i = 0; i < 3; i++) dma16(r0, vo, so + i * 8 * 896, lb + i * 1024);
      dma16(r0, min(lane >> 3, 3) * 896 + (lane & 7) * 16, so + 24 * 896, lb + 3072);
    }
  }
  unsigned tag = *(const CAS unsigned*)(wsp + WS_EPOCH);
  asm volatile("" : "+s"(tag));
  tag = tag * 1664525u + 1013904223u;
  STAMP(1);

  if (is_prep) {
    if (w == 0) {
      VMW(14);
      const unsigned a0 = L_JOB + (8 * g + q) * 64 + p * 8, a1 = L_JOB + (8 * g + q) * 64 + 32 + (p ^ (p >> 1)) * 8;
#pragma unroll
      for (int m = 0; m < 3; m++)
#pragma unroll
        for (int T = 0; T < 2; T++) {
          const unsigned a = (T ? a1 : a0) + m * 2048;
          st16(trf(a, a + 256), ws, lane * 16, WS_WQKV + ((hq * 3 + m) * 28 + e * 2 + T) * 1024);
        }
      VMW(0);
      if (lane == 0) st4(tag, ws, 0, WS_FLAGW + hq * 128 + e * 4);
    }
  } else if (is_mod) {
    VMW(14);
    mod_partial(w, lane);
    BAR();
    if (w == 7) mod_final(ws, mj >> 1, mj & 1, tag, lane);
  }
  STAMP(2);
  if (late1) VMW(4); else if ((is_prep && w == 0) || (is_mod && w == 7)) VMW(1); else VMW(0);
  STAMP(3);
  st8(v2u{SENT, SENT}, ws, lane * 8, WS_KVP + ((h * 8 + w) * 16 + tg) * 512);
  st16(v4u{SENT, SENT, SENT, SENT}, ws, lane * 16, WS_O + ((tg * 8 + w) * 16 + hq) * 1024);

  float xh[14][8];
  {
    const unsigned a0 = L_X + w * 14336 + l15 * 128 + ((g ^ (l15 >> 1)) * 16);
    const unsigned a1 = L_X + w * 14336 + l15 * 128 + (((4 + g) ^ (l15 >> 1)) * 16);
    float s1 = 0.f, s2 = 0.f;
#pragma unroll
    for (int ks = 0; ks < 14; ks++) {
      const v4u v = lr16(((ks & 1) ? a1 : a0) + (ks >> 1) * 2048);
#pragma unroll
      for (int j = 0; j < 4; j++) {
        s1 = dot2(v[j], 0x3F803F80u, s1);
        s2 = dot2(v[j], v[j], s2);
        xh[ks][2 * j] = blo(v[j]);
        xh[ks][2 * j + 1] = bhi(v[j]);
      }
    }
    RED4(s1, addf);
    RED4(s2, addf);
    const float mu = s1 * (1.f / D);
    const float rs = __builtin_amdgcn_rsqf(fmaxf(s2 * (1.f / D) - mu * mu, 0.f) + 1e-5f);
    const float nb = -mu * rs;
#pragma unroll
    for (int ks = 0; ks < 14; ks++)
#pragma unroll
      for (int j = 0; j < 8; j++) { xh[ks][j] = fmaf(xh[ks][j], rs, nb); PIN(xh[ks][j]); }
  }
  SB();
  if (w == 2 || w == 3) {
#pragma unroll
    for (int T = 0; T < 2; T++) {
      const int f = 7 * (l15 >> 2) + 4 * T + (l15 & 3);
      const bool okf = !(T == 1 && (l15 & 3) == 3);
      unsigned v[8];
#pragma unroll
      for (int j = 0; j < 8; j++) {
        const int d = (j < 4 ? 4 * g + j : 12 + 4 * g + j);
        const unsigned t = *(const LAS u16*)(L_PHR + (w - 2) * 2048 + (d * 28 + f) * 2);
        v[j] = (okf && d < 28) ? t : 0u;
      }
      lw16(L_PHF + ((w - 2) * 2 + T) * 1024 + lane * 16, v4u{v[0] | v[1] << 16, v[2] | v[3] << 16, v[4] | v[5] << 16, v[6] | v[7] << 16});
    }
  }
  if (late1) {
    VMW(2);
    const unsigned lb = L_JOB + w * 4096;
    if (u1 < 784) {
      const int ks = u1 / 28, cb = u1 % 28;
      const unsigned a = lb + (16 * (g & 1) + 8 * (g >> 1) + q) * 128 + p * 8;
#pragma unroll
      for (int s = 0; s < 4; s++) st16(trf(a + s * 32, a + s * 32 + 512), ws, lane * 16, WS_WGU + ((cb * 4 + s) * 28 + ks) * 1024);
    } else {
      const int v = u1 - 784, hh = v / 7, cb = v % 7;
      const unsigned a = lb + (4 * g + q) * 128 + p * 8;
#pragma unroll
      for (int s = 0; s < 4; s++) st16(trf(a + s * 32, a + s * 32 + 2048), ws, lane * 16, WS_WO + ((cb * 4 + s) * 16 + hh) * 1024);
    }
  }
  STAMP(4);
  if (w < 7) {
    const f2 m = mod_get(ws, 2 * tid, tag);
    if (tid < 224) lw8(L_SA + tid * 8, v2u{u_(m.x), u_(m.y)});
    else lw8(L_CA + (tid - 224) * 8, v2u{u_(rbf(1.f + m.x)), u_(rbf(1.f + m.y))});
  } else {
    _Pragma("nounroll") for (int sp = 0; sp < SPIN; sp++) {
      MEMB();
      const unsigned f = ld4(ws, min(lane, 13) * 4, WS_FLAGW + h * 128);
      if (__all(f == tag)) break;
    }
  }
  BAR();
  STAMP(5);
  if (w < 7) {
#pragma unroll
    for (int m = 0; m < 3; m++)
#pragma unroll
      for (int i = 0; i < 4; i++) dma16c(rws, lane * 16, WS_WQKV + (h * 84 + m * 28 + w * 4 + i) * 1024, L_W + (m * 28 + w * 4 + i) * 1024);
  }
  const bool mod2 = bid < 56;
  const int u2 = (bid - 56) * 2 + w;
  const bool late2 = !mod2 && w < 2 && u2 < 392;
  if (mod2) {
    mod_issue(rmod, rc, 14 + (bid >> 1), bid & 1, w, lane);
  } else if (late2) {
    const int kp = u2 / 7, cb = u2 % 7;
    const unsigned vo = (lane >> 3) * 896 + (lane & 7) * 16;
    const v4u r0 = mkrs(wd, F * D * 2);
#pragma unroll
    for (int i = 0; i < 4; i++) dma16(r0, vo, (kp * 32 + i * 8) * 896 + cb * 128, L_JOB + w * 4096 + i * 1024);
  }
  v4u hf[14];
#pragma unroll
  for (int ks = 0; ks < 14; ks++) {
    const f4 c0 = lrf4(L_CA + ks * 128 + g * 32), c1 = lrf4(L_CA + ks * 128 + g * 32 + 16);
    const f4 s0 = lrf4(L_SA + ks * 128 + g * 32), s1 = lrf4(L_SA + ks * 128 + g * 32 + 16);
    hf[ks] = v4u{pk2(fmaf(xh[ks][0], c0.x, s0.x), fmaf(xh[ks][1], c0.y, s0.y)), pk2(fmaf(xh[ks][2], c0.z, s0.z), fmaf(xh[ks][3], c0.w, s0.w)),
                 pk2(fmaf(xh[ks][4], c1.x, s1.x), fmaf(xh[ks][5], c1.y, s1.y)), pk2(fmaf(xh[ks][6], c1.z, s1.z), fmaf(xh[ks][7], c1.w, s1.w))};
    PIN(hf[ks]);
  }
  SB();
  if (w == 7) VMW(0); else if (mod2) VMW(13); else if (late2) VMW(12); else VMW(8);
  BAR();
  STAMP(6);
  if (tid == 448) st4(tag, ws, 0, WS_FLAGG + bid * 4);
  const int t32 = xb * 8 + (idx & 7), cq = idx >> 3, ct = cq * 7 + w;
  const int jj = idx >> 2, j = par * 7 + jj;
  const bool ffn = jj < 7;
  const int v7 = w < 7 ? lane * 16 : OFF, vf = ffn ? lane * 16 : OFF;
  {
    const v4u sv = {SENT, SENT, SENT, SENT};
    st16(sv, ws, v7, WS_Y + (t32 * 28 + ct) * 1024);
    st16(sv, ws, (w < 7 || lane < 16) ? lane * 16 : OFF, w < 7 ? WS_X1 + (t32 * 28 + ct) * 1024 : WS_ST + t32 * 1024 + cq * 256);
#pragma unroll
    for (int t = 0; t < 4; t++) st16(sv, ws, vf, WS_ACT + (((tg * 4 + t) * 8 + w) * 14 + j) * 1024);
  }
  const unsigned wa = L_W + lane * 16;
  f4 z4 = {0.f, 0.f, 0.f, 0.f};
  f4 k0 = z4, k1 = z4;
#pragma unroll
  for (int ks = 0; ks < 14; ks++) {
    k0 = mfma16(lr16(wa + (ks * 2) * 1024), hf[ks], k0);
    k1 = mfma16(lr16(wa + (ks * 2 + 1) * 1024), hf[ks], k1);
  }
  PIN(k0); PIN(k1);
  SB();
  if (mod2) VMW(11); else if (late2) VMW(10); else VMW(6);
  BAR();
  STAMP(7);
  v4u gfl = ld16(ws, lane * 16, WS_FLAGG);
  {
    const v4u kb = {pk2(k0.x, k0.y), pk2(k0.z, k0.w), pk2(k1.x, k1.y), pk2(k1.z, k1.w)};
    const f4 p0 = mfma16(lr16(L_PHF + lane * 16), kb, z4), p1 = mfma16(lr16(L_PHF + 1024 + lane * 16), kb, z4);
    f4 v0 = z4, v1 = z4;
#pragma unroll
    for (int ks = 0; ks < 14; ks++) {
      v0 = mfma16(lr16(wa + (28 + ks * 2) * 1024), hf[ks], v0);
      v1 = mfma16(lr16(wa + (28 + ks * 2 + 1) * 1024), hf[ks], v1);
    }
    v4u fp, fm;
    featmap(p0, p1, fp, fm);
    const unsigned a = L_PV + (w * 16 + l15) * 200 + g * 8;
    lw8(a, v2u{fp.x, fp.y});
    lw8(a + 32, v2u{fp.z, fp.w});
    lw8(a + 64, v2u{fm.x, fm.y});
    lw8(a + 96, v2u{fm.z, fm.w});
    lw8(a + 128, v2u{pk2(v0.x, v0.y), pk2(v0.z, v0.w)});
    lw8(a + 160, g == 3 ? v2u{0x3F803F80u, 0x3F803F80u} : v2u{pk2(v1.x, v1.y), pk2(v1.z, v1.w)});
  }
  _Pragma("nounroll") for (int sp = 0; sp < SPIN; sp++) {
    if (__all(gfl.x == tag && gfl.y == tag && gfl.z == tag && gfl.w == tag)) break;
    MEMB();
    gfl = ld16(ws, lane * 16, WS_FLAGG);
  }
  MEMB();
  v4u wof[16];
  {
    const int so = WS_WO + ct * 16384;
#pragma unroll
    for (int i = 0; i < 16; i++) wof[i] = ld16(ws, v7 + (i & 3) * 1024, so + (i >> 2) * 4096);
  }
  SB();
  BAR();
  STAMP(8);
  {
    const unsigned a = L_PV + (8 * g + q) * 200 + p * 8;
    const unsigned aa = a + (w >> 1) * 32, ab = a + 128 + (w & 1) * 32;
    f4 kv = z4;
#pragma unroll
    for (int kk = 0; kk < 4; kk++) kv = mfma16(trf(aa + kk * 6400, aa + kk * 6400 + 800), trf(ab + kk * 6400, ab + kk * 6400 + 800), kv);
    st8(v2u{nz(pk2(kv.x, kv.y)), nz(pk2(kv.z, kv.w))}, ws, lane * 8, WS_KVP + ((h * 8 + w) * 16 + tg) * 512);
  }
  SB();
  STAMP(9);
  if (mod2) {
    mod_partial(w, lane);
    BAR();
    if (w == 7) mod_final(ws, 14 + (bid >> 1), bid & 1, tag, lane);
  } else if (late2) {
    const int kp = u2 / 7, cb = u2 % 7;
    const unsigned a = L_JOB + w * 4096 + (4 * (g >> 1) + q) * 128 + (g & 1) * 32 + p * 8;
#pragma unroll
    for (int s = 0; s < 4; s++) {
      const int kst = kp * 2 + (s >> 1);
      const unsigned b = a + (s >> 1) * 2048 + (s & 1) * 64;
      st16(trf(b, b + 1024), ws, lane * 16, WS_WD + (((cb * 8 + (kst & 7)) * 14 + (kst >> 3)) * 2 + (s & 1)) * 1024);
    }
  }
  v4u qp, qm;
  v4u kvr[8];
  const int kvo = WS_KVP + (hq * 8 + w) * 8192;
  {
    f4 q0 = z4, q1 = z4;
#pragma unroll
    for (int ks = 0; ks < 14; ks++) {
      q0 = mfma16(lr16(wa + (56 + ks * 2) * 1024), hf[ks], q0);
      q1 = mfma16(lr16(wa + (56 + ks * 2 + 1) * 1024), hf[ks], q1);
    }
    SB();
#pragma unroll
    for (int i = 0; i < 8; i++) kvr[i] = ld16(ws, lane * 16 + (i & 3) * 1024, kvo + (i >> 2) * 4096);
    SB();
    const v4u qb = {pk2(q0.x, q0.y), pk2(q0.z, q0.w), pk2(q1.x, q1.y), pk2(q1.z, q1.w)};
    featmap(mfma16(lr16(L_PHF + 2048 + lane * 16), qb, z4), mfma16(lr16(L_PHF + 3072 + lane * 16), qb, z4), qp, qm);
    PIN(qp); PIN(qm);
  }
  SB();
  STAMP(10);
  _Pragma("nounroll") for (int sp = 0; sp < SPIN; sp++) {
    unsigned m = 0;
#pragma unroll
    for (int i = 0; i < 8; i++) m = mx4(kvr[i], m);
    if (__all(m != SENT)) break;
    MEMB();
#pragma unroll
    for (int i = 0; i < 8; i++) kvr[i] = ld16(ws, lane * 16 + (i & 3) * 1024, kvo + (i >> 2) * 4096);
  }
  MEMB();
  constexpr int NW2 = 8;
  v4u wgf[28];
  const int wgo = WS_WGU + (j * 8 + w) * 28 * 1024;
#pragma unroll
  for (int i = 0; i < NW2; i++) wgf[i] = ld16(ws, vf + (i & 3) * 1024, wgo + (i >> 2) * 4096);
  const v4u xres = *(const GAS v4u*)(x + (t32 * 32 + (lane & 31)) * D + min(ct, 27) * 16 + (lane >> 5) * 8);
  SB();
  {
    float s[8];
#pragma unroll
    for (int c = 0; c < 8; c++) s[c] = 0.f;
#pragma unroll
    for (int i = 0; i < 8; i++)
#pragma unroll
      for (int c = 0; c < 4; c++) { s[2 * c] += blo(kvr[i][c]); s[2 * c + 1] += bhi(kvr[i][c]); }
    float t[4];
#pragma unroll
    for (int c = 0; c < 4; c++) { auto r = __builtin_amdgcn_permlane32_swap(u_(s[c]), u_(s[4 + c]), false, false); t[c] = f_(r[0]) + f_(r[1]); }
    lw8(L_KVS + (w >> 2) * 2048 + (w & 1) * 1024 + (2 * (lane & 31) + (lane >> 5)) * 16 + ((w >> 1) & 1) * 8, v2u{pk2(t[0], t[1]), pk2(t[2], t[3])});
  }
  BAR();
  STAMP(11);
  if (tid == 448) st4(tag, ws, 0, WS_FLAGH + bid * 4);
  {
    f4 o0 = mfma16(lr16(L_KVS + lane * 16), qp, z4), o1 = mfma16(lr16(L_KVS + 1024 + lane * 16), qp, z4);
    o0 = mfma16(lr16(L_KVS + 2048 + lane * 16), qm, o0);
    o1 = mfma16(lr16(L_KVS + 3072 + lane * 16), qm, o1);
    auto r = __builtin_amdgcn_permlane32_swap(u_(o1.x), u_(o1.x), false, false);
    r = __builtin_amdgcn_permlane16_swap(r[1], r[1], false, false);
    const float rd = __builtin_amdgcn_rcpf(f_(r[1]) + 1e-6f);
    v4u o = {nz(pk2(o0.x * rd, o0.y * rd)), nz(pk2(o0.z * rd, o0.w * rd)), nz(pk2(o1.x * rd, o1.y * rd)), nz(pk2(o1.z * rd, o1.w * rd))};
    if (g == 3) { o.z = 0; o.w = 0; }
    st16(o, ws, lane * 16, WS_O + ((tg * 8 + w) * 16 + hq) * 1024);
  }
  {
    const int col = w < 7 ? 1344 + 2 * tid : 896 + min(cq * 112 + 2 * lane, 446);
    const f2 m = mod_get(ws, col, tag);
    if (w == 7) lw8(L_GA + lane * 8, v2u{u_(m.x), u_(m.y)});
    else if (tid < 224) *(LAS unsigned*)(L_MB + 896 + tid * 4) = pk2(m.x, m.y);
    else {
      const unsigned c1 = pk2(1.f + m.x, 1.f + m.y);
      *(LAS unsigned*)(L_MB + (tid - 224) * 4) = c1;
      lw8(L_CBF + (tid - 224) * 8, v2u{c1 << 16, c1 & 0xffff0000u});
    }
  }
  VMW(0);
  STAMP(12);
  {
    v4u ob[4], hfl;
    _Pragma("nounroll") for (int sp = 0; sp < SPIN; sp++) {
      MEMB();
#pragma unroll
      for (int i = 0; i < 4; i++) ob[i] = ld16(ws, lane * 16 + i * 1024, WS_O + t32 * 32768 + w * 4096);
      unsigned m = 0;
#pragma unroll
      for (int i = 0; i < 4; i++) m = mx4(ob[i], m);
      if (__all(m != SENT)) break;
    }
    MEMB();
    hfl = ld16(ws, lane * 16, WS_FLAGH);
#pragma unroll
    for (int i = NW2; i < 28; i++) wgf[i] = ld16(ws, vf + (i & 3) * 1024, wgo + (i >> 2) * 4096);
    SB();
#pragma unroll
    for (int i = 0; i < 4; i++) lw16(L_OB + w * 4096 + i * 1024 + lane * 16, ob[i]);
    BAR();
    STAMP(13);
    if (bid == 0 && tid == 0) st4(tag, ws, 0, WS_EPOCH);
    if (w < 7) {
      f4 a0 = z4, a1 = z4;
#pragma unroll
      for (int i = 0; i < 16; i++) {
        a0 = mfma16(wof[i], lr16(L_OB + i * 1024 + lane * 16), a0);
        a1 = mfma16(wof[i], lr16(L_OB + (16 + i) * 1024 + lane * 16), a1);
      }
      float v[8];
#pragma unroll
      for (int c = 0; c < 4; c++) { auto r = __builtin_amdgcn_permlane16_swap(u_(a0[c]), u_(a1[c]), false, false); v[c] = f_(r[0]); v[4 + c] = f_(r[1]); }
      const int c0 = w * 16 + (lane >> 5) * 8;
      const f4 g0 = lrf4(L_GA + c0 * 4), g1 = lrf4(L_GA + c0 * 4 + 16);
      const f4 b0 = lrf4(L_CBF + (cq * 112 + c0) * 4), b1 = lrf4(L_CBF + (cq * 112 + c0) * 4 + 16);
      float s1 = 0.f, s2 = 0.f;
#pragma unroll
      for (int c = 0; c < 8; c++) {
        const float xv = (c & 1) ? bhi(xres[c >> 1]) : blo(xres[c >> 1]);
        v[c] = fmaf(c < 4 ? g0[c & 3] : g1[c & 3], v[c], xv);
        s1 += v[c];
        s2 = fmaf(v[c], v[c], s2);
      }
      st16(v4u{nz(pk2(v[0] * b0.x, v[1] * b0.y)), nz(pk2(v[2] * b0.z, v[3] * b0.w)), nz(pk2(v[4] * b1.x, v[5] * b1.y)), nz(pk2(v[6] * b1.z, v[7] * b1.w))},
           ws, lane * 16, WS_Y + (t32 * 28 + ct) * 1024);
      auto r = __builtin_amdgcn_permlane32_swap(u_(s1), u_(s2), false, false);
      *(LAS float*)(L_STP + w * 256 + (lane & 31) * 8 + (lane >> 5) * 4) = f_(r[0]) + f_(r[1]);
      st16(v4u{nz(pk2(v[0], v[1])), nz(pk2(v[2], v[3])), nz(pk2(v[4], v[5])), nz(pk2(v[6], v[7]))}, ws, lane * 16, WS_X1 + (t32 * 28 + ct) * 1024);
    }
    BAR();
    if (w == 7) {
      float s = 0.f;
#pragma unroll
      for (int i = 0; i < 7; i++) s += *(const LAS float*)(L_STP + i * 256 + lane * 4);
      st4(nz(u_(s)), ws, lane * 4, WS_ST + t32 * 1024 + cq * 256);
    }
    STAMP(14);
    _Pragma("nounroll") for (int sp = 0; sp < SPIN; sp++) {
      if (__all(hfl.x == tag && hfl.y == tag && hfl.z == tag && hfl.w == tag)) break;
      MEMB();
      hfl = ld16(ws, lane * 16, WS_FLAGH);
    }
    MEMB();
  }
  if (ffn) {
    f16v z16;
#pragma unroll
    for (int i = 0; i < 16; i++) z16[i] = 0.f;
    v4u fixa;
    {
      f16v ub = z16;
      const unsigned a = L_MB + (lane & 1) * 896 + (lane >> 5) * 16;
#pragma unroll
      for (int ks = 0; ks < 28; ks++) ub = mfma32(lr16(a + ks * 32), wgf[ks], ub);
      const float uh = rbf(ub[0]), bh = rbf(ub[1]);
      fixa = v4u{pk2(uh, uh), pk2(ub[0] - uh, bh), pk2(bh, ub[1] - bh), 0u};
    }
    if (w == 7) {
      const f2 m = mod_get(ws, 2240 + min(jj * 64 + 2 * lane, 446), tag);
      lw8(L_GB + lane * 8, v2u{u_(m.x), u_(m.y)});
    }
    VMW(0);
    STAMP(15);
    const int tx = 2 * (j & 1);
    v4u yb[4];
    auto y_issue = [&](int t) {
      const int tt = tg * 4 + (t ^ tx);
      if (w < 7) {
#pragma unroll
        for (int i = 0; i < 4; i++) yb[i] = ld16(ws, lane * 16 + i * 1024, WS_Y + (tt * 28 + w * 4) * 1024);
      } else {
        const v2u a = ld8(ws, (lane & 31) * 8 + (lane >> 5) * 512, WS_ST + tt * 1024), b = ld8(ws, (lane & 31) * 8 + (lane >> 5) * 512 + 256, WS_ST + tt * 1024);
        yb[0] = v4u{a.x, a.y, b.x, b.y};
        yb[1] = yb[2] = yb[3] = v4u{0, 0, 0, 0};
      }
    };
    auto y_commit = [&](int t) {
      _Pragma("nounroll") for (int sp = 0; sp < SPIN; sp++) {
        unsigned m = 0;
#pragma unroll
        for (int i = 0; i < 4; i++) m = mx4(yb[i], m);
        if (__all(m != SENT)) break;
        MEMB();
        y_issue(t);
      }
      MEMB();
      const unsigned sl = L_YR + (t & 1) * YSLOT + lane * 16;
      if (w < 7) {
#pragma unroll
        for (int i = 0; i < 4; i++) lw16(sl + (w * 4 + i) * 1024, yb[i]);
      } else {
        float s1 = f_(yb[0].x) + f_(yb[0].z), s2 = f_(yb[0].y) + f_(yb[0].w);
        auto r = __builtin_amdgcn_permlane32_swap(u_(s1), u_(s1), false, false);
        s1 = f_(r[0]) + f_(r[1]);
        r = __builtin_amdgcn_permlane32_swap(u_(s2), u_(s2), false, false);
        s2 = f_(r[0]) + f_(r[1]);
        const float mu = s1 * (1.f / D);
        const float var = fmaxf(s2 * (1.f / D) - mu * mu, 0.f) + 1e-5f;
        const float rs = __builtin_amdgcn_rsqf(var), sg = var * rs;
        const float mh = rbf(mu), sh = rbf(sg);
        v4u fx = {pk2(-mh, mh - mu), pk2(-mh, sh), pk2(sg - sh, sh), u_(rs)};
        if (lane >= 32) { fx.x = 0; fx.y = 0; fx.z = 0; }
        lw16(sl + 28 * 1024, fx);
      }
    };
    y_issue(0);
    y_commit(0);
    y_issue(1);
    SB();
    BAR();
    STAMP(16);
#pragma unroll
    for (int t = 0; t < 4; t++) {
      const unsigned sl = L_YR + (t & 1) * YSLOT + lane * 16;
      f16v acc = z16;
#pragma unroll
      for (int ks = 0; ks < 14; ks++) acc = mfma32(wgf[ks], lr16(sl + ks * 1024), acc);
      if (t < 3) {
        SB();
        y_commit(t + 1);
        if (t < 2) y_issue(t + 2);
        SB();
      }
#pragma unroll
      for (int ks = 14; ks < 28; ks++) acc = mfma32(wgf[ks], lr16(sl + ks * 1024), acc);
      v4u fx = lr16(sl + 28 * 1024);
      const float rs = f_(fx.w);
      fx.w = 0;
      acc = mfma32(fixa, fx, acc);
      BAR();
      float a[8];
#pragma unroll
      for (int i = 0; i < 8; i++) a[i] = gelu_mul(acc[i] * rs, acc[8 + i] * rs);
      st16(v4u{nz(pk2(a[0], a[1])), nz(pk2(a[2], a[3])), nz(pk2(a[4], a[5])), nz(pk2(a[6], a[7]))}, ws, lane * 16,
           WS_ACT + (((tg * 4 + (t ^ tx)) * 8 + w) * 14 + j) * 1024);
    }
    STAMP(17);
    const int t64 = xb * 4 + (idx & 3), ue = t64 & 1;
    const int wdo = WS_WD + (jj * 8 + w) * 28 * 1024;
    const int ao = WS_ACT + (t64 * 16 + w) * 14 * 1024;
    f16v dacc[2][2] = {{z16, z16}, {z16, z16}};
    const v4u x1v0 = ld16(ws, ((tid >> 8) * 28 + ((tid & 7) >> 1)) * 1024 + (((tid >> 3) & 31) + (tid & 1) * 32) * 16, WS_X1 + (t64 * 56 + jj * 4) * 1024);
#pragma unroll
    for (int hf2 = 0; hf2 < 2; hf2++) {
      const int jp = hf2 ^ ue;
      v4u A[14], B[2][7];
#pragma unroll
      for (int i = 0; i < 7; i++) {
        A[2 * i] = ld16(ws, lane * 16, wdo + (2 * i + jp) * 2048);
        A[2 * i + 1] = ld16(ws, lane * 16 + 1024, wdo + (2 * i + jp) * 2048);
      }
#pragma unroll
      for (int a = 0; a < 2; a++)
#pragma unroll
        for (int i = 0; i < 7; i++) B[a][i] = ld16(ws, lane * 16, ao + a * 114688 + (2 * i + jp) * 1024);
      SB();
#pragma unroll
      for (int a = 0; a < 2; a++) {
        _Pragma("nounroll") for (int sp = 0; sp < SPIN; sp++) {
          unsigned m = 0;
#pragma unroll
          for (int i = 0; i < 7; i++) m = mx4(B[a][i], m);
          if (__all(m != SENT)) break;
          MEMB();
#pragma unroll
          for (int i = 0; i < 7; i++) B[a][i] = ld16(ws, lane * 16, ao + a * 114688 + (2 * i + jp) * 1024);
        }
        MEMB();
#pragma unroll
        for (int i = 0; i < 7; i++) {
          dacc[a][0] = mfma32(A[2 * i], B[a][i], dacc[a][0]);
          dacc[a][1] = mfma32(A[2 * i + 1], B[a][i], dacc[a][1]);
        }
      }
    }
    STAMP(18);
    {
      const int tk = lane & 31, hh = lane >> 5;
#pragma unroll
      for (int a = 0; a < 2; a++)
#pragma unroll
        for (int c = 0; c < 2; c++)
#pragma unroll
          for (int r = 0; r < 4; r++)
            lw16(L_RD + (w * 2 + a) * 8192 + tk * 256 + (((8 * c + 2 * r + hh) ^ (tk & 15)) * 16),
                 v4u{u_(dacc[a][c][4 * r]), u_(dacc[a][c][4 * r + 1]), u_(dacc[a][c][4 * r + 2]), u_(dacc[a][c][4 * r + 3])});
    }
    v4u x1v = x1v0;
    _Pragma("nounroll") for (int sp = 0; sp < SPIN; sp++) {
      if (__all(mx4(x1v, 0) != SENT)) break;
      MEMB();
      x1v = ld16(ws, ((tid >> 8) * 28 + ((tid & 7) >> 1)) * 1024 + (((tid >> 3) & 31) + (tid & 1) * 32) * 16, WS_X1 + (t64 * 56 + jj * 4) * 1024);
    }
    BAR();
    {
      const int tk = (tid >> 3) & 31, a = tid >> 8, ch = tid & 7;
      const unsigned b0 = L_RD + a * 8192 + tk * 256 + (((2 * ch) ^ (tk & 15)) * 16), b1 = L_RD + a * 8192 + tk * 256 + (((2 * ch + 1) ^ (tk & 15)) * 16);
      f4 s0 = z4, s1 = z4;
#pragma unroll
      for (int i = 0; i < 8; i++) { s0 += lrf4(b0 + i * 16384); s1 += lrf4(b1 + i * 16384); }
      const f4 g0 = lrf4(L_GB + ch * 32), g1 = lrf4(L_GB + ch * 32 + 16);
      const v4u o = {pk2(fmaf(g0.x, s0.x, blo(x1v.x)), fmaf(g0.y, s0.y, bhi(x1v.x))), pk2(fmaf(g0.z, s0.z, blo(x1v.y)), fmaf(g0.w, s0.w, bhi(x1v.y))),
                     pk2(fmaf(g1.x, s1.x, blo(x1v.z)), fmaf(g1.y, s1.y, bhi(x1v.z))), pk2(fmaf(g1.z, s1.z, blo(x1v.w)), fmaf(g1.w, s1.w, bhi(x1v.w)))};
      *(GAS v4u*)(out + (t64 * 64 + (tid >> 3)) * D + jj * 64 + ch * 8) = o;
    }
  }
  VMW(0);
  STAMP(19);
  if (lane == 0) {
    GAS u64* dp = (GAS u64*)(wsp + WS_DBG) + (bid * 8 + w) * 32;
#pragma unroll
    for (int i = 0; i < 20; i++) dp[i] = (ffn || i < 15 || i == 19) ? stamp[i] : 0;
  }
}

__global__ void k_amp(const unsigned long long* rec){
  __shared__ unsigned long long sm[3][256];
  unsigned long long t0=~0ull, mx=0, sum=0; unsigned cnt=0;
  for(int i=threadIdx.x;i<2048;i+=256){ unsigned long long a=rec[i*32], b=rec[i*32+PSTAMP]; t0=a<t0?a:t0; if(b){ mx=b>mx?b:mx; sum+=b; cnt++; } }
  sm[0][threadIdx.x]=t0; sm[1][threadIdx.x]=PKIND==0?mx:sum; sm[2][threadIdx.x]=cnt;
  __syncthreads();
  if(threadIdx.x==0){
    t0=~0ull; mx=0; sum=0; unsigned long long c=0;
    for(int i=0;i<256;i++){ t0=sm[0][i]<t0?sm[0][i]:t0; mx=sm[1][i]>mx?sm[1][i]:mx; sum+=sm[1][i]; c+=sm[2][i]; }
    unsigned long long tau = PKIND==0 ? (mx-t0)*100 : ((sum-c*t0)*100)/c;
    if(tau>1000000) tau=1000000;
    unsigned long long s=__builtin_amdgcn_s_memrealtime(); while(__builtin_amdgcn_s_memrealtime()<s+tau) __builtin_amdgcn_s_sleep(4);
  }
}

extern "C" void kernel_launch(void* const* d_in, const int* in_sizes, int n_in, void* d_out, int out_size, void* d_ws, size_t ws_size,
                              hipStream_t stream) {
  (void)hipFuncSetAttribute((const void*)fused_block, hipFuncAttributeMaxDynamicSharedMemorySize, L_TOTAL);
  fused_block<<<256, 512, L_TOTAL, stream>>>((const u16*)d_in[0], (const u16*)d_in[1], (const u16*)d_in[2], (const u16*)d_in[3], (const u16*)d_in[5],
                                             (const u16*)d_in[6], (const u16*)d_in[7], (const u16*)d_in[8], (const u16*)d_in[9], (u16*)d_out, (char*)d_ws);
  k_amp<<<1,256,0,stream>>>((const unsigned long long*)((char*)d_ws+WS_DBG));
}
```

```cpp
#define PK 2
#include <hip/hip_runtime.h>
#include <hip/hip_bf16.h>
#pragma clang diagnostic ignored "-Wint-to-pointer-cast"

typedef unsigned short u16;
typedef unsigned long long u64;
typedef unsigned v4u __attribute__((ext_vector_type(4)));
typedef unsigned v2u __attribute__((ext_vector_type(2)));
typedef float f2 __attribute__((ext_vector_type(2)));
typedef float f4 __attribute__((ext_vector_type(4)));
typedef float f16v __attribute__((ext_vector_type(16)));
typedef __bf16 b2 __attribute__((ext_vector_type(2)));
typedef __bf16 b8 __attribute__((ext_vector_type(8)));
typedef short s4 __attribute__((ext_vector_type(4)));
#define GAS __attribute__((address_space(1)))
#define LAS __attribute__((address_space(3)))
#define CAS __attribute__((address_space(4)))
#define DI __device__ __forceinline__
#define SB() __builtin_amdgcn_sched_barrier(0)
#define MEMB() asm volatile("" ::: "memory")
#define PIN(x) asm volatile("" : "+v"(x))
#define VMW(n) asm volatile("s_waitcnt vmcnt(" #n ")" ::: "memory")
#define BAR() do { asm volatile("s_waitcnt lgkmcnt(0)" ::: "memory"); __builtin_amdgcn_s_barrier(); MEMB(); } while (0)

namespace {
constexpr int S = 2048, D = 448, F = 1792;
constexpr unsigned SENT = 0xFFFFFFFFu;
constexpr int SPIN = 4096;
constexpr int SC1 = 16;
constexpr int OFF = 0x40000000;
constexpr int WS_EPOCH = 0;
constexpr int WS_FLAGW = 1024;
constexpr int WS_FLAGG = 4096;
constexpr int WS_FLAGH = 8192;
constexpr int WS_MOD = 16384;
constexpr int WS_WQKV = 65536;
constexpr int WS_WO = WS_WQKV + 16 * 84 * 1024;
constexpr int WS_WGU = WS_WO + 28 * 16 * 1024;
constexpr int WS_WD = WS_WGU + 14 * 8 * 28 * 1024;
constexpr int WS_KVP = WS_WD + 7 * 8 * 28 * 1024;
constexpr int WS_O = WS_KVP + 16 * 8 * 16 * 512;
constexpr int WS_Y = WS_O + 128 * 16 * 1024;
constexpr int WS_X1 = WS_Y + 64 * 28 * 1024;
constexpr int WS_ST = WS_X1 + 64 * 28 * 1024;
constexpr int WS_ACT = WS_ST + 64 * 1024;
constexpr int WS_END = WS_ACT + 64 * 112 * 1024;
constexpr int WS_DBG = 32 << 20;
constexpr int L_X = 0;
constexpr int L_W = 0;
constexpr int L_PV = 86016;
constexpr int L_JOB = 114688;
constexpr int L_CA = 147456;
constexpr int L_SA = L_CA + 1792;
constexpr int L_PHR = L_SA + 1792;
constexpr int L_PHF = L_PHR + 4096;
constexpr int L_CST = L_PHF + 4096;
constexpr int L_RED = L_CST + 2048;
constexpr int L_DUMP = L_RED + 2048;
constexpr int L_TOTAL = 163840;
static_assert(L_DUMP + 256 <= L_TOTAL, "lds");
constexpr int L_KVS = L_PHR;
constexpr int L_OB = 0;
constexpr int L_MB = 32768;
constexpr int L_CBF = L_MB + 2048;
constexpr int L_GA = L_CBF + 2048;
constexpr int L_STP = L_GA + 512;
constexpr int L_GB = L_CA;
constexpr int L_YR = 40960;
constexpr int YSLOT = 29696;
constexpr int L_RD = 0;

DI unsigned pk2(float a, float b) { f2 v = {a, b}; return __builtin_bit_cast(unsigned, __builtin_convertvector(v, b2)); }
DI float blo(unsigned u) { return __uint_as_float(u << 16); }
DI float bhi(unsigned u) { return __uint_as_float(u & 0xffff0000u); }
DI float rbf(float a) { return blo(pk2(a, a)); }
DI unsigned nz(unsigned u) { return min(u, 0xFFFFFFFEu); }
DI f4 mfma16(v4u a, v4u b, f4 c) { return __builtin_amdgcn_mfma_f32_16x16x32_bf16(__builtin_bit_cast(b8, a), __builtin_bit_cast(b8, b), c, 0, 0, 0); }
DI f16v mfma32(v4u a, v4u b, f16v c) { return __builtin_amdgcn_mfma_f32_32x32x16_bf16(__builtin_bit_cast(b8, a), __builtin_bit_cast(b8, b), c, 0, 0, 0); }
DI float dot2(unsigned a, unsigned b, float c) { return __builtin_amdgcn_fdot2_f32_bf16(__builtin_bit_cast(b2, a), __builtin_bit_cast(b2, b), c, false); }
DI unsigned mx4(v4u v, unsigned m) { m = max(max(v.x, v.y), m); return max(max(v.z, v.w), m); }
DI v4u mkrs(const void* p, unsigned n) { u64 a = (u64)p; return v4u{(unsigned)a, (unsigned)(a >> 32), n, 0x00020000u}; }
DI void dma16(v4u rs, unsigned voff, unsigned soff, unsigned lds) {
  asm volatile("s_mov_b32 m0, %3\n\ts_nop 0\n\tbuffer_load_dwordx4 %0, %1, %2 offen lds" ::"v"(voff), "s"(rs), "s"(soff), "s"(lds) : "memory");
}
DI void dma16c(v4u rs, unsigned voff, unsigned soff, unsigned lds) {
  asm volatile("s_mov_b32 m0, %3\n\ts_nop 0\n\tbuffer_load_dwordx4 %0, %1, %2 offen sc1 lds" ::"v"(voff), "s"(rs), "s"(soff), "s"(lds) : "memory");
}
DI void dma4(v4u rs, unsigned voff, unsigned soff, unsigned lds) {
  asm volatile("s_mov_b32 m0, %3\n\ts_nop 0\n\tbuffer_load_dword %0, %1, %2 offen lds" ::"v"(voff), "s"(rs), "s"(soff), "s"(lds) : "memory");
}
typedef __amdgpu_buffer_rsrc_t rsrc;
DI v4u ld16(rsrc r, int vo, int so) { return __builtin_amdgcn_raw_buffer_load_b128(r, vo, so, SC1); }
DI v2u ld8(rsrc r, int vo, int so) { return __builtin_amdgcn_raw_buffer_load_b64(r, vo, so, SC1); }
DI unsigned ld4(rsrc r, int vo, int so) { return __builtin_amdgcn_raw_buffer_load_b32(r, vo, so, SC1); }
DI void st16(v4u v, rsrc r, int vo, int so) { __builtin_amdgcn_raw_buffer_store_b128(v, r, vo, so, SC1); }
DI void st8(v2u v, rsrc r, int vo, int so) { __builtin_amdgcn_raw_buffer_store_b64(v, r, vo, so, SC1); }
DI void st4(unsigned v, rsrc r, int vo, int so) { __builtin_amdgcn_raw_buffer_store_b32(v, r, vo, so, SC1); }
DI v4u lr16(unsigned a) { return *(const LAS v4u*)a; }
DI f4 lrf4(unsigned a) { return *(const LAS f4*)a; }
DI void lw16(unsigned a, v4u v) { *(LAS v4u*)a = v; }
DI void lw8(unsigned a, v2u v) { *(LAS v2u*)a = v; }
DI v2u trd(unsigned a) { return __builtin_bit_cast(v2u, __builtin_amdgcn_ds_read_tr16_b64_v4i16((LAS s4*)a)); }
DI v4u trf(unsigned a0, unsigned a1) { v2u x = trd(a0), y = trd(a1); return v4u{x.x, x.y, y.x, y.y}; }
DI float f_(unsigned u) { return __uint_as_float(u); }
DI unsigned u_(float f) { return __float_as_uint(f); }
#define RED4(x, OP) do { auto r_ = __builtin_amdgcn_permlane32_swap(u_(x), u_(x), false, false); x = OP(f_(r_[0]), f_(r_[1])); \
  r_ = __builtin_amdgcn_permlane16_swap(u_(x), u_(x), false, false); x = OP(f_(r_[0]), f_(r_[1])); } while (0)
DI float addf(float a, float b) { return a + b; }

DI void featmap(f4 p0, f4 p1, v4u& fp, v4u& fm) {
  float mx = fmaxf(fmaxf(fmaxf(p0.x, p0.y), fmaxf(p0.z, p0.w)), fmaxf(fmaxf(p1.x, p1.y), p1.z));
  float mn = fminf(fminf(fminf(p0.x, p0.y), fminf(p0.z, p0.w)), fminf(fminf(p1.x, p1.y), p1.z));
  RED4(mx, fmaxf);
  RED4(mn, fminf);
  const float L = 1.44269504089f;
  const float a = -mx * L, b = mn * L;
  float p[7] = {p0.x, p0.y, p0.z, p0.w, p1.x, p1.y, p1.z}, e[7], g[7];
  float se = 0.f, sg = 0.f;
#pragma unroll
  for (int i = 0; i < 7; i++) {
    e[i] = __builtin_amdgcn_exp2f(fmaf(p[i], L, a));
    g[i] = __builtin_amdgcn_exp2f(fmaf(p[i], -L, b));
    se += e[i];
    sg += g[i];
  }
  RED4(se, addf);
  RED4(sg, addf);
  const float re = __builtin_amdgcn_rcpf(se), rg = __builtin_amdgcn_rcpf(sg);
  fp = v4u{pk2(e[0] * re, e[1] * re), pk2(e[2] * re, e[3] * re), pk2(e[4] * re, e[5] * re), pk2(e[6] * re, 0.f)};
  fm = v4u{pk2(g[0] * rg, g[1] * rg), pk2(g[2] * rg, g[3] * rg), pk2(g[4] * rg, g[5] * rg), pk2(g[6] * rg, 0.f)};
}

DI void mod_issue(v4u rm, v4u rc, int blk, int half, int w, int lane) {
  if (w < 7) {
    const int r0 = half * 224 + w * 32;
    dma4(rc, min(lane, 15) * 4, r0 * 2, L_CST + w * 256);
    const unsigned vo = (lane >> 3) * 5376 + (lane & 7) * 16;
#pragma unroll
    for (int i = 0; i < 4; i++) dma16(rm, vo, (r0 + 8 * i) * 5376 + blk * 128, L_JOB + w * 4096 + i * 1024);
  }
}
DI void mod_partial(int w, int lane) {
  float acc[8];
#pragma unroll
  for (int j = 0; j < 8; j++) acc[j] = 0.f;
  if (w < 7) {
#pragma unroll
    for (int i = 0; i < 4; i++) {
      const float cv = blo(*(const LAS u16*)(L_CST + w * 256 + (8 * i + (lane >> 3)) * 2));
      const float sv = rbf(cv * __builtin_amdgcn_rcpf(1.f + __builtin_amdgcn_exp2f(-1.44269504089f * cv)));
      const v4u v = lr16(L_JOB + w * 4096 + i * 1024 + lane * 16);
#pragma unroll
      for (int j = 0; j < 4; j++) { acc[2 * j] = fmaf(sv, blo(v[j]), acc[2 * j]); acc[2 * j + 1] = fmaf(sv, bhi(v[j]), acc[2 * j + 1]); }
    }
  }
  float t[4], u[2];
#pragma unroll
  for (int j = 0; j < 4; j++) { auto r = __builtin_amdgcn_permlane32_swap(u_(acc[j]), u_(acc[4 + j]), false, false); t[j] = f_(r[0]) + f_(r[1]); }
#pragma unroll
  for (int j = 0; j < 2; j++) { auto r = __builtin_amdgcn_permlane16_swap(u_(t[j]), u_(t[2 + j]), false, false); u[j] = f_(r[0]) + f_(r[1]); }
#pragma unroll
  for (int j = 0; j < 2; j++) u[j] += f_(__builtin_amdgcn_update_dpp(0u, u_(u[j]), 0x128  , 0xf, 0xf, false));
  if (!(lane & 8)) lw8(L_RED + w * 256 + ((lane & 7) * 8 + (lane >> 5) * 4 + ((lane >> 4) & 1) * 2) * 4, v2u{u_(u[0]), u_(u[1])});
}
DI void mod_final(rsrc ws, int blk, int half, unsigned tag, int lane) {
  float s = 0.f;
#pragma unroll
  for (int i = 0; i < 7; i++) s += *(const LAS float*)(L_RED + i * 256 + lane * 4);
  st8(v2u{u_(s), tag}, ws, lane * 8, WS_MOD + (half * 2688 + blk * 64) * 8);
}
DI f2 mod_get(rsrc ws, int col, unsigned tag) {
  v4u a, b;
  _Pragma("nounroll") for (int sp = 0; sp < SPIN; sp++) {
    MEMB();
    a = ld16(ws, col * 8, WS_MOD);
    b = ld16(ws, col * 8, WS_MOD + 2688 * 8);
    if (__all(a.y == tag && a.w == tag && b.y == tag && b.w == tag)) break;
  }
  return f2{rbf(f_(a.x) + f_(b.x)), rbf(f_(a.z) + f_(b.z))};
}
DI float gelu_mul(float g, float u) {
  const float t = g * fmaf(g * g, -0.044715f * 2.3022081986f, -2.3022081986f);
  return g * u * __builtin_amdgcn_rcpf(1.f + __builtin_amdgcn_exp2f(t));
}
}

#define STAMP(i)
#define CODE_LINES 256

__global__ void __launch_bounds__(512, 2)
fused_block(const u16* __restrict__ x, const u16* __restrict__ wqkv, const u16* __restrict__ wphi, const u16* __restrict__ wo,
            const u16* __restrict__ wg, const u16* __restrict__ wu, const u16* __restrict__ wd, const u16* __restrict__ cc,
            const u16* __restrict__ wmod, u16* __restrict__ out, char* __restrict__ wsp) {
  STAMP(0);
  const int tid = threadIdx.x, lane = tid & 63, w = __builtin_amdgcn_readfirstlane(tid >> 6);
  const int bid = blockIdx.x, xcd = bid & 7, idx = bid >> 3, par = xcd & 1;
  {
    u64 pc;
    asm volatile("s_getpc_b64 %0" : "=s"(pc));
    dma4(mkrs((const void*)(pc & ~127ull), CODE_LINES * 128), (lane >> 5) * 32768 + (idx * 8 + w) * 128, 0, L_DUMP);
  }
  const int tg = (xcd >> 1) * 4 + (idx & 3), h = par * 8 + (idx >> 2), hq = h ^ 8;
  const int xb = (xcd + 2) & 7;
  const int g = lane >> 4, l15 = lane & 15, q = (lane >> 2) & 3, p = lane & 3;
  const rsrc ws = __builtin_amdgcn_make_buffer_rsrc((void*)wsp, 0, WS_END, 0x00020000);
  const v4u rws = mkrs(wsp, WS_END);
  const v4u rmod = mkrs(wmod, D * 6 * D * 2), rc = mkrs(cc, D * 2);

  const int e = (tg - 2 * (h & 7)) & 15;
  const int mj = (e - 14) + 2 * h;
  const bool is_prep = e < 14, is_mod = !is_prep && mj < 28;
  if (is_prep) {
    if (w == 0) {
      const v4u rq = mkrs(wqkv, D * 3 * D * 2);
      const unsigned vo = (lane >> 2) * 2688 + min((lane & 3) * 16, 40);
#pragma unroll
      for (int m = 0; m < 3; m++) {
        const int col = m == 0 ? 896 + hq * 56 : (m == 1 ? 1792 + hq * 56 : h * 56);
#pragma unroll
        for (int i = 0; i < 2; i++) dma16(rq, vo, (e * 32 + 16 * i) * 2688 + col, L_JOB + m * 2048 + i * 1024);
      }
    }
  } else if (is_mod) {
    mod_issue(rmod, rc, mj >> 1, mj & 1, w, lane);
  }
  if (w == 2 || w == 3) {
    const v4u rp = mkrs(wphi, 16 * 784 * 2);
    const int hh = w == 2 ? h : hq;
    dma16(rp, lane * 16, hh * 1568, L_PHR + (w - 2) * 2048);
    dma16(rp, min(lane, 33) * 16, hh * 1568 + 1024, L_PHR + (w - 2) * 2048 + 1024);
  }
  {
    const v4u rx = mkrs(x, S * D * 2);
    const unsigned v0 = (lane >> 3) * 896 + (((lane & 7) ^ g) * 16);
    const unsigned v1 = (8 + (lane >> 3)) * 896 + (((lane & 7) ^ (4 + g)) * 16);
    const int so = (tg * 128 + w * 16) * 896, lb = L_X + w * 14336;
#pragma unroll
    for (int l = 0; l < 7; l++) {
      dma16(rx, v0, so + l * 128, lb + l * 2048);
      dma16(rx, v1, so + l * 128, lb + l * 2048 + 1024);
    }
  }
  const int u1 = (e * 16 + h) * 4 + (w - 4);
  const bool late1 = is_prep && w >= 4;
  if (late1) {
    const unsigned lb = L_JOB + w * 4096;
    if (u1 < 784) {
      const int ks = u1 / 28, cb = u1 % 28;
      const unsigned vo = (lane >> 3) * 3584 + (lane & 7) * 16;
      const v4u r0 = mkrs(wg, D * F * 2), r1 = mkrs(wu, D * F * 2);
      const int so = ks * 16 * 3584 + cb * 128;
      dma16(r0, vo, so, lb);
      dma16(r0, vo, so + 8 * 3584, lb + 1024);
      dma16(r1, vo, so, lb + 2048);
      dma16(r1, vo, so + 8 * 3584, lb + 3072);
    } else {
      const int v = u1 - 784, hh = v / 7, cb = v % 7;
      const unsigned vo = (lane >> 3) * 896 + (lane & 7) * 16;
      const v4u r0 = mkrs(wo, D * D * 2);
      const int so = hh * 28 * 896 + cb * 128;
#pragma unroll
      for (int i = 0; i < 3; i++) dma16(r0, vo, so + i * 8 * 896, lb + i * 1024);
      dma16(r0, min(lane >> 3, 3) * 896 + (lane & 7) * 16, so + 24 * 896, lb + 3072);
    }
  }
  unsigned tag = *(const CAS unsigned*)(wsp + WS_EPOCH);
  asm volatile("" : "+s"(tag));
  tag = tag * 1664525u + 1013904223u;
  STAMP(1);

  if (is_prep) {
    if (w == 0) {
      VMW(14);
      const unsigned a0 = L_JOB + (8 * g + q) * 64 + p * 8, a1 = L_JOB + (8 * g + q) * 64 + 32 + (p ^ (p >> 1)) * 8;
#pragma unroll
      for (int m = 0; m < 3; m++)
#pragma unroll
        for (int T = 0; T < 2; T++) {
          const unsigned a = (T ? a1 : a0) + m * 2048;
          st16(trf(a, a + 256), ws, lane * 16, WS_WQKV + ((hq * 3 + m) * 28 + e * 2 + T) * 1024);
        }
      VMW(0);
      if (lane == 0) st4(tag, ws, 0, WS_FLAGW + hq * 128 + e * 4);
    }
  } else if (is_mod) {
    VMW(14);
    mod_partial(w, lane);
    BAR();
    if (w == 7) mod_final(ws, mj >> 1, mj & 1, tag, lane);
  }
  STAMP(2);
  if (late1) VMW(4); else if ((is_prep && w == 0) || (is_mod && w == 7)) VMW(1); else VMW(0);
  STAMP(3);
  st8(v2u{SENT, SENT}, ws, lane * 8, WS_KVP + ((h * 8 + w) * 16 + tg) * 512);
  st16(v4u{SENT, SENT, SENT, SENT}, ws, lane * 16, WS_O + ((tg * 8 + w) * 16 + hq) * 1024);

  float xh[14][8];
  {
    const unsigned a0 = L_X + w * 14336 + l15 * 128 + ((g ^ (l15 >> 1)) * 16);
    const unsigned a1 = L_X + w * 14336 + l15 * 128 + (((4 + g) ^ (l15 >> 1)) * 16);
    float s1 = 0.f, s2 = 0.f;
#pragma unroll
    for (int ks = 0; ks < 14; ks++) {
      const v4u v = lr16(((ks & 1) ? a1 : a0) + (ks >> 1) * 2048);
#pragma unroll
      for (int j = 0; j < 4; j++) {
        s1 = dot2(v[j], 0x3F803F80u, s1);
        s2 = dot2(v[j], v[j], s2);
        xh[ks][2 * j] = blo(v[j]);
        xh[ks][2 * j + 1] = bhi(v[j]);
      }
    }
    RED4(s1, addf);
    RED4(s2, addf);
    const float mu = s1 * (1.f / D);
    const float rs = __builtin_amdgcn_rsqf(fmaxf(s2 * (1.f / D) - mu * mu, 0.f) + 1e-5f);
    const float nb = -mu * rs;
#pragma unroll
    for (int ks = 0; ks < 14; ks++)
#pragma unroll
      for (int j = 0; j < 8; j++) { xh[ks][j] = fmaf(xh[ks][j], rs, nb); PIN(xh[ks][j]); }
  }
  SB();
  if (w == 2 || w == 3) {
#pragma unroll
    for (int T = 0; T < 2; T++) {
      const int f = 7 * (l15 >> 2) + 4 * T + (l15 & 3);
      const bool okf = !(T == 1 && (l15 & 3) == 3);
      unsigned v[8];
#pragma unroll
      for (int j = 0; j < 8; j++) {
        const int d = (j < 4 ? 4 * g + j : 12 + 4 * g + j);
        const unsigned t = *(const LAS u16*)(L_PHR + (w - 2) * 2048 + (d * 28 + f) * 2);
        v[j] = (okf && d < 28) ? t : 0u;
      }
      lw16(L_PHF + ((w - 2) * 2 + T) * 1024 + lane * 16, v4u{v[0] | v[1] << 16, v[2] | v[3] << 16, v[4] | v[5] << 16, v[6] | v[7] << 16});
    }
  }
  if (late1) {
    VMW(2);
    const unsigned lb = L_JOB + w * 4096;
    if (u1 < 784) {
      const int ks = u1 / 28, cb = u1 % 28;
      const unsigned a = lb + (16 * (g & 1) + 8 * (g >> 1) + q) * 128 + p * 8;
#pragma unroll
      for (int s = 0; s < 4; s++) st16(trf(a + s * 32, a + s * 32 + 512), ws, lane * 16, WS_WGU + ((cb * 4 + s) * 28 + ks) * 1024);
    } else {
      const int v = u1 - 784, hh = v / 7, cb = v % 7;
      const unsigned a = lb + (4 * g + q) * 128 + p * 8;
#pragma unroll
      for (int s = 0; s < 4; s++) st16(trf(a + s * 32, a + s * 32 + 2048), ws, lane * 16, WS_WO + ((cb * 4 + s) * 16 + hh) * 1024);
    }
  }
  STAMP(4);
  if (w < 7) {
    const f2 m = mod_get(ws, 2 * tid, tag);
    if (tid < 224) lw8(L_SA + tid * 8, v2u{u_(m.x), u_(m.y)});
    else lw8(L_CA + (tid - 224) * 8, v2u{u_(rbf(1.f + m.x)), u_(rbf(1.f + m.y))});
  } else {
    _Pragma("nounroll") for (int sp = 0; sp < SPIN; sp++) {
      MEMB();
      const unsigned f = ld4(ws, min(lane, 13) * 4, WS_FLAGW + h * 128);
      if (__all(f == tag)) break;
    }
  }
  BAR();
  STAMP(5);
  if (w < 7) {
#pragma unroll
    for (int m = 0; m < 3; m++)
#pragma unroll
      for (int i = 0; i < 4; i++) dma16c(rws, lane * 16, WS_WQKV + (h * 84 + m * 28 + w * 4 + i) * 1024, L_W + (m * 28 + w * 4 + i) * 1024);
  }
  const bool mod2 = bid < 56;
  const int u2 = (bid - 56) * 2 + w;
  const bool late2 = !mod2 && w < 2 && u2 < 392;
  if (mod2) {
    mod_issue(rmod, rc, 14 + (bid >> 1), bid & 1, w, lane);
  } else if (late2) {
    const int kp = u2 / 7, cb = u2 % 7;
    const unsigned vo = (lane >> 3) * 896 + (lane & 7) * 16;
    const v4u r0 = mkrs(wd, F * D * 2);
#pragma unroll
    for (int i = 0; i < 4; i++) dma16(r0, vo, (kp * 32 + i * 8) * 896 + cb * 128, L_JOB + w * 4096 + i * 1024);
  }
  v4u hf[14];
#pragma unroll
  for (int ks = 0; ks < 14; ks++) {
    const f4 c0 = lrf4(L_CA + ks * 128 + g * 32), c1 = lrf4(L_CA + ks * 128 + g * 32 + 16);
    const f4 s0 = lrf4(L_SA + ks * 128 + g * 32), s1 = lrf4(L_SA + ks * 128 + g * 32 + 16);
    hf[ks] = v4u{pk2(fmaf(xh[ks][0], c0.x, s0.x), fmaf(xh[ks][1], c0.y, s0.y)), pk2(fmaf(xh[ks][2], c0.z, s0.z), fmaf(xh[ks][3], c0.w, s0.w)),
                 pk2(fmaf(xh[ks][4], c1.x, s1.x), fmaf(xh[ks][5], c1.y, s1.y)), pk2(fmaf(xh[ks][6], c1.z, s1.z), fmaf(xh[ks][7], c1.w, s1.w))};
    PIN(hf[ks]);
  }
  SB();
  if (w == 7) VMW(0); else if (mod2) VMW(13); else if (late2) VMW(12); else VMW(8);
  BAR();
  STAMP(6);
  if (tid == 448) st4(tag, ws, 0, WS_FLAGG + bid * 4);
  const int t32 = xb * 8 + (idx & 7), cq = idx >> 3, ct = cq * 7 + w;
  const int jj = idx >> 2, j = par * 7 + jj;
  const bool ffn = jj < 7;
  const int v7 = w < 7 ? lane * 16 : OFF, vf = ffn ? lane * 16 : OFF;
  {
    const v4u sv = {SENT, SENT, SENT, SENT};
    st16(sv, ws, v7, WS_Y + (t32 * 28 + ct) * 1024);
    st16(sv, ws, (w < 7 || lane < 16) ? lane * 16 : OFF, w < 7 ? WS_X1 + (t32 * 28 + ct) * 1024 : WS_ST + t32 * 1024 + cq * 256);
#pragma unroll
    for (int t = 0; t < 4; t++) st16(sv, ws, vf, WS_ACT + (((tg * 4 + t) * 8 + w) * 14 + j) * 1024);
  }
  const unsigned wa = L_W + lane * 16;
  f4 z4 = {0.f, 0.f, 0.f, 0.f};
  f4 k0 = z4, k1 = z4;
#pragma unroll
  for (int ks = 0; ks < 14; ks++) {
    k0 = mfma16(lr16(wa + (ks * 2) * 1024), hf[ks], k0);
    k1 = mfma16(lr16(wa + (ks * 2 + 1) * 1024), hf[ks], k1);
  }
  PIN(k0); PIN(k1);
  SB();
  if (mod2) VMW(11); else if (late2) VMW(10); else VMW(6);
  BAR();
  STAMP(7);
  v4u gfl = ld16(ws, lane * 16, WS_FLAGG);
  {
    const v4u kb = {pk2(k0.x, k0.y), pk2(k0.z, k0.w), pk2(k1.x, k1.y), pk2(k1.z, k1.w)};
    const f4 p0 = mfma16(lr16(L_PHF + lane * 16), kb, z4), p1 = mfma16(lr16(L_PHF + 1024 + lane * 16), kb, z4);
    f4 v0 = z4, v1 = z4;
#pragma unroll
    for (int ks = 0; ks < 14; ks++) {
      v0 = mfma16(lr16(wa + (28 + ks * 2) * 1024), hf[ks], v0);
      v1 = mfma16(lr16(wa + (28 + ks * 2 + 1) * 1024), hf[ks], v1);
    }
    v4u fp, fm;
    featmap(p0, p1, fp, fm);
    const unsigned a = L_PV + (w * 16 + l15) * 200 + g * 8;
    lw8(a, v2u{fp.x, fp.y});
    lw8(a + 32, v2u{fp.z, fp.w});
    lw8(a + 64, v2u{fm.x, fm.y});
    lw8(a + 96, v2u{fm.z, fm.w});
    lw8(a + 128, v2u{pk2(v0.x, v0.y), pk2(v0.z, v0.w)});
    lw8(a + 160, g == 3 ? v2u{0x3F803F80u, 0x3F803F80u} : v2u{pk2(v1.x, v1.y), pk2(v1.z, v1.w)});
  }
  _Pragma("nounroll") for (int sp = 0; sp < SPIN; sp++) {
    if (__all(gfl.x == tag && gfl.y == tag && gfl.z == tag && gfl.w == tag)) break;
    MEMB();
    gfl = ld16(ws, lane * 16, WS_FLAGG);
  }
  MEMB();
  v4u wof[16];
  {
    const int so = WS_WO + ct * 16384;
#pragma unroll
    for (int i = 0; i < 16; i++) wof[i] = ld16(ws, v7 + (i & 3) * 1024, so + (i >> 2) * 4096);
  }
  SB();
  BAR();
  STAMP(8);
  {
    const unsigned a = L_PV + (8 * g + q) * 200 + p * 8;
    const unsigned aa = a + (w >> 1) * 32, ab = a + 128 + (w & 1) * 32;
    f4 kv = z4;
#pragma unroll
    for (int kk = 0; kk < 4; kk++) kv = mfma16(trf(aa + kk * 6400, aa + kk * 6400 + 800), trf(ab + kk * 6400, ab + kk * 6400 + 800), kv);
    st8(v2u{nz(pk2(kv.x, kv.y)), nz(pk2(kv.z, kv.w))}, ws, lane * 8, WS_KVP + ((h * 8 + w) * 16 + tg) * 512);
  }
  SB();
  STAMP(9);
  if (mod2) {
    mod_partial(w, lane);
    BAR();
    if (w == 7) mod_final(ws, 14 + (bid >> 1), bid & 1, tag, lane);
  } else if (late2) {
    const int kp = u2 / 7, cb = u2 % 7;
    const unsigned a = L_JOB + w * 4096 + (4 * (g >> 1) + q) * 128 + (g & 1) * 32 + p * 8;
#pragma unroll
    for (int s = 0; s < 4; s++) {
      const int kst = kp * 2 + (s >> 1);
      const unsigned b = a + (s >> 1) * 2048 + (s & 1) * 64;
      st16(trf(b, b + 1024), ws, lane * 16, WS_WD + (((cb * 8 + (kst & 7)) * 14 + (kst >> 3)) * 2 + (s & 1)) * 1024);
    }
  }
  v4u qp, qm;
  v4u kvr[8];
  const int kvo = WS_KVP + (hq * 8 + w) * 8192;
  {
    f4 q0 = z4, q1 = z4;
#pragma unroll
    for (int ks = 0; ks < 14; ks++) {
      q0 = mfma16(lr16(wa + (56 + ks * 2) * 1024), hf[ks], q0);
      q1 = mfma16(lr16(wa + (56 + ks * 2 + 1) * 1024), hf[ks], q1);
    }
    SB();
#pragma unroll
    for (int i = 0; i < 8; i++) kvr[i] = ld16(ws, lane * 16 + (i & 3) * 1024, kvo + (i >> 2) * 4096);
    SB();
    const v4u qb = {pk2(q0.x, q0.y), pk2(q0.z, q0.w), pk2(q1.x, q1.y), pk2(q1.z, q1.w)};
    featmap(mfma16(lr16(L_PHF + 2048 + lane * 16), qb, z4), mfma16(lr16(L_PHF + 3072 + lane * 16), qb, z4), qp, qm);
    PIN(qp); PIN(qm);
  }
  SB();
  STAMP(10);
  _Pragma("nounroll") for (int sp = 0; sp < SPIN; sp++) {
    unsigned m = 0;
#pragma unroll
    for (int i = 0; i < 8; i++) m = mx4(kvr[i], m);
    if (__all(m != SENT)) break;
    MEMB();
#pragma unroll
    for (int i = 0; i < 8; i++) kvr[i] = ld16(ws, lane * 16 + (i & 3) * 1024, kvo + (i >> 2) * 4096);
  }
  MEMB();
  constexpr int NW2 = 8;
  v4u wgf[28];
  const int wgo = WS_WGU + (j * 8 + w) * 28 * 1024;
#pragma unroll
  for (int i = 0; i < NW2; i++) wgf[i] = ld16(ws, vf + (i & 3) * 1024, wgo + (i >> 2) * 4096);
  const v4u xres = *(const GAS v4u*)(x + (t32 * 32 + (lane & 31)) * D + min(ct, 27) * 16 + (lane >> 5) * 8);
  SB();
  {
    float s[8];
#pragma unroll
    for (int c = 0; c < 8; c++) s[c] = 0.f;
#pragma unroll
    for (int i = 0; i < 8; i++)
#pragma unroll
      for (int c = 0; c < 4; c++) { s[2 * c] += blo(kvr[i][c]); s[2 * c + 1] += bhi(kvr[i][c]); }
    float t[4];
#pragma unroll
    for (int c = 0; c < 4; c++) { auto r = __builtin_amdgcn_permlane32_swap(u_(s[c]), u_(s[4 + c]), false, false); t[c] = f_(r[0]) + f_(r[1]); }
    lw8(L_KVS + (w >> 2) * 2048 + (w & 1) * 1024 + (2 * (lane & 31) + (lane >> 5)) * 16 + ((w >> 1) & 1) * 8, v2u{pk2(t[0], t[1]), pk2(t[2], t[3])});
  }
  BAR();
  STAMP(11);
  if (tid == 448) st4(tag, ws, 0, WS_FLAGH + bid * 4);
  {
    f4 o0 = mfma16(lr16(L_KVS + lane * 16), qp, z4), o1 = mfma16(lr16(L_KVS + 1024 + lane * 16), qp, z4);
    o0 = mfma16(lr16(L_KVS + 2048 + lane * 16), qm, o0);
    o1 = mfma16(lr16(L_KVS + 3072 + lane * 16), qm, o1);
    auto r = __builtin_amdgcn_permlane32_swap(u_(o1.x), u_(o1.x), false, false);
    r = __builtin_amdgcn_permlane16_swap(r[1], r[1], false, false);
    const float rd = __builtin_amdgcn_rcpf(f_(r[1]) + 1e-6f);
    v4u o = {nz(pk2(o0.x * rd, o0.y * rd)), nz(pk2(o0.z * rd, o0.w * rd)), nz(pk2(o1.x * rd, o1.y * rd)), nz(pk2(o1.z * rd, o1.w * rd))};
    if (g == 3) { o.z = 0; o.w = 0; }
    st16(o, ws, lane * 16, WS_O + ((tg * 8 + w) * 16 + hq) * 1024);
  }
  {
    const int col = w < 7 ? 1344 + 2 * tid : 896 + min(cq * 112 + 2 * lane, 446);
    const f2 m = mod_get(ws, col, tag);
    if (w == 7) lw8(L_GA + lane * 8, v2u{u_(m.x), u_(m.y)});
    else if (tid < 224) *(LAS unsigned*)(L_MB + 896 + tid * 4) = pk2(m.x, m.y);
    else {
      const unsigned c1 = pk2(1.f + m.x, 1.f + m.y);
      *(LAS unsigned*)(L_MB + (tid - 224) * 4) = c1;
      lw8(L_CBF + (tid - 224) * 8, v2u{c1 << 16, c1 & 0xffff0000u});
    }
  }
  VMW(0);
  STAMP(12);
  {
    v4u ob[4], hfl;
    _Pragma("nounroll") for (int sp = 0; sp < SPIN; sp++) {
      MEMB();
#pragma unroll
      for (int i = 0; i < 4; i++) ob[i] = ld16(ws, lane * 16 + i * 1024, WS_O + t32 * 32768 + w * 4096);
      unsigned m = 0;
#pragma unroll
      for (int i = 0; i < 4; i++) m = mx4(ob[i], m);
      if (__all(m != SENT)) break;
    }
    MEMB();
    hfl = ld16(ws, lane * 16, WS_FLAGH);
#pragma unroll
    for (int i = NW2; i < 28; i++) wgf[i] = ld16(ws, vf + (i & 3) * 1024, wgo + (i >> 2) * 4096);
    SB();
#pragma unroll
    for (int i = 0; i < 4; i++) lw16(L_OB + w * 4096 + i * 1024 + lane * 16, ob[i]);
    BAR();
    STAMP(13);
    if (bid == 0 && tid == 0) st4(tag, ws, 0, WS_EPOCH);
    if (w < 7) {
      f4 a0 = z4, a1 = z4;
#pragma unroll
      for (int i = 0; i < 16; i++) {
        a0 = mfma16(wof[i], lr16(L_OB + i * 1024 + lane * 16), a0);
        a1 = mfma16(wof[i], lr16(L_OB + (16 + i) * 1024 + lane * 16), a1);
      }
      float v[8];
#pragma unroll
      for (int c = 0; c < 4; c++) { auto r = __builtin_amdgcn_permlane16_swap(u_(a0[c]), u_(a1[c]), false, false); v[c] = f_(r[0]); v[4 + c] = f_(r[1]); }
      const int c0 = w * 16 + (lane >> 5) * 8;
      const f4 g0 = lrf4(L_GA + c0 * 4), g1 = lrf4(L_GA + c0 * 4 + 16);
      const f4 b0 = lrf4(L_CBF + (cq * 112 + c0) * 4), b1 = lrf4(L_CBF + (cq * 112 + c0) * 4 + 16);
      float s1 = 0.f, s2 = 0.f;
#pragma unroll
      for (int c = 0; c < 8; c++) {
        const float xv = (c & 1) ? bhi(xres[c >> 1]) : blo(xres[c >> 1]);
        v[c] = fmaf(c < 4 ? g0[c & 3] : g1[c & 3], v[c], xv);
        s1 += v[c];
        s2 = fmaf(v[c], v[c], s2);
      }
      st16(v4u{nz(pk2(v[0] * b0.x, v[1] * b0.y)), nz(pk2(v[2] * b0.z, v[3] * b0.w)), nz(pk2(v[4] * b1.x, v[5] * b1.y)), nz(pk2(v[6] * b1.z, v[7] * b1.w))},
           ws, lane * 16, WS_Y + (t32 * 28 + ct) * 1024);
      auto r = __builtin_amdgcn_permlane32_swap(u_(s1), u_(s2), false, false);
      *(LAS float*)(L_STP + w * 256 + (lane & 31) * 8 + (lane >> 5) * 4) = f_(r[0]) + f_(r[1]);
      st16(v4u{nz(pk2(v[0], v[1])), nz(pk2(v[2], v[3])), nz(pk2(v[4], v[5])), nz(pk2(v[6], v[7]))}, ws, lane * 16, WS_X1 + (t32 * 28 + ct) * 1024);
    }
    BAR();
    if (w == 7) {
      float s = 0.f;
#pragma unroll
      for (int i = 0; i < 7; i++) s += *(const LAS float*)(L_STP + i * 256 + lane * 4);
      st4(nz(u_(s)), ws, lane * 4, WS_ST + t32 * 1024 + cq * 256);
    }
    STAMP(14);
    _Pragma("nounroll") for (int sp = 0; sp < SPIN; sp++) {
      if (__all(hfl.x == tag && hfl.y == tag && hfl.z == tag && hfl.w == tag)) break;
      MEMB();
      hfl = ld16(ws, lane * 16, WS_FLAGH);
    }
    MEMB();
  }
  if (ffn) {
    f16v z16;
#pragma unroll
    for (int i = 0; i < 16; i++) z16[i] = 0.f;
    v4u fixa;
    {
      f16v ub = z16;
      const unsigned a = L_MB + (lane & 1) * 896 + (lane >> 5) * 16;
#pragma unroll
      for (int ks = 0; ks < 28; ks++) ub = mfma32(lr16(a + ks * 32), wgf[ks], ub);
      const float uh = rbf(ub[0]), bh = rbf(ub[1]);
      fixa = v4u{pk2(uh, uh), pk2(ub[0] - uh, bh), pk2(bh, ub[1] - bh), 0u};
    }
    if (w == 7) {
      const f2 m = mod_get(ws, 2240 + min(jj * 64 + 2 * lane, 446), tag);
      lw8(L_GB + lane * 8, v2u{u_(m.x), u_(m.y)});
    }
    VMW(0);
    STAMP(15);
    const int tx = 2 * (j & 1);
    v4u yb[4];
    auto y_issue = [&](int t) {
      const int tt = tg * 4 + (t ^ tx);
      if (w < 7) {
#pragma unroll
        for (int i = 0; i < 4; i++) yb[i] = ld16(ws, lane * 16 + i * 1024, WS_Y + (tt * 28 + w * 4) * 1024);
      } else {
        const v2u a = ld8(ws, (lane & 31) * 8 + (lane >> 5) * 512, WS_ST + tt * 1024), b = ld8(ws, (lane & 31) * 8 + (lane >> 5) * 512 + 256, WS_ST + tt * 1024);
        yb[0] = v4u{a.x, a.y, b.x, b.y};
        yb[1] = yb[2] = yb[3] = v4u{0, 0, 0, 0};
      }
    };
    auto y_commit = [&](int t) {
      _Pragma("nounroll") for (int sp = 0; sp < SPIN; sp++) {
        unsigned m = 0;
#pragma unroll
        for (int i = 0; i < 4; i++) m = mx4(yb[i], m);
        if (__all(m != SENT)) break;
        MEMB();
        y_issue(t);
      }
      MEMB();
      const unsigned sl = L_YR + (t & 1) * YSLOT + lane * 16;
      if (w < 7) {
#pragma unroll
        for (int i = 0; i < 4; i++) lw16(sl + (w * 4 + i) * 1024, yb[i]);
      } else {
        float s1 = f_(yb[0].x) + f_(yb[0].z), s2 = f_(yb[0].y) + f_(yb[0].w);
        auto r = __builtin_amdgcn_permlane32_swap(u_(s1), u_(s1), false, false);
        s1 = f_(r[0]) + f_(r[1]);
        r = __builtin_amdgcn_permlane32_swap(u_(s2), u_(s2), false, false);
        s2 = f_(r[0]) + f_(r[1]);
        const float mu = s1 * (1.f / D);
        const float var = fmaxf(s2 * (1.f / D) - mu * mu, 0.f) + 1e-5f;
        const float rs = __builtin_amdgcn_rsqf(var), sg = var * rs;
        const float mh = rbf(mu), sh = rbf(sg);
        v4u fx = {pk2(-mh, mh - mu), pk2(-mh, sh), pk2(sg - sh, sh), u_(rs)};
        if (lane >= 32) { fx.x = 0; fx.y = 0; fx.z = 0; }
        lw16(sl + 28 * 1024, fx);
      }
    };
    y_issue(0);
    y_commit(0);
    y_issue(1);
    SB();
    BAR();
    STAMP(16);
#pragma unroll
    for (int t = 0; t < 4; t++) {
      const unsigned sl = L_YR + (t & 1) * YSLOT + lane * 16;
      f16v acc = z16;
#pragma unroll
      for (int ks = 0; ks < 14; ks++) acc = mfma32(wgf[ks], lr16(sl + ks * 1024), acc);
      if (t < 3) {
        SB();
        y_commit(t + 1);
        if (t < 2) y_issue(t + 2);
        SB();
      }
#pragma unroll
      for (int ks = 14; ks < 28; ks++) acc = mfma32(wgf[ks], lr16(sl + ks * 1024), acc);
      v4u fx = lr16(sl + 28 * 1024);
      const float rs = f_(fx.w);
      fx.w = 0;
      acc = mfma32(fixa, fx, acc);
      BAR();
      float a[8];
#pragma unroll
      for (int i = 0; i < 8; i++) a[i] = gelu_mul(acc[i] * rs, acc[8 + i] * rs);
      st16(v4u{nz(pk2(a[0], a[1])), nz(pk2(a[2], a[3])), nz(pk2(a[4], a[5])), nz(pk2(a[6], a[7]))}, ws, lane * 16,
           WS_ACT + (((tg * 4 + (t ^ tx)) * 8 + w) * 14 + j) * 1024);
    }
    STAMP(17);
    const int t64 = xb * 4 + (idx & 3), ue = t64 & 1;
    const int wdo = WS_WD + (jj * 8 + w) * 28 * 1024;
    const int ao = WS_ACT + (t64 * 16 + w) * 14 * 1024;
    f16v dacc[2][2] = {{z16, z16}, {z16, z16}};
    const v4u x1v0 = ld16(ws, ((tid >> 8) * 28 + ((tid & 7) >> 1)) * 1024 + (((tid >> 3) & 31) + (tid & 1) * 32) * 16, WS_X1 + (t64 * 56 + jj * 4) * 1024);
#pragma unroll
    for (int hf2 = 0; hf2 < 2; hf2++) {
      const int jp = hf2 ^ ue;
      v4u A[14], B[2][7];
#pragma unroll
      for (int i = 0; i < 7; i++) {
        A[2 * i] = ld16(ws, lane * 16, wdo + (2 * i + jp) * 2048);
        A[2 * i + 1] = ld16(ws, lane * 16 + 1024, wdo + (2 * i + jp) * 2048);
      }
#pragma unroll
      for (int a = 0; a < 2; a++)
#pragma unroll
        for (int i = 0; i < 7; i++) B[a][i] = ld16(ws, lane * 16, ao + a * 114688 + (2 * i + jp) * 1024);
      SB();
#pragma unroll
      for (int a = 0; a < 2; a++) {
        _Pragma("nounroll") for (int sp = 0; sp < SPIN; sp++) {
          unsigned m = 0;
#pragma unroll
          for (int i = 0; i < 7; i++) m = mx4(B[a][i], m);
          if (__all(m != SENT)) break;
          MEMB();
#pragma unroll
          for (int i = 0; i < 7; i++) B[a][i] = ld16(ws, lane * 16, ao + a * 114688 + (2 * i + jp) * 1024);
        }
        MEMB();
#pragma unroll
        for (int i = 0; i < 7; i++) {
          dacc[a][0] = mfma32(A[2 * i], B[a][i], dacc[a][0]);
          dacc[a][1] = mfma32(A[2 * i + 1], B[a][i], dacc[a][1]);
        }
      }
    }
    STAMP(18);
    {
      const int tk = lane & 31, hh = lane >> 5;
#pragma unroll
      for (int a = 0; a < 2; a++)
#pragma unroll
        for (int c = 0; c < 2; c++)
#pragma unroll
          for (int r = 0; r < 4; r++)
            lw16(L_RD + (w * 2 + a) * 8192 + tk * 256 + (((8 * c + 2 * r + hh) ^ (tk & 15)) * 16),
                 v4u{u_(dacc[a][c][4 * r]), u_(dacc[a][c][4 * r + 1]), u_(dacc[a][c][4 * r + 2]), u_(dacc[a][c][4 * r + 3])});
    }
    v4u x1v = x1v0;
    _Pragma("nounroll") for (int sp = 0; sp < SPIN; sp++) {
      if (__all(mx4(x1v, 0) != SENT)) break;
      MEMB();
      x1v = ld16(ws, ((tid >> 8) * 28 + ((tid & 7) >> 1)) * 1024 + (((tid >> 3) & 31) + (tid & 1) * 32) * 16, WS_X1 + (t64 * 56 + jj * 4) * 1024);
    }
    BAR();
    {
      const int tk = (tid >> 3) & 31, a = tid >> 8, ch = tid & 7;
      const unsigned b0 = L_RD + a * 8192 + tk * 256 + (((2 * ch) ^ (tk & 15)) * 16), b1 = L_RD + a * 8192 + tk * 256 + (((2 * ch + 1) ^ (tk & 15)) * 16);
      f4 s0 = z4, s1 = z4;
#pragma unroll
      for (int i = 0; i < 8; i++) { s0 += lrf4(b0 + i * 16384); s1 += lrf4(b1 + i * 16384); }
      const f4 g0 = lrf4(L_GB + ch * 32), g1 = lrf4(L_GB + ch * 32 + 16);
      const v4u o = {pk2(fmaf(g0.x, s0.x, blo(x1v.x)), fmaf(g0.y, s0.y, bhi(x1v.x))), pk2(fmaf(g0.z, s0.z, blo(x1v.y)), fmaf(g0.w, s0.w, bhi(x1v.y))),
                     pk2(fmaf(g1.x, s1.x, blo(x1v.z)), fmaf(g1.y, s1.y, bhi(x1v.z))), pk2(fmaf(g1.z, s1.z, blo(x1v.w)), fmaf(g1.w, s1.w, bhi(x1v.w)))};
      *(GAS v4u*)(out + (t64 * 64 + (tid >> 3)) * D + jj * 64 + ch * 8) = o;
    }
  }
}

__global__ void k_pre(unsigned long long* rec){ if(threadIdx.x==0) rec[0]=__builtin_amdgcn_s_memrealtime(); }
__global__ void k_amp(const unsigned long long* rec, const unsigned long long* prer){
  unsigned long long ts=__builtin_amdgcn_s_memrealtime();
  __shared__ unsigned long long sm[2][256];
  unsigned long long t0=~0ull, mx=0;
  for(int i=threadIdx.x;i<2048;i+=256){ unsigned long long a=rec[i*32], b=rec[i*32+19]; t0=a<t0?a:t0; mx=b>mx?b:mx; }
  sm[0][threadIdx.x]=t0; sm[1][threadIdx.x]=mx;
  __syncthreads();
  if(threadIdx.x==0){
    t0=~0ull; mx=0;
    for(int i=0;i<256;i++){ t0=sm[0][i]<t0?sm[0][i]:t0; mx=sm[1][i]>mx?sm[1][i]:mx; }
    unsigned long long tau = PK==0 ? (t0-prer[0])*100 : (PK==1 ? (ts-mx)*100 : 0);
    if(tau>1000000) tau=1000000;
    unsigned long long s=__builtin_amdgcn_s_memrealtime(); while(__builtin_amdgcn_s_memrealtime()<s+tau) __builtin_amdgcn_s_sleep(4);
  }
}

extern "C" void kernel_launch(void* const* d_in, const int* in_sizes, int n_in, void* d_out, int out_size, void* d_ws, size_t ws_size,
                              hipStream_t stream) {
  (void)hipFuncSetAttribute((const void*)fused_block, hipFuncAttributeMaxDynamicSharedMemorySize, L_TOTAL);
  k_pre<<<256,64,0,stream>>>((unsigned long long*)((char*)d_ws+(40<<20)));
  fused_block<<<256, 512, L_TOTAL, stream>>>((const u16*)d_in[0], (const u16*)d_in[1], (const u16*)d_in[2], (const u16*)d_in[3], (const u16*)d_in[5],
                                             (const u16*)d_in[6], (const u16*)d_in[7], (const u16*)d_in[8], (const u16*)d_in[9], (u16*)d_out, (char*)d_ws);
}
```

```cpp
#include <hip/hip_runtime.h>
#include <hip/hip_bf16.h>

namespace {
constexpr int WS_EPOCH = 0;
constexpr int WS_MOD = 4096;
constexpr int WS_FLAGW = 49152;
constexpr int WS_FLAGH1 = 81920;
constexpr int WS_FLAGG1 = 114688;
constexpr int WS_FLAGX1 = 147456;
constexpr int WS_FLAGK1 = 180224;
constexpr int WS_WQKV = 212992;
constexpr int WS_WO = 1589248;
constexpr int WS_WGU = 2048000;
constexpr int WS_WD = 5259264;
constexpr int WS_KVP = 6864896;
constexpr int WS_O = 7913472;
constexpr int WS_Y = 10010624;
constexpr int WS_X1 = 11845632;
constexpr int WS_ST = 13680640;
constexpr int WS_ACT = 13746176;
constexpr int WS_END = 21086208;
constexpr int WS_DBG = 21086208;
constexpr int L_TOTAL = 163840;
struct Desc { const void* p; unsigned bytes; unsigned fmt; };
struct Args { Desc d[11]; };
}

__global__ void __launch_bounds__(512, 2) fused_block(Args a) {
#if defined(__HIP_DEVICE_COMPILE__)
  const unsigned tid = threadIdx.x, bid = blockIdx.x;
  const unsigned long long ka = (unsigned long long)__builtin_amdgcn_kernarg_segment_ptr();
  asm volatile(
    "s_load_dwordx16 s[8:23], s[0:1], 0x0\n"
    "s_load_dwordx16 s[24:39], s[0:1], 0x40\n"
    "s_load_dwordx8 s[40:47], s[0:1], 0x80\n"
    "s_load_dwordx4 s[48:51], s[0:1], 0xa0\n"
    "v_readfirstlane_b32 s52, v0\n"
    "s_getpc_b64 s[4:5]\n"
    "s_and_b32 s3, s2, 0xf8\n"
    "s_lshr_b32 s52, s52, 6\n"
    "s_and_b32 s4, s4, 0xffffff80\n"
    "s_add_u32 s3, s3, s52\n"
    "s_mov_b32 s6, 28416\n"
    "s_lshl_b32 s3, s3, 7\n"
    "s_mov_b32 s7, 0x20000\n"
    "v_mov_b32 v8, s3\n"
    "buffer_load_dword v255, v8, s[4:7], 0 offen\n"
    "v_and_b32 v1, 63, v0\n"
    "s_lshr_b32 s54, s2, 3\n"
    "s_lshr_b32 s59, s2, 5\n"
    "s_and_b32 s3, s2, 6\n"
    "s_and_b32 s4, s54, 3\n"
    "s_lshl1_add_u32 s55, s3, s4\n"
    "v_lshlrev_b32 v2, 4, v1\n"
    "s_and_b32 s3, s2, 1\n"
    "s_lshl3_add_u32 s56, s3, s59\n"
    "s_xor_b32 s57, s56, 8\n"
    "s_and_b32 s4, s59, 4\n"
    "s_lshl_b32 s4, s4, 1\n"
    "s_xor_b32 s58, s55, s4\n"
    "v_lshrrev_b32 v4, 4, v1\n"
    "v_lshrrev_b32 v12, 3, v1\n"
    "s_lshl1_add_u32 s77, s56, s58\n"
    "s_sub_u32 s77, s77, 14\n"
    "v_mul_u32_u24 v12, 896, v12\n"
    "s_cmp_lt_u32 s52, 4\n"
    "s_cbranch_scc0 .Lfb_L1\n"
    "s_cmp_lt_u32 s58, 14\n"
    "s_cbranch_scc0 .Lfb_L2\n"
    "s_cmp_lt_u32 s52, 3\n"
    "s_cbranch_scc0 .Lfb_L3\n"
    "v_lshrrev_b32 v8, 2, v1\n"
    "v_and_b32 v9, 48, v2\n"
    "v_mul_u32_u24 v8, 2688, v8\n"
    "v_min_u32 v9, 40, v9\n"
    "s_mul_i32 s3, s58, 86016\n"
    "v_add_u32 v8, v8, v9\n"
    "s_lshl4_add_u32 s4, s52, s57\n"
    "s_add_u32 s4, s4, 16\n"
    "s_cmp_lt_u32 s52, 2\n"
    "s_cselect_b32 s4, s4, s56\n"
    "s_mul_i32 s4, s4, 56\n"
    "v_add_u32 v9, 41984, v8\n"
    "s_lshl_b32 s5, s52, 11\n"
    "s_add_u32 m0, s5, 131072\n"
    "s_add_u32 s4, s4, s3\n"
    "s_waitcnt lgkmcnt(0)\n"
    "buffer_load_dwordx4 v8, s[16:19], s4 offen lds\n"
    "buffer_load_dwordx4 v9, s[16:19], s4 offen offset:1024 lds\n"
    "s_branch .Lfb_L4\n"
    ".Lfb_L3:\n"
    "s_cmp_lt_u32 s58, 7\n"
    "s_cbranch_scc0 .Lfb_L5\n"
    "v_and_b32 v9, 0x70, v2\n"
    "v_add_u32 v8, v12, v9\n"
    "v_min_u32 v13, 2688, v12\n"
    "v_add_u32 v9, v13, v9\n"
    "s_mul_i32 s3, s56, 25088\n"
    "s_lshl_b32 s4, s58, 7\n"
    "s_add_u32 s3, s3, s4\n"
    "s_mov_b32 m0, 137216\n"
    "s_waitcnt lgkmcnt(0)\n"
    "buffer_load_dwordx4 v8, s[32:35], s3 offen lds\n"
    "s_add_u32 s3, s3, 6144\n"
    "buffer_load_dwordx4 v8, s[32:35], s3 offen offset:1024 lds\n"
    "s_add_u32 s3, s3, 6144\n"
    "buffer_load_dwordx4 v8, s[32:35], s3 offen offset:2048 lds\n"
    "s_add_u32 s3, s3, 6144\n"
    "buffer_load_dwordx4 v9, s[32:35], s3 offen offset:3072 lds\n"
    ".Lfb_L5:\n"
    ".Lfb_L4:\n"
    "s_branch .Lfb_L6\n"
    ".Lfb_L2:\n"
    "s_cmp_lt_u32 s77, 28\n"
    "s_cbranch_scc0 .Lfb_L7\n"
    "s_and_b32 s3, s77, 1\n"
    "s_lshl2_add_u32 s3, s3, s52\n"
    "s_mul_i32 s3, s3, 56\n"
    "s_mul_i32 s4, s3, 5376\n"
    "s_lshr_b32 s5, s77, 1\n"
    "s_lshl_b32 s5, s5, 7\n"
    "s_add_u32 s4, s4, s5\n"
    "v_lshrrev_b32 v8, 3, v1\n"
    "v_min_u32 v13, 55, v1\n"
    "v_mul_u32_u24 v8, 5376, v8\n"
    "v_and_b32 v9, 0x70, v2\n"
    "v_lshlrev_b32 v13, 1, v13\n"
    "v_add_u32 v8, v8, v9\n"
    "s_lshl_b32 s3, s3, 1\n"
    "s_waitcnt lgkmcnt(0)\n"
    "buffer_load_ushort v253, v13, s[24:27], s3 offen\n"
    "buffer_load_dwordx4 v[16:19], v8, s[20:23], s4 offen\n"
    "s_add_u32 s4, s4, 43008\n"
    "buffer_load_dwordx4 v[20:23], v8, s[20:23], s4 offen\n"
    "s_add_u32 s4, s4, 43008\n"
    "buffer_load_dwordx4 v[24:27], v8, s[20:23], s4 offen\n"
    "s_add_u32 s4, s4, 43008\n"
    "buffer_load_dwordx4 v[28:31], v8, s[20:23], s4 offen\n"
    "s_add_u32 s4, s4, 43008\n"
    "buffer_load_dwordx4 v[32:35], v8, s[20:23], s4 offen\n"
    "s_add_u32 s4, s4, 43008\n"
    "buffer_load_dwordx4 v[36:39], v8, s[20:23], s4 offen\n"
    "s_add_u32 s4, s4, 43008\n"
    "buffer_load_dwordx4 v[40:43], v8, s[20:23], s4 offen\n"
    ".Lfb_L7:\n"
    ".Lfb_L6:\n"
    "s_cmp_lt_u32 s52, 2\n"
    "s_cbranch_scc0 .Lfb_L8\n"
    "s_cmp_eq_u32 s52, 0\n"
    "s_cselect_b32 s3, s56, s57\n"
    "s_mul_i32 s3, s3, 1568\n"
    "s_lshl_b32 s4, s52, 11\n"
    "s_add_u32 m0, s4, 150528\n"
    "v_min_u32 v8, 528, v2\n"
    "s_waitcnt lgkmcnt(0)\n"
    "buffer_load_dwordx4 v2, s[28:31], s3 offen lds\n"
    "buffer_load_dwordx4 v8, s[28:31], s3 offen offset:1024 lds\n"
    ".Lfb_L8:\n"
    "s_waitcnt lgkmcnt(0)\n"
    "s_load_dword s53, s[8:9], 0x0\n"
    "s_barrier\n"
    "v_and_b32 v13, 7, v1\n"
    "v_xor_b32 v10, v13, v4\n"
    "v_xor_b32 v11, 4, v10\n"
    "v_lshl_add_u32 v10, v10, 4, v12\n"
    "v_lshl_add_u32 v11, v11, 4, v12\n"
    "v_add_u32 v11, 6144, v11\n"
    "s_add_u32 s3, s52, s56\n"
    "s_and_b32 s67, s3, 7\n"
    "s_lshl3_add_u32 s3, s55, s67\n"
    "s_mul_i32 s72, s3, 14336\n"
    "s_mul_i32 s70, s52, 14336\n"
    "s_and_b32 s73, s56, 3\n"
    "s_lshl_b32 s73, s73, 8\n"
    "s_mov_b32 s71, s70\n"
    "s_branch .Lfb_L9\n"
    ".Lfb_L1:\n"
    "v_mov_b32 v8, 158720\n"
    "v_mov_b32 v9, 0\n"
    "ds_write_b32 v8, v9\n"
    "v_and_b32 v13, 7, v1\n"
    "v_xor_b32 v10, v13, v4\n"
    "v_xor_b32 v11, 4, v10\n"
    "v_lshl_add_u32 v10, v10, 4, v12\n"
    "v_lshl_add_u32 v11, v11, 4, v12\n"
    "v_add_u32 v11, 6144, v11\n"
    "s_add_u32 s3, s52, s56\n"
    "s_and_b32 s67, s3, 7\n"
    "s_lshl3_add_u32 s3, s55, s67\n"
    "s_mul_i32 s72, s3, 14336\n"
    "s_mul_i32 s70, s52, 14336\n"
    "s_and_b32 s73, s56, 3\n"
    "s_lshl_b32 s73, s73, 8\n"
    "s_mov_b32 s71, s70\n"
    "s_waitcnt lgkmcnt(0)\n"
    "s_barrier\n"
    "s_load_dword s53, s[8:9], 0x0\n"
    "s_setprio 1\n"
    "s_lshl4_add_u32 m0, s73, s71\n"
    "s_add_u32 s65, s72, s73\n"
    "buffer_load_dwordx4 v10, s[12:15], s65 offen lds\n"
    "buffer_load_dwordx4 v11, s[12:15], s65 offen offset:1024 lds\n"
    "s_add_u32 s73, s73, 128\n"
    "s_cmp_eq_u32 s73, 896\n"
    "s_cselect_b32 s73, 0, s73\n"
    "s_lshl4_add_u32 m0, s73, s71\n"
    "s_add_u32 s65, s72, s73\n"
    "buffer_load_dwordx4 v10, s[12:15], s65 offen lds\n"
    "buffer_load_dwordx4 v11, s[12:15], s65 offen offset:1024 lds\n"
    ".Lfb_L9:\n"
    "v_and_b32 v5, 15, v1\n"
    "v_bfe_u32 v6, v1, 2, 2\n"
    "v_and_b32 v7, 3, v1\n"
    "v_mov_b32 v3, 1073741824\n"
    "v_lshlrev_b32 v249, 3, v1\n"
    "v_lshl_add_u32 v239, v4, 3, v6\n"
    "s_waitcnt lgkmcnt(0)\n"
    "s_mul_i32 s53, s53, 1664525\n"
    "s_add_u32 s53, s53, 1013904223\n"
    "s_cmp_lt_u32 s52, 4\n"
    "s_cbranch_scc0 .Lfb_L10\n"
    "s_setprio 2\n"
    "s_cmp_lt_u32 s58, 14\n"
    "s_cbranch_scc0 .Lfb_L11\n"
    "s_cmp_lt_u32 s52, 3\n"
    "s_cbranch_scc0 .Lfb_L12\n"
    "s_lshl_b32 s3, s52, 11\n"
    "s_add_u32 s3, s3, 131072\n"
    "v_lshrrev_b32 v9, 1, v7\n"
    "v_lshl_add_u32 v8, v239, 6, s3\n"
    "v_xor_b32 v9, v9, v7\n"
    "v_lshl_add_u32 v9, v9, 3, v8\n"
    "v_lshl_add_u32 v8, v7, 3, v8\n"
    "s_mul_i32 s3, s57, 42\n"
    "s_mul_i32 s4, s52, 14\n"
    "s_add_u32 s3, s3, s4\n"
    "s_add_u32 s3, s3, s58\n"
    "s_lshl_b32 s3, s3, 11\n"
    "s_add_u32 s3, s3, 212992\n"
    "s_waitcnt vmcnt(0)\n"
    "ds_read_b64_tr_b16 v[16:17], v8\n"
    "ds_read_b64_tr_b16 v[18:19], v8 offset:256\n"
    "ds_read_b64_tr_b16 v[20:21], v9 offset:32\n"
    "ds_read_b64_tr_b16 v[22:23], v9 offset:288\n"
    "s_waitcnt lgkmcnt(2)\n"
    "buffer_store_dwordx4 v[16:19], v2, s[8:11], s3 offen sc1\n"
    "s_waitcnt lgkmcnt(0)\n"
    "buffer_store_dwordx4 v[20:23], v2, s[8:11], s3 offen offset:1024 sc1\n"
    "s_branch .Lfb_L13\n"
    ".Lfb_L12:\n"
    "s_cmp_lt_u32 s58, 7\n"
    "s_cbranch_scc0 .Lfb_L14\n"
    "v_lshrrev_b32 v8, 2, v1\n"
    "v_lshlrev_b32 v8, 7, v8\n"
    "v_lshl_add_u32 v8, v7, 3, v8\n"
    "v_add_u32 v8, 137216, v8\n"
    "s_lshl_b32 s3, s58, 6\n"
    "s_add_u32 s3, s3, s56\n"
    "s_lshl_b32 s3, s3, 10\n"
    "s_add_u32 s3, s3, 1589248\n"
    "s_waitcnt vmcnt(0)\n"
    "ds_read_b64_tr_b16 v[16:17], v8\n"
    "ds_read_b64_tr_b16 v[18:19], v8 offset:2048\n"
    "ds_read_b64_tr_b16 v[20:21], v8 offset:32\n"
    "ds_read_b64_tr_b16 v[22:23], v8 offset:2080\n"
    "ds_read_b64_tr_b16 v[24:25], v8 offset:64\n"
    "ds_read_b64_tr_b16 v[26:27], v8 offset:2112\n"
    "ds_read_b64_tr_b16 v[28:29], v8 offset:96\n"
    "ds_read_b64_tr_b16 v[30:31], v8 offset:2144\n"
    "s_waitcnt lgkmcnt(6)\n"
    "buffer_store_dwordx4 v[16:19], v2, s[8:11], s3 offen sc1\n"
    "s_add_u32 s3, s3, 16384\n"
    "s_waitcnt lgkmcnt(4)\n"
    "buffer_store_dwordx4 v[20:23], v2, s[8:11], s3 offen sc1\n"
    "s_add_u32 s3, s3, 16384\n"
    "s_waitcnt lgkmcnt(2)\n"
    "buffer_store_dwordx4 v[24:27], v2, s[8:11], s3 offen sc1\n"
    "s_add_u32 s3, s3, 16384\n"
    "s_waitcnt lgkmcnt(0)\n"
    "buffer_store_dwordx4 v[28:31], v2, s[8:11], s3 offen sc1\n"
    ".Lfb_L14:\n"
    "s_waitcnt vmcnt(0)\n"
    "v_mov_b32 v255, 0\n"
    ".Lfb_L13:\n"
    "s_branch .Lfb_L15\n"
    ".Lfb_L11:\n"
    "s_cmp_lt_u32 s77, 28\n"
    "s_cbranch_scc0 .Lfb_L16\n"
    "s_lshl_b32 s3, s52, 8\n"
    "v_lshrrev_b32 v8, 3, v1\n"
    "v_lshlrev_b32 v8, 2, v8\n"
    "v_mov_b32 v52, 0\n"
    "v_mov_b32 v53, 0\n"
    "v_mov_b32 v54, 0\n"
    "v_mov_b32 v55, 0\n"
    "v_mov_b32 v56, 0\n"
    "v_mov_b32 v57, 0\n"
    "v_mov_b32 v58, 0\n"
    "v_mov_b32 v59, 0\n"
    "v_and_b32 v13, 7, v1\n"
    "v_lshlrev_b32 v13, 2, v13\n"
    "v_add_u32 v13, v13, v4\n"
    "s_add_u32 s4, s3, 160768\n"
    "v_lshl_add_u32 v13, v13, 3, s4\n"
    "s_waitcnt vmcnt(7)\n"
    "v_lshlrev_b32 v253, 16, v253\n"
    "v_mul_f32 v9, 0xbfb8aa3b, v253\n"
    "v_exp_f32 v9, v9\n"
    "s_nop 0\n"
    "v_add_f32 v9, 1.0, v9\n"
    "v_rcp_f32 v9, v9\n"
    "s_nop 0\n"
    "v_mul_f32 v9, v253, v9\n"
    "v_cvt_pk_bf16_f32 v9, v9, 0\n"
    "ds_bpermute_b32 v50, v8, v9 offset:192\n"
    "ds_bpermute_b32 v49, v8, v9 offset:160\n"
    "ds_bpermute_b32 v48, v8, v9 offset:128\n"
    "ds_bpermute_b32 v47, v8, v9 offset:96\n"
    "ds_bpermute_b32 v46, v8, v9 offset:64\n"
    "ds_bpermute_b32 v45, v8, v9 offset:32\n"
    "ds_bpermute_b32 v44, v8, v9 offset:0\n"
    "s_waitcnt lgkmcnt(0)\n"
    "v_lshlrev_b32 v60, 16, v44\n"
    "v_lshlrev_b32 v61, 16, v45\n"
    "v_lshlrev_b32 v62, 16, v46\n"
    "v_lshlrev_b32 v63, 16, v47\n"
    "v_lshlrev_b32 v64, 16, v48\n"
    "v_lshlrev_b32 v65, 16, v49\n"
    "v_lshlrev_b32 v66, 16, v50\n"
    "s_waitcnt vmcnt(6)\n"
    "v_dot2c_f32_bf16 v52, v16, v44\n"
    "v_dot2c_f32_bf16 v53, v16, v60\n"
    "v_dot2c_f32_bf16 v54, v17, v44\n"
    "v_dot2c_f32_bf16 v55, v17, v60\n"
    "v_dot2c_f32_bf16 v56, v18, v44\n"
    "v_dot2c_f32_bf16 v57, v18, v60\n"
    "v_dot2c_f32_bf16 v58, v19, v44\n"
    "v_dot2c_f32_bf16 v59, v19, v60\n"
    "s_waitcnt vmcnt(5)\n"
    "v_dot2c_f32_bf16 v52, v20, v45\n"
    "v_dot2c_f32_bf16 v53, v20, v61\n"
    "v_dot2c_f32_bf16 v54, v21, v45\n"
    "v_dot2c_f32_bf16 v55, v21, v61\n"
    "v_dot2c_f32_bf16 v56, v22, v45\n"
    "v_dot2c_f32_bf16 v57, v22, v61\n"
    "v_dot2c_f32_bf16 v58, v23, v45\n"
    "v_dot2c_f32_bf16 v59, v23, v61\n"
    "s_waitcnt vmcnt(4)\n"
    "v_dot2c_f32_bf16 v52, v24, v46\n"
    "v_dot2c_f32_bf16 v53, v24, v62\n"
    "v_dot2c_f32_bf16 v54, v25, v46\n"
    "v_dot2c_f32_bf16 v55, v25, v62\n"
    "v_dot2c_f32_bf16 v56, v26, v46\n"
    "v_dot2c_f32_bf16 v57, v26, v62\n"
    "v_dot2c_f32_bf16 v58, v27, v46\n"
    "v_dot2c_f32_bf16 v59, v27, v62\n"
    "s_waitcnt vmcnt(3)\n"
    "v_dot2c_f32_bf16 v52, v28, v47\n"
    "v_dot2c_f32_bf16 v53, v28, v63\n"
    "v_dot2c_f32_bf16 v54, v29, v47\n"
    "v_dot2c_f32_bf16 v55, v29, v63\n"
    "v_dot2c_f32_bf16 v56, v30, v47\n"
    "v_dot2c_f32_bf16 v57, v30, v63\n"
    "v_dot2c_f32_bf16 v58, v31, v47\n"
    "v_dot2c_f32_bf16 v59, v31, v63\n"
    "s_waitcnt vmcnt(2)\n"
    "v_dot2c_f32_bf16 v52, v32, v48\n"
    "v_dot2c_f32_bf16 v53, v32, v64\n"
    "v_dot2c_f32_bf16 v54, v33, v48\n"
    "v_dot2c_f32_bf16 v55, v33, v64\n"
    "v_dot2c_f32_bf16 v56, v34, v48\n"
    "v_dot2c_f32_bf16 v57, v34, v64\n"
    "v_dot2c_f32_bf16 v58, v35, v48\n"
    "v_dot2c_f32_bf16 v59, v35, v64\n"
    "s_waitcnt vmcnt(1)\n"
    "v_dot2c_f32_bf16 v52, v36, v49\n"
    "v_dot2c_f32_bf16 v53, v36, v65\n"
    "v_dot2c_f32_bf16 v54, v37, v49\n"
    "v_dot2c_f32_bf16 v55, v37, v65\n"
    "v_dot2c_f32_bf16 v56, v38, v49\n"
    "v_dot2c_f32_bf16 v57, v38, v65\n"
    "v_dot2c_f32_bf16 v58, v39, v49\n"
    "v_dot2c_f32_bf16 v59, v39, v65\n"
    "s_waitcnt vmcnt(0)\n"
    "v_dot2c_f32_bf16 v52, v40, v50\n"
    "v_dot2c_f32_bf16 v53, v40, v66\n"
    "v_dot2c_f32_bf16 v54, v41, v50\n"
    "v_dot2c_f32_bf16 v55, v41, v66\n"
    "v_dot2c_f32_bf16 v56, v42, v50\n"
    "v_dot2c_f32_bf16 v57, v42, v66\n"
    "v_dot2c_f32_bf16 v58, v43, v50\n"
    "v_dot2c_f32_bf16 v59, v43, v66\n"
    "v_permlane32_swap_b32 v52, v56\n"
    "v_permlane32_swap_b32 v53, v57\n"
    "v_permlane32_swap_b32 v54, v58\n"
    "v_permlane32_swap_b32 v55, v59\n"
    "v_add_f32 v52, v52, v56\n"
    "v_add_f32 v53, v53, v57\n"
    "v_add_f32 v54, v54, v58\n"
    "v_add_f32 v55, v55, v59\n"
    "s_nop 0\n"
    "v_permlane16_swap_b32 v52, v54\n"
    "v_permlane16_swap_b32 v53, v55\n"
    "v_add_f32 v52, v52, v54\n"
    "v_add_f32 v53, v53, v55\n"
    "s_nop 0\n"
    "v_add_f32_dpp v52, v52, v52 row_ror:8 row_mask:0xf bank_mask:0xf\n"
    "v_add_f32_dpp v53, v53, v53 row_ror:8 row_mask:0xf bank_mask:0xf\n"
    "ds_write_b64 v13, v[52:53]\n"
    "s_mov_b64 exec, 1\n"
    "v_mov_b32 v8, 158720\n"
    "v_mov_b32 v9, 1\n"
    "ds_add_rtn_u32 v9, v8, v9\n"
    "s_waitcnt lgkmcnt(0)\n"
    "v_readfirstlane_b32 s3, v9\n"
    "s_mov_b64 exec, -1\n"
    "s_cmp_eq_u32 s3, 3\n"
    "s_cbranch_scc0 .Lfb_L17\n"
    "s_lshr_b32 s6, s77, 1\n"
    "s_and_b32 s7, s77, 1\n"
    "v_lshlrev_b32 v13, 2, v1\n"
    "v_add_u32 v13, 160768, v13\n"
    "ds_read_b32 v19, v13 offset:768\n"
    "ds_read_b32 v18, v13 offset:512\n"
    "ds_read_b32 v17, v13 offset:256\n"
    "ds_read_b32 v16, v13\n"
    "s_mul_i32 s3, s7, 21504\n"
    "s_lshl_b32 s4, s6, 9\n"
    "s_add_u32 s3, s3, s4\n"
    "s_add_u32 s3, s3, 4096\n"
    "s_waitcnt lgkmcnt(0)\n"
    "v_add_f32 v16, v16, v17\n"
    "v_add_f32 v16, v16, v18\n"
    "v_add_f32 v16, v16, v19\n"
    "v_mov_b32 v17, s53\n"
    "buffer_store_dwordx2 v[16:17], v249, s[8:11], s3 offen sc1\n"
    ".Lfb_L17:\n"
    ".Lfb_L16:\n"
    ".Lfb_L15:\n"
    ".Lfb_L10:\n"
    "v_lshrrev_b32 v8, 1, v5\n"
    "v_xor_b32 v8, v8, v4\n"
    "v_lshlrev_b32 v9, 7, v5\n"
    "v_add_u32 v9, s70, v9\n"
    "v_xor_b32 v13, 4, v8\n"
    "v_lshl_add_u32 v139, v8, 4, v9\n"
    "v_lshl_add_u32 v140, v13, 4, v9\n"
    "s_add_u32 s97, s2, 2\n"
    "s_and_b32 s97, s97, 7\n"
    "s_and_b32 s4, s54, 7\n"
    "s_lshl3_add_u32 s61, s97, s4\n"
    "s_lshr_b32 s62, s54, 3\n"
    "s_and_b32 s3, s2, 1\n"
    "s_mul_i32 s3, s3, 7\n"
    "s_add_u32 s60, s3, s59\n"
    "s_lshr_b32 s63, s52, 1\n"
    "s_lshl_b32 s30, s52, 12\n"
    "s_lshl3_add_u32 s3, s56, s52\n"
    "s_lshl4_add_u32 s3, s3, s55\n"
    "s_lshl_b32 s3, s3, 9\n"
    "s_add_u32 s66, s3, 6864896\n"
    "s_lshl3_add_u32 s3, s55, s67\n"
    "s_lshl4_add_u32 s3, s3, s57\n"
    "s_lshl_b32 s3, s3, 10\n"
    "s_add_u32 s67, s3, 7913472\n"
    "s_mul_i32 s4, s62, 7\n"
    "s_add_u32 s4, s4, s52\n"
    "s_mul_i32 s3, s61, 28\n"
    "s_add_u32 s3, s3, s4\n"
    "s_lshl_b32 s3, s3, 10\n"
    "s_add_u32 s69, s3, 10010624\n"
    "s_lshl_b32 s3, s4, 14\n"
    "s_add_u32 s16, s3, 1589248\n"
    "s_min_u32 s4, s4, 27\n"
    "s_mul_i32 s3, s61, 28672\n"
    "s_lshl_b32 s4, s4, 5\n"
    "s_add_u32 s29, s3, s4\n"
    "s_lshl2_add_u32 s5, s61, s62\n"
    "s_lshl_b32 s5, s5, 8\n"
    "s_add_u32 s5, s5, 13680640\n"
    "s_cmp_lt_u32 s52, 7\n"
    "s_cselect_b32 s69, s69, s5\n"
    "s_cselect_b64 s[80:81], -1, 0\n"
    "s_lshl_b32 s3, s63, 4\n"
    "s_lshl_b64 s[78:79], 0xffff, s3\n"
    "s_and_b64 s[78:79], s[78:79], s[80:81]\n"
    "s_mov_b32 s82, 0\n"
    "s_mov_b32 s83, 0xffff0000\n"
    "s_mul_i32 s3, s56, 86016\n"
    "s_add_u32 s3, s3, s30\n"
    "s_add_u32 s68, s3, 212992\n"
    "s_lshl3_add_u32 s3, s57, s52\n"
    "s_lshl_b32 s3, s3, 13\n"
    "s_add_u32 s17, s3, 6864896\n"
    "s_add_u32 s18, s17, 4096\n"
    "s_lshl3_add_u32 s3, s60, s52\n"
    "s_mul_i32 s3, s3, 28672\n"
    "s_add_u32 s19, s3, 2048000\n"
    "s_lshl_b32 s3, s61, 15\n"
    "s_add_u32 s3, s3, s30\n"
    "s_add_u32 s28, s3, 7913472\n"
    "s_lshl_b32 s31, s52, 7\n"
    "s_mul_i32 s3, s56, 14\n"
    "s_add_u32 s3, s3, s58\n"
    "s_sub_u32 s95, s3, 196\n"
    "s_sub_u32 s4, 59, s77\n"
    "s_cmp_lt_u32 s58, 14\n"
    "s_cselect_b32 s95, s95, s4\n"
    "s_and_b32 s6, s52, 1\n"
    "s_lshl1_add_u32 s6, s56, s6\n"
    "v_lshrrev_b32 v8, 3, v1\n"
    "v_and_b32 v9, 0x70, v2\n"
    "s_add_u32 s96, s30, 114688\n"
    "s_cmp_lt_u32 s95, 56\n"
    "s_cbranch_scc0 .Lfb_L18\n"
    "s_and_b32 s3, s95, 1\n"
    "s_mul_i32 s3, s3, 7\n"
    "s_add_u32 s3, s3, s52\n"
    "s_lshl_b32 s98, s3, 6\n"
    "s_mul_i32 s74, s3, 172032\n"
    "s_lshr_b32 s4, s95, 1\n"
    "s_add_u32 s4, s4, 14\n"
    "s_lshl_b32 s4, s4, 7\n"
    "s_add_u32 s74, s74, s4\n"
    "s_sub_u32 s34, s74, 688128\n"
    "s_mov_b64 s[36:37], s[20:21]\n"
    "s_mov_b32 s38, s22\n"
    "s_mov_b64 s[44:45], s[20:21]\n"
    "s_mov_b32 s46, s22\n"
    "s_movk_i32 s88, 5376\n"
    "s_movk_i32 s35, 5376\n"
    "s_cmp_lt_u32 s52, 7\n"
    "s_cselect_b32 s76, 3, 0\n"
    "s_branch .Lfb_L19\n"
    ".Lfb_L18:\n"
    "s_cmp_lt_u32 s52, 4\n"
    "s_cbranch_scc0 .Lfb_L20\n"
    "s_mul_i32 s6, s6, 112\n"
    "s_lshl1_add_u32 s6, s58, s6\n"
    "s_add_u32 s6, s6, s63\n"
    "s_lshl_b32 s6, s6, 10\n"
    "s_add_u32 s75, s6, 2048000\n"
    "s_movk_i32 s89, 28672\n"
    "s_branch .Lfb_L21\n"
    ".Lfb_L20:\n"
    "s_mul_i32 s34, s58, 114688\n"
    "s_lshl_b32 s7, s6, 7\n"
    "s_add_u32 s34, s34, s7\n"
    "s_cmp_ge_u32 s52, 6\n"
    "s_cselect_b64 s[36:37], s[40:41], s[36:37]\n"
    "s_lshr_b32 s4, s56, 1\n"
    "s_lshl1_add_u32 s6, s3, s52\n"
    "s_sub_u32 s6, s6, 4\n"
    "s_lshl_b32 s7, s6, 12\n"
    "s_add_u32 s75, s7, 5259264\n"
    "s_mul_i32 s7, s4, 56\n"
    "s_sub_u32 s6, s6, s7\n"
    "s_mul_i32 s74, s6, 28672\n"
    "s_lshl_b32 s7, s4, 7\n"
    "s_add_u32 s74, s74, s7\n"
    "s_movk_i32 s89, 1024\n"
    ".Lfb_L21:\n"
    "s_movk_i32 s88, 896\n"
    "s_movk_i32 s35, 3584\n"
    "s_max_u32 s4, s58, s56\n"
    "s_cmp_lt_u32 s4, 14\n"
    "s_cselect_b32 s4, s52, 7\n"
    "s_cmp_lt_u32 s4, 6\n"
    "s_cselect_b32 s76, 1, 0\n"
    ".Lfb_L19:\n"
    "v_mad_u32_u24 v248, v8, s88, v9\n"
    "v_mad_u32_u24 v122, v8, s35, v9\n"
    "s_lshl3_add_u32 s88, s88, -1024\n"
    "s_lshl3_add_u32 s35, s35, -1024\n"
    "v_and_b32 v8, 1, v1\n"
    "v_cmp_eq_u32 vcc, v4, v6\n"
    "v_lshlrev_b32 v13, 4, v8\n"
    "v_mov_b32 v9, 0xffff\n"
    "v_lshlrev_b32 v9, v13, v9\n"
    "v_cndmask_b32 v9, 0, v9, vcc\n"
    "v_cmp_gt_u32 vcc, 2, v7\n"
    "v_mov_b32 v130, 0\n"
    "v_mov_b32 v131, 0\n"
    "v_cndmask_b32 v237, 0, v9, vcc\n"
    "v_xor_b32 v238, v9, v237\n"
    "v_and_b32 v128, 0x3f803f80, v237\n"
    "v_and_b32 v129, 0x3f803f80, v238\n"
    "v_and_b32 v132, 0x3f803f80, v237\n"
    "v_and_b32 v133, 0x3f803f80, v238\n"
    "v_mov_b32 v124, 0x3f803f80\n"
    "v_mov_b32 v125, 0x3f803f80\n"
    "v_mov_b32 v126, 0x3f803f80\n"
    "v_mov_b32 v127, 0x3f803f80\n"
    "v_lshlrev_b32 v142, 4, v4\n"
    "v_add_u32 v142, 148480, v142\n"
    "v_lshl_add_u32 v142, v8, 10, v142\n"
    "v_add_u32 v142, s31, v142\n"
    "v_cmp_eq_u32 vcc, v4, v6\n"
    "v_mov_b32 v9, 16\n"
    "s_nop 0\n"
    "v_cndmask_b32 v9, v9, v5, vcc\n"
    "v_lshlrev_b32 v9, 4, v9\n"
    "v_add_u32 v251, 144384, v9\n"
    "v_add_u32 v245, s30, v2\n"
    "v_add_u32 v246, 61440, v245\n"
    "v_add_u32 v244, 61440, v2\n"
    "v_add_u32 v243, 154624, v2\n"
    "s_and_b32 s4, s52, 1\n"
    "s_lshl3_add_u32 s3, s4, 28672\n"
    "v_add_u32 v247, s3, v2\n"
    "s_mul_i32 s3, s52, 3200\n"
    "v_mul_u32_u24 v242, 200, v5\n"
    "s_add_u32 s3, s3, 92160\n"
    "v_lshl_add_u32 v242, v4, 3, v242\n"
    "v_add_u32 v242, s3, v242\n"
    "v_mul_u32_u24 v240, 200, v239\n"
    "v_lshl_add_u32 v240, v7, 3, v240\n"
    "s_lshl_b32 s3, s63, 5\n"
    "s_add_u32 s3, s3, 92160\n"
    "s_lshl_b32 s5, s4, 5\n"
    "s_add_u32 s5, s5, 92288\n"
    "v_add_u32 v241, s5, v240\n"
    "v_add_u32 v240, s3, v240\n"
    "s_lshr_b32 s3, s52, 2\n"
    "s_lshl1_add_u32 s3, s3, s4\n"
    "s_lshl_b32 s3, s3, 10\n"
    "s_and_b32 s4, s63, 1\n"
    "s_lshl3_add_u32 s3, s4, s3\n"
    "s_add_u32 s3, s3, 150528\n"
    "v_and_b32 v8, 31, v1\n"
    "v_lshrrev_b32 v9, 5, v1\n"
    "v_lshl_add_u32 v134, v8, 1, v9\n"
    "v_lshl_add_u32 v134, v134, 4, s3\n"
    "v_add_u32 v135, 150528, v2\n"
    "v_mul_u32_u24 v136, 896, v8\n"
    "v_lshl_add_u32 v136, v9, 4, v136\n"
    "s_cmp_lt_u32 s59, 7\n"
    "s_cselect_b64 s[84:85], -1, 0\n"
    "v_cndmask_b32 v254, v3, v2, s[84:85]\n"
    "s_mul_i32 s3, s62, 56\n"
    "v_add_u32 v8, s3, v1\n"
    "v_min_u32 v8, 223, v8\n"
    "v_add_u32 v8, 448, v8\n"
    "v_add_u32 v9, 672, v0\n"
    "v_cndmask_b32 v8, v8, v9, s[80:81]\n"
    "v_lshlrev_b32 v137, 4, v8\n"
    "v_lshlrev_b32 v13, 7, v239\n"
    "v_lshl_add_u32 v13, v7, 3, v13\n"
    "v_add_u32 v252, s96, v13\n"
    "s_cmp_lt_u32 s52, 4\n"
    "s_cbranch_scc0 .Lfb_L22\n"
    "s_cmp_lt_u32 s58, 14\n"
    "s_cbranch_scc0 .Lfb_L23\n"
    "s_lshl4_add_u32 s4, s57, s58\n"
    "s_lshl_b32 s4, s4, 7\n"
    "s_waitcnt vmcnt(0)\n"
    "s_cmp_lt_u32 s52, 3\n"
    "s_cbranch_scc0 .Lfb_L24\n"
    "s_add_u32 s4, s4, 49152\n"
    "s_mov_b64 exec, 1\n"
    "v_mov_b32 v8, 158720\n"
    "v_mov_b32 v9, 1\n"
    "ds_add_rtn_u32 v9, v8, v9\n"
    "s_waitcnt lgkmcnt(0)\n"
    "v_readfirstlane_b32 s3, v9\n"
    "s_mov_b64 exec, -1\n"
    "s_cmp_eq_u32 s3, 2\n"
    "s_cbranch_scc0 .Lfb_L25\n"
    "s_mov_b64 exec, 1\n"
    "v_mov_b32 v14, s53\n"
    "buffer_store_dword v14, off, s[8:11], s4 sc1\n"
    "s_mov_b64 exec, -1\n"
    ".Lfb_L25:\n"
    "s_branch .Lfb_L26\n"
    ".Lfb_L24:\n"
    "s_cmp_lt_u32 s58, 7\n"
    "s_cbranch_scc0 .Lfb_L27\n"
    "s_add_u32 s4, s4, 81920\n"
    "s_mov_b64 exec, 1\n"
    "v_mov_b32 v14, s53\n"
    "buffer_store_dword v14, off, s[8:11], s4 sc1\n"
    "s_mov_b64 exec, -1\n"
    ".Lfb_L27:\n"
    ".Lfb_L26:\n"
    ".Lfb_L23:\n"
    "s_and_b32 s93, s54, 0\n"
    "s_lshl_b32 s93, s93, 13\n"
    "s_add_u32 s20, s19, s93\n"
    "s_add_u32 s3, s19, 28672\n"
    "s_add_u32 s21, s20, 4096\n"
    "s_cmp_eq_u32 s21, s3\n"
    "s_cselect_b32 s21, s19, s21\n"
    "s_add_u32 s22, s21, 4096\n"
    "s_add_u32 s23, s22, 4096\n"
    "s_cmp_eq_u32 s23, s3\n"
    "s_cselect_b32 s23, s19, s23\n"
    "s_add_u32 s32, s23, 4096\n"
    "s_add_u32 s33, s32, 4096\n"
    "s_cmp_eq_u32 s33, s3\n"
    "s_cselect_b32 s33, s19, s33\n"
    "s_add_u32 s94, s33, 4096\n"
    "v_and_b32 v8, 1, v1\n"
    "v_lshl_add_u32 v8, s52, 1, v8\n"
    "s_min_u32 s3, s60, 13\n"
    "s_and_b32 s4, s3, 4\n"
    "s_lshl_b32 s4, s4, 1\n"
    "v_min_u32 v9, 13, v8\n"
    "v_xor_b32 v9, s4, v9\n"
    "v_mov_b32 v14, s3\n"
    "s_lshr_b32 s3, s61, 2\n"
    "v_mov_b32 v12, s3\n"
    "v_cmp_eq_u32 vcc, 1, v4\n"
    "s_nop 1\n"
    "v_cndmask_b32 v9, v9, v12, vcc\n"
    "v_cndmask_b32 v14, v14, v8, vcc\n"
    "v_and_b32 v138, 3, v9\n"
    "v_and_b32 v13, 7, v14\n"
    "v_lshl_add_u32 v138, v13, 2, v138\n"
    "v_lshrrev_b32 v13, 2, v9\n"
    "v_lshl_add_u32 v138, v138, 2, v13\n"
    "v_lshrrev_b32 v13, 3, v14\n"
    "v_lshl_add_u32 v138, v138, 1, v13\n"
    "s_lshr_b32 s3, s55, 1\n"
    "s_add_u32 s3, s3, 6\n"
    "s_and_b32 s3, s3, 7\n"
    "s_and_b32 s4, s55, 1\n"
    "s_lshl_b32 s4, s4, 5\n"
    "s_add_u32 s3, s3, s4\n"
    "v_lshrrev_b32 v9, 2, v8\n"
    "v_and_b32 v12, 3, v8\n"
    "v_lshl_add_u32 v9, v9, 3, v12\n"
    "v_lshl_add_u32 v9, v9, 3, s3\n"
    "v_cmp_gt_u32 vcc, 32, v1\n"
    "v_mov_b32 v14, s57\n"
    "s_nop 0\n"
    "v_cndmask_b32 v138, v9, v138, vcc\n"
    "v_lshlrev_b32 v138, 7, v138\n"
    "v_and_b32 v143, 3, v8\n"
    "v_and_b32 v13, 7, v14\n"
    "v_lshl_add_u32 v143, v13, 2, v143\n"
    "v_lshrrev_b32 v13, 2, v8\n"
    "v_lshl_add_u32 v143, v143, 2, v13\n"
    "v_lshrrev_b32 v13, 3, v14\n"
    "v_lshl_add_u32 v143, v143, 1, v13\n"
    "v_lshlrev_b32 v143, 7, v143\n"
    "s_min_u32 s3, s59, 6\n"
    "v_bfe_u32 v14, v1, 1, 1\n"
    "v_lshl_add_u32 v14, s3, 1, v14\n"
    "v_min_u32 v9, 13, v8\n"
    "v_and_b32 v12, 4, v14\n"
    "v_lshlrev_b32 v12, 1, v12\n"
    "v_xor_b32 v9, v9, v12\n"
    "v_and_b32 v8, 3, v9\n"
    "v_and_b32 v13, 7, v14\n"
    "v_lshl_add_u32 v8, v13, 2, v8\n"
    "v_lshrrev_b32 v13, 2, v9\n"
    "v_lshl_add_u32 v8, v8, 2, v13\n"
    "v_lshrrev_b32 v13, 3, v14\n"
    "v_lshl_add_u32 v8, v8, 1, v13\n"
    "v_lshlrev_b32 v8, 7, v8\n"
    "v_cndmask_b32 v138, v138, v8, s[82:83]\n"
    "s_setprio 0\n"
    "s_barrier\n"
    "s_cmp_eq_u32 s76, 3\n"
    "s_cbranch_scc0 .Lfb_L28\n"
    "v_lshlrev_b32 v8, 4, v4\n"
    "buffer_load_dwordx4 v[116:119], v8, s[24:27], s98 offen\n"
    ".Lfb_L28:\n"
    "s_branch .Lfb_L29\n"
    ".Lfb_L22:\n"
    "s_setprio 1\n"
    "s_sub_u32 s6, s52, 4\n"
    "v_min_u32 v112, 55, v1\n"
    "v_lshlrev_b32 v112, 5, v112\n"
    "s_mul_i32 s90, s6, 1792\n"
    "s_add_u32 s90, s90, 4096\n"
    "s_add_u32 s91, s90, 21504\n"
    "s_lshl_b32 s3, s6, 2\n"
    "v_add_u32 v113, s3, v7\n"
    "v_min_u32 v113, 13, v113\n"
    "s_lshl_b32 s3, s56, 11\n"
    "s_add_u32 s3, s3, 49152\n"
    "v_lshl_add_u32 v113, v113, 7, s3\n"
    "s_and_b32 s3, s52, 1\n"
    "s_mul_i32 s4, s3, 448\n"
    "s_cmp_lt_u32 s52, 6\n"
    "s_cselect_b32 s3, 1024, 0\n"
    "s_cselect_b32 s99, 0, 1.0\n"
    "s_add_u32 s3, s3, s4\n"
    "s_add_u32 s3, s3, 148480\n"
    "v_lshrrev_b32 v8, 2, v112\n"
    "v_add_u32 v141, s3, v8\n"
    "s_and_b32 s3, s52, 1\n"
    "s_mul_i32 s3, s3, 1904\n"
    "s_add_u32 s3, s3, 144384\n"
    "v_lshrrev_b32 v8, 8, v112\n"
    "v_mul_u32_u24 v8, 272, v8\n"
    "v_and_b32 v9, 0xc0, v112\n"
    "v_add3_u32 v8, v8, v9, s3\n"
    "v_bfe_u32 v9, v112, 5, 1\n"
    "v_lshl_add_u32 v123, v9, 3, v8\n"
    "v_mov_b32 v36, 0\n"
    "v_mov_b32 v37, 0\n"
    "v_mov_b32 v38, 0\n"
    "v_mov_b32 v39, 0\n"
    "v_mov_b32 v40, 0\n"
    "v_mov_b32 v41, 0\n"
    "v_mov_b32 v42, 0\n"
    "v_mov_b32 v43, 0\n"
    "s_cmp_eq_u32 s52, 5\n"
    "s_cbranch_scc0 .Lfb_L30\n"
    "v_min_u32 v8, 13, v1\n"
    "v_mul_u32_u24 v8, 272, v8\n"
    "v_add_u32 v8, 144640, v8\n"
    "ds_write_b128 v8, v[36:39]\n"
    ".Lfb_L30:\n"
    "s_waitcnt vmcnt(3)\n"
    "s_sleep 4\n"
    "s_add_u32 s73, s73, 128\n"
    "s_lshl4_add_u32 m0, s73, s71\n"
    "s_add_u32 s65, s72, s73\n"
    "buffer_load_dwordx4 v10, s[12:15], s65 offen lds\n"
    "buffer_load_dwordx4 v11, s[12:15], s65 offen offset:1024 lds\n"
    "s_add_u32 s73, s73, 128\n"
    "s_cmp_eq_u32 s73, 896\n"
    "s_cselect_b32 s73, 0, s73\n"
    "s_lshl4_add_u32 m0, s73, s71\n"
    "s_add_u32 s65, s72, s73\n"
    "buffer_load_dwordx4 v10, s[12:15], s65 offen lds\n"
    "buffer_load_dwordx4 v11, s[12:15], s65 offen offset:1024 lds\n"
    "s_add_u32 s73, s73, 128\n"
    "s_lshl4_add_u32 m0, s73, s71\n"
    "s_add_u32 s65, s72, s73\n"
    "buffer_load_dwordx4 v10, s[12:15], s65 offen lds\n"
    "buffer_load_dwordx4 v11, s[12:15], s65 offen offset:1024 lds\n"
    "s_add_u32 s73, s73, 128\n"
    "s_cmp_eq_u32 s73, 896\n"
    "s_cselect_b32 s73, 0, s73\n"
    "s_lshl4_add_u32 m0, s73, s71\n"
    "s_add_u32 s65, s72, s73\n"
    "buffer_load_dwordx4 v10, s[12:15], s65 offen lds\n"
    "buffer_load_dwordx4 v11, s[12:15], s65 offen offset:1024 lds\n"
    "s_add_u32 s73, s73, 128\n"
    "s_lshl4_add_u32 m0, s73, s71\n"
    "s_add_u32 s65, s72, s73\n"
    "buffer_load_dwordx4 v10, s[12:15], s65 offen lds\n"
    "buffer_load_dwordx4 v11, s[12:15], s65 offen offset:1024 lds\n"
    "s_add_u32 s3, s52, s56\n"
    "s_bitcmp1_b32 s3, 2\n"
    "s_cselect_b32 s3, -1, 1\n"
    "s_mul_i32 s3, s3, 57344\n"
    "s_add_u32 s72, s72, s3\n"
    "s_mov_b32 s73, 0\n"
    "s_sub_u32 s71, s71, 57344\n"
    "s_cmp_eq_u32 s76, 3\n"
    "s_cbranch_scc0 .Lfb_L31\n"
    "v_lshlrev_b32 v8, 4, v4\n"
    "buffer_load_dwordx4 v[116:119], v8, s[24:27], s98 offen\n"
    ".Lfb_L31:\n"
    "s_lshl4_add_u32 m0, s73, s71\n"
    "s_add_u32 s65, s72, s73\n"
    "buffer_load_dwordx4 v10, s[12:15], s65 offen lds\n"
    "buffer_load_dwordx4 v11, s[12:15], s65 offen offset:1024 lds\n"
    "s_add_u32 s73, s73, 128\n"
    "s_cmp_eq_u32 s73, 896\n"
    "s_cselect_b32 s73, 0, s73\n"
    "s_lshl4_add_u32 m0, s73, s71\n"
    "s_add_u32 s65, s72, s73\n"
    "buffer_load_dwordx4 v10, s[12:15], s65 offen lds\n"
    "buffer_load_dwordx4 v11, s[12:15], s65 offen offset:1024 lds\n"
    "s_add_u32 s73, s73, 128\n"
    "s_lshl4_add_u32 m0, s73, s71\n"
    "s_add_u32 s65, s72, s73\n"
    "buffer_load_dwordx4 v10, s[12:15], s65 offen lds\n"
    "buffer_load_dwordx4 v11, s[12:15], s65 offen offset:1024 lds\n"
    "s_add_u32 s73, s73, 128\n"
    "s_cmp_eq_u32 s73, 896\n"
    "s_cselect_b32 s73, 0, s73\n"
    "s_lshl4_add_u32 m0, s73, s71\n"
    "s_add_u32 s65, s72, s73\n"
    "buffer_load_dwordx4 v10, s[12:15], s65 offen lds\n"
    "buffer_load_dwordx4 v11, s[12:15], s65 offen offset:1024 lds\n"
    "s_add_u32 s73, s73, 128\n"
    "s_lshl4_add_u32 m0, s73, s71\n"
    "s_add_u32 s65, s72, s73\n"
    "buffer_load_dwordx4 v10, s[12:15], s65 offen lds\n"
    "buffer_load_dwordx4 v11, s[12:15], s65 offen offset:1024 lds\n"
    "s_add_u32 s73, s73, 128\n"
    "s_cmp_eq_u32 s73, 896\n"
    "s_cselect_b32 s73, 0, s73\n"
    "s_lshl4_add_u32 m0, s73, s71\n"
    "s_add_u32 s65, s72, s73\n"
    "buffer_load_dwordx4 v10, s[12:15], s65 offen lds\n"
    "buffer_load_dwordx4 v11, s[12:15], s65 offen offset:1024 lds\n"
    "s_add_u32 s73, s73, 128\n"
    "s_lshl4_add_u32 m0, s73, s71\n"
    "s_add_u32 s65, s72, s73\n"
    "buffer_load_dwordx4 v10, s[12:15], s65 offen lds\n"
    "buffer_load_dwordx4 v11, s[12:15], s65 offen offset:1024 lds\n"
    "s_barrier\n"
    "s_max_u32 s4, s58, s56\n"
    "s_cmp_lt_u32 s4, 14\n"
    "s_cselect_b32 s4, 0, s95\n"
    "s_cmp_lt_u32 s4, 56\n"
    "s_cbranch_scc0 .Lfb_L32\n"
    "s_sub_u32 m0, s96, 16384\n"
    "s_nop 0\n"
    "buffer_load_dwordx4 v122, s[36:39], s34 offen lds\n"
    "s_add_u32 s34, s34, s35\n"
    "buffer_load_dwordx4 v122, s[36:39], s34 offen offset:1024 lds\n"
    "s_add_u32 s34, s34, s35\n"
    "buffer_load_dwordx4 v122, s[36:39], s34 offen offset:2048 lds\n"
    "s_add_u32 s34, s34, s35\n"
    "buffer_load_dwordx4 v122, s[36:39], s34 offen offset:3072 lds\n"
    "s_waitcnt vmcnt(4)\n"
    "s_branch .Lfb_L33\n"
    ".Lfb_L32:\n"
    "s_waitcnt vmcnt(0)\n"
    ".Lfb_L33:\n"
    ".Lfb_L29:\n"
    "s_barrier\n"
    "ds_read_b128 v[180:183], v140\n"
    "ds_read_b128 v[176:179], v139\n"
    "ds_read_b128 v[188:191], v140 offset:2048\n"
    "ds_read_b128 v[184:187], v139 offset:2048\n"
    "ds_read_b128 v[196:199], v140 offset:4096\n"
    "ds_read_b128 v[192:195], v139 offset:4096\n"
    "ds_read_b128 v[204:207], v140 offset:6144\n"
    "ds_read_b128 v[200:203], v139 offset:6144\n"
    "ds_read_b128 v[212:215], v140 offset:8192\n"
    "ds_read_b128 v[208:211], v139 offset:8192\n"
    "ds_read_b128 v[220:223], v140 offset:10240\n"
    "ds_read_b128 v[216:219], v139 offset:10240\n"
    "ds_read_b128 v[228:231], v140 offset:12288\n"
    "ds_read_b128 v[224:227], v139 offset:12288\n"
    "s_cmp_ge_u32 s52, 4\n"
    "s_cbranch_scc0 .Lfb_L34\n"
    "s_cmp_ge_u32 s52, 4\n"
    "s_cselect_b32 s3, s76, 0\n"
    "s_cmp_lg_u32 s3, 0\n"
    "s_cbranch_scc0 .Lfb_L35\n"
    "s_mov_b32 m0, s96\n"
    "s_nop 0\n"
    "buffer_load_dwordx4 v248, s[44:47], s74 offen lds\n"
    "s_add_u32 s74, s74, s88\n"
    "buffer_load_dwordx4 v248, s[44:47], s74 offen offset:1024 lds\n"
    "s_add_u32 s74, s74, s88\n"
    "buffer_load_dwordx4 v248, s[44:47], s74 offen offset:2048 lds\n"
    "s_add_u32 s74, s74, s88\n"
    "buffer_load_dwordx4 v248, s[44:47], s74 offen offset:3072 lds\n"
    ".Lfb_L35:\n"
    "v_mov_b32 v64, -1\n"
    "v_mov_b32 v65, -1\n"
    "v_mov_b32 v66, -1\n"
    "v_mov_b32 v67, -1\n"
    "v_lshrrev_b32 v8, 5, v1\n"
    "v_and_b32 v9, 0x1f0, v2\n"
    "v_lshl_add_u32 v8, v8, 15, v9\n"
    "s_sub_u32 s3, s66, 32768\n"
    "buffer_store_dwordx4 v[64:67], v8, s[8:11], s3 offen sc1\n"
    "s_waitcnt lgkmcnt(12)\n"
    "v_mfma_f32_16x16x32_bf16 v[48:51], v[176:179], v[176:179], 0\n"
    "v_mfma_f32_16x16x32_bf16 v[52:55], v[124:127], v[176:179], 0\n"
    "v_mfma_f32_16x16x32_bf16 v[48:51], v[180:183], v[180:183], v[48:51]\n"
    "v_mfma_f32_16x16x32_bf16 v[52:55], v[124:127], v[180:183], v[52:55]\n"
    "s_waitcnt lgkmcnt(10)\n"
    "v_mfma_f32_16x16x32_bf16 v[48:51], v[184:187], v[184:187], v[48:51]\n"
    "v_mfma_f32_16x16x32_bf16 v[52:55], v[124:127], v[184:187], v[52:55]\n"
    "v_mfma_f32_16x16x32_bf16 v[48:51], v[188:191], v[188:191], v[48:51]\n"
    "v_mfma_f32_16x16x32_bf16 v[52:55], v[124:127], v[188:191], v[52:55]\n"
    "s_waitcnt lgkmcnt(8)\n"
    "v_mfma_f32_16x16x32_bf16 v[48:51], v[192:195], v[192:195], v[48:51]\n"
    "v_mfma_f32_16x16x32_bf16 v[52:55], v[124:127], v[192:195], v[52:55]\n"
    "v_mfma_f32_16x16x32_bf16 v[48:51], v[196:199], v[196:199], v[48:51]\n"
    "v_mfma_f32_16x16x32_bf16 v[52:55], v[124:127], v[196:199], v[52:55]\n"
    "s_waitcnt lgkmcnt(6)\n"
    "v_mfma_f32_16x16x32_bf16 v[48:51], v[200:203], v[200:203], v[48:51]\n"
    "v_mfma_f32_16x16x32_bf16 v[52:55], v[124:127], v[200:203], v[52:55]\n"
    "v_mfma_f32_16x16x32_bf16 v[48:51], v[204:207], v[204:207], v[48:51]\n"
    "v_mfma_f32_16x16x32_bf16 v[52:55], v[124:127], v[204:207], v[52:55]\n"
    "s_waitcnt lgkmcnt(4)\n"
    "v_mfma_f32_16x16x32_bf16 v[48:51], v[208:211], v[208:211], v[48:51]\n"
    "v_mfma_f32_16x16x32_bf16 v[52:55], v[124:127], v[208:211], v[52:55]\n"
    "v_mfma_f32_16x16x32_bf16 v[48:51], v[212:215], v[212:215], v[48:51]\n"
    "v_mfma_f32_16x16x32_bf16 v[52:55], v[124:127], v[212:215], v[52:55]\n"
    "s_waitcnt lgkmcnt(2)\n"
    "v_mfma_f32_16x16x32_bf16 v[48:51], v[216:219], v[216:219], v[48:51]\n"
    "v_mfma_f32_16x16x32_bf16 v[52:55], v[124:127], v[216:219], v[52:55]\n"
    "v_mfma_f32_16x16x32_bf16 v[48:51], v[220:223], v[220:223], v[48:51]\n"
    "v_mfma_f32_16x16x32_bf16 v[52:55], v[124:127], v[220:223], v[52:55]\n"
    "s_waitcnt lgkmcnt(0)\n"
    "v_mfma_f32_16x16x32_bf16 v[48:51], v[224:227], v[224:227], v[48:51]\n"
    "v_mfma_f32_16x16x32_bf16 v[52:55], v[124:127], v[224:227], v[52:55]\n"
    "v_mfma_f32_16x16x32_bf16 v[48:51], v[228:231], v[228:231], v[48:51]\n"
    "v_mfma_f32_16x16x32_bf16 v[52:55], v[124:127], v[228:231], v[52:55]\n"
    "v_cmp_eq_u32 vcc, 1, v7\n"
    "v_cmp_eq_u32 s[40:41], 2, v7\n"
    "v_cmp_eq_u32 s[42:43], 3, v7\n"
    "v_cmp_eq_u32 s[84:85], v4, v6\n"
    "s_nop 2\n"
    "v_cndmask_b32 v8, v48, v49, vcc\n"
    "v_cndmask_b32 v8, v8, v50, s[40:41]\n"
    "v_cndmask_b32 v8, v8, v51, s[42:43]\n"
    "v_cndmask_b32 v9, 0, v8, s[84:85]\n"
    "v_cndmask_b32 v8, 0, v8, s[84:85]\n"
    "v_mul_f32 v12, 0x3b124925, v52\n"
    "v_cvt_pk_bf16_f32 v232, -v12, -v12\n"
    "v_permlane32_swap_b32 v8, v9\n"
    "v_add_f32 v8, v8, v9\n"
    "v_mov_b32 v9, v8\n"
    "v_mov_b32 v234, v232\n"
    "s_nop 0\n"
    "v_permlane16_swap_b32 v8, v9\n"
    "v_add_f32 v8, v8, v9\n"
    "v_mul_f32 v8, 0x3b124925, v8\n"
    "v_fma_f32 v8, -v12, v12, v8\n"
    "v_max_f32 v8, 0, v8\n"
    "v_add_f32 v8, 0x3727c5ac, v8\n"
    "v_sqrt_f32 v8, v8\n"
    "s_nop 0\n"
    "v_cvt_pk_bf16_f32 v233, v8, v8\n"
    "v_lshlrev_b32 v8, 16, v233\n"
    "v_mov_b32 v235, v233\n"
    "v_rcp_f32 v236, v8\n"
    "buffer_load_dwordx4 v[16:19], v112, s[8:11], s90 offen sc1\n"
    "buffer_load_dwordx4 v[20:23], v112, s[8:11], s90 offen offset:16 sc1\n"
    "buffer_load_dwordx4 v[24:27], v112, s[8:11], s91 offen sc1\n"
    "buffer_load_dwordx4 v[28:31], v112, s[8:11], s91 offen offset:16 sc1\n"
    "buffer_load_dword v32, v113, s[8:11], 0 offen sc1\n"
    "s_and_b32 s93, s54, 0\n"
    "s_lshl_b32 s93, s93, 13\n"
    "s_add_u32 s20, s19, s93\n"
    "s_add_u32 s3, s19, 28672\n"
    "s_add_u32 s21, s20, 4096\n"
    "s_cmp_eq_u32 s21, s3\n"
    "s_cselect_b32 s21, s19, s21\n"
    "s_add_u32 s22, s21, 4096\n"
    "s_add_u32 s23, s22, 4096\n"
    "s_cmp_eq_u32 s23, s3\n"
    "s_cselect_b32 s23, s19, s23\n"
    "s_add_u32 s32, s23, 4096\n"
    "s_add_u32 s33, s32, 4096\n"
    "s_cmp_eq_u32 s33, s3\n"
    "s_cselect_b32 s33, s19, s33\n"
    "s_add_u32 s94, s33, 4096\n"
    "v_and_b32 v8, 1, v1\n"
    "v_lshl_add_u32 v8, s52, 1, v8\n"
    "s_min_u32 s3, s60, 13\n"
    "s_and_b32 s4, s3, 4\n"
    "s_lshl_b32 s4, s4, 1\n"
    "v_min_u32 v9, 13, v8\n"
    "v_xor_b32 v9, s4, v9\n"
    "v_mov_b32 v14, s3\n"
    "s_lshr_b32 s3, s61, 2\n"
    "v_mov_b32 v12, s3\n"
    "v_cmp_eq_u32 vcc, 1, v4\n"
    "s_nop 1\n"
    "v_cndmask_b32 v9, v9, v12, vcc\n"
    "v_cndmask_b32 v14, v14, v8, vcc\n"
    "v_and_b32 v138, 3, v9\n"
    "v_and_b32 v13, 7, v14\n"
    "v_lshl_add_u32 v138, v13, 2, v138\n"
    "v_lshrrev_b32 v13, 2, v9\n"
    "v_lshl_add_u32 v138, v138, 2, v13\n"
    "v_lshrrev_b32 v13, 3, v14\n"
    "v_lshl_add_u32 v138, v138, 1, v13\n"
    "s_lshr_b32 s3, s55, 1\n"
    "s_add_u32 s3, s3, 6\n"
    "s_and_b32 s3, s3, 7\n"
    "s_and_b32 s4, s55, 1\n"
    "s_lshl_b32 s4, s4, 5\n"
    "s_add_u32 s3, s3, s4\n"
    "v_lshrrev_b32 v9, 2, v8\n"
    "v_and_b32 v12, 3, v8\n"
    "v_lshl_add_u32 v9, v9, 3, v12\n"
    "v_lshl_add_u32 v9, v9, 3, s3\n"
    "v_cmp_gt_u32 vcc, 32, v1\n"
    "v_mov_b32 v14, s57\n"
    "s_nop 0\n"
    "v_cndmask_b32 v138, v9, v138, vcc\n"
    "v_lshlrev_b32 v138, 7, v138\n"
    "v_and_b32 v143, 3, v8\n"
    "v_and_b32 v13, 7, v14\n"
    "v_lshl_add_u32 v143, v13, 2, v143\n"
    "v_lshrrev_b32 v13, 2, v8\n"
    "v_lshl_add_u32 v143, v143, 2, v13\n"
    "v_lshrrev_b32 v13, 3, v14\n"
    "v_lshl_add_u32 v143, v143, 1, v13\n"
    "v_lshlrev_b32 v143, 7, v143\n"
    "s_min_u32 s3, s59, 6\n"
    "v_bfe_u32 v14, v1, 1, 1\n"
    "v_lshl_add_u32 v14, s3, 1, v14\n"
    "v_min_u32 v9, 13, v8\n"
    "v_and_b32 v12, 4, v14\n"
    "v_lshlrev_b32 v12, 1, v12\n"
    "v_xor_b32 v9, v9, v12\n"
    "v_and_b32 v8, 3, v9\n"
    "v_and_b32 v13, 7, v14\n"
    "v_lshl_add_u32 v8, v13, 2, v8\n"
    "v_lshrrev_b32 v13, 2, v9\n"
    "v_lshl_add_u32 v8, v8, 2, v13\n"
    "v_lshrrev_b32 v13, 3, v14\n"
    "v_lshl_add_u32 v8, v8, 1, v13\n"
    "v_lshlrev_b32 v8, 7, v8\n"
    "v_cndmask_b32 v138, v138, v8, s[82:83]\n"
    "s_cmp_eq_u32 s76, 3\n"
    "s_cbranch_scc0 .Lfb_L36\n"
    "v_lshlrev_b32 v96, 16, v116\n"
    "v_and_b32 v97, 0xffff0000, v116\n"
    "v_lshlrev_b32 v98, 16, v117\n"
    "v_and_b32 v99, 0xffff0000, v117\n"
    "v_lshlrev_b32 v100, 16, v118\n"
    "v_and_b32 v101, 0xffff0000, v118\n"
    "v_lshlrev_b32 v102, 16, v119\n"
    "v_and_b32 v103, 0xffff0000, v119\n"
    "v_mul_f32 v104, 0xbfb8aa3b, v96\n"
    "v_mul_f32 v105, 0xbfb8aa3b, v97\n"
    "v_mul_f32 v106, 0xbfb8aa3b, v98\n"
    "v_mul_f32 v107, 0xbfb8aa3b, v99\n"
    "v_mul_f32 v108, 0xbfb8aa3b, v100\n"
    "v_mul_f32 v109, 0xbfb8aa3b, v101\n"
    "v_mul_f32 v110, 0xbfb8aa3b, v102\n"
    "v_mul_f32 v111, 0xbfb8aa3b, v103\n"
    "v_exp_f32 v104, v104\n"
    "v_exp_f32 v105, v105\n"
    "v_exp_f32 v106, v106\n"
    "v_exp_f32 v107, v107\n"
    "v_exp_f32 v108, v108\n"
    "v_exp_f32 v109, v109\n"
    "v_exp_f32 v110, v110\n"
    "v_exp_f32 v111, v111\n"
    "v_add_f32 v104, 1.0, v104\n"
    "v_add_f32 v105, 1.0, v105\n"
    "v_add_f32 v106, 1.0, v106\n"
    "v_add_f32 v107, 1.0, v107\n"
    "v_add_f32 v108, 1.0, v108\n"
    "v_add_f32 v109, 1.0, v109\n"
    "v_add_f32 v110, 1.0, v110\n"
    "v_add_f32 v111, 1.0, v111\n"
    "v_rcp_f32 v104, v104\n"
    "v_rcp_f32 v105, v105\n"
    "v_rcp_f32 v106, v106\n"
    "v_rcp_f32 v107, v107\n"
    "v_rcp_f32 v108, v108\n"
    "v_rcp_f32 v109, v109\n"
    "v_rcp_f32 v110, v110\n"
    "v_rcp_f32 v111, v111\n"
    "v_mul_f32 v96, v96, v104\n"
    "v_mul_f32 v97, v97, v105\n"
    "v_mul_f32 v98, v98, v106\n"
    "v_mul_f32 v99, v99, v107\n"
    "v_mul_f32 v100, v100, v108\n"
    "v_mul_f32 v101, v101, v109\n"
    "v_mul_f32 v102, v102, v110\n"
    "v_mul_f32 v103, v103, v111\n"
    "v_cvt_pk_bf16_f32 v116, v96, v97\n"
    "v_cvt_pk_bf16_f32 v117, v98, v99\n"
    "v_cvt_pk_bf16_f32 v118, v100, v101\n"
    "v_cvt_pk_bf16_f32 v119, v102, v103\n"
    "s_lshl1_add_u32 s3, s31, 160768\n"
    "v_lshl_add_u32 v120, v1, 2, s3\n"
    ".Lfb_L36:\n"
    "s_waitcnt vmcnt(4)\n"
    "v_xor_b32 v12, s53, v17\n"
    "v_bitop3_b32 v12, v19, s53, v12 bitop3:0xbe\n"
    "s_waitcnt vmcnt(3)\n"
    "v_bitop3_b32 v12, v21, s53, v12 bitop3:0xbe\n"
    "v_bitop3_b32 v12, v23, s53, v12 bitop3:0xbe\n"
    "s_waitcnt vmcnt(2)\n"
    "v_bitop3_b32 v12, v25, s53, v12 bitop3:0xbe\n"
    "v_bitop3_b32 v12, v27, s53, v12 bitop3:0xbe\n"
    "s_waitcnt vmcnt(1)\n"
    "v_bitop3_b32 v12, v29, s53, v12 bitop3:0xbe\n"
    "v_bitop3_b32 v12, v31, s53, v12 bitop3:0xbe\n"
    "s_waitcnt vmcnt(0)\n"
    "v_bitop3_b32 v12, v32, s53, v12 bitop3:0xbe\n"
    "v_cmp_ne_u32 vcc, 0, v12\n"
    "s_cbranch_vccnz .Lfb_S37\n"
    ".Lfb_B38:\n"
    "v_add_f32 v16, v16, v24\n"
    "v_add_f32 v18, v18, v26\n"
    "v_cvt_pk_bf16_f32 v34, v16, v18\n"
    "v_lshlrev_b32 v16, 16, v34\n"
    "v_and_b32 v18, 0xffff0000, v34\n"
    "v_add_f32 v16, s99, v16\n"
    "v_add_f32 v18, s99, v18\n"
    "v_cvt_pk_bf16_f32 v34, v16, v18\n"
    "v_add_f32 v20, v20, v28\n"
    "v_add_f32 v22, v22, v30\n"
    "v_cvt_pk_bf16_f32 v35, v20, v22\n"
    "v_lshlrev_b32 v20, 16, v35\n"
    "v_and_b32 v22, 0xffff0000, v35\n"
    "v_add_f32 v20, s99, v20\n"
    "v_add_f32 v22, s99, v22\n"
    "v_cvt_pk_bf16_f32 v35, v20, v22\n"
    "s_cmp_ge_u32 s52, 6\n"
    "s_cbranch_scc0 .Lfb_L41\n"
    "v_and_b32 v36, 0xffff, v34\n"
    "v_and_b32 v38, 0xffff0000, v34\n"
    "v_and_b32 v41, 0xffff, v35\n"
    "v_and_b32 v43, 0xffff0000, v35\n"
    "ds_write2_b64 v123, v[36:37], v[38:39] offset1:2\n"
    "ds_write2_b64 v123, v[40:41], v[42:43] offset0:4 offset1:6\n"
    ".Lfb_L41:\n"
    "ds_write_b64 v141, v[34:35]\n"
    "s_setprio 0\n"
    "s_branch .Lfb_L42\n"
    ".Lfb_L34:\n"
    "s_waitcnt lgkmcnt(12)\n"
    "v_mfma_f32_16x16x32_bf16 v[48:51], v[176:179], v[176:179], 0\n"
    "v_mfma_f32_16x16x32_bf16 v[52:55], v[124:127], v[176:179], 0\n"
    "v_mfma_f32_16x16x32_bf16 v[48:51], v[180:183], v[180:183], v[48:51]\n"
    "v_mfma_f32_16x16x32_bf16 v[52:55], v[124:127], v[180:183], v[52:55]\n"
    "s_waitcnt lgkmcnt(10)\n"
    "v_mfma_f32_16x16x32_bf16 v[48:51], v[184:187], v[184:187], v[48:51]\n"
    "v_mfma_f32_16x16x32_bf16 v[52:55], v[124:127], v[184:187], v[52:55]\n"
    "v_mfma_f32_16x16x32_bf16 v[48:51], v[188:191], v[188:191], v[48:51]\n"
    "v_mfma_f32_16x16x32_bf16 v[52:55], v[124:127], v[188:191], v[52:55]\n"
    "s_waitcnt lgkmcnt(8)\n"
    "v_mfma_f32_16x16x32_bf16 v[48:51], v[192:195], v[192:195], v[48:51]\n"
    "v_mfma_f32_16x16x32_bf16 v[52:55], v[124:127], v[192:195], v[52:55]\n"
    "v_mfma_f32_16x16x32_bf16 v[48:51], v[196:199], v[196:199], v[48:51]\n"
    "v_mfma_f32_16x16x32_bf16 v[52:55], v[124:127], v[196:199], v[52:55]\n"
    "s_waitcnt lgkmcnt(6)\n"
    "v_mfma_f32_16x16x32_bf16 v[48:51], v[200:203], v[200:203], v[48:51]\n"
    "v_mfma_f32_16x16x32_bf16 v[52:55], v[124:127], v[200:203], v[52:55]\n"
    "v_mfma_f32_16x16x32_bf16 v[48:51], v[204:207], v[204:207], v[48:51]\n"
    "v_mfma_f32_16x16x32_bf16 v[52:55], v[124:127], v[204:207], v[52:55]\n"
    "s_waitcnt lgkmcnt(4)\n"
    "v_mfma_f32_16x16x32_bf16 v[48:51], v[208:211], v[208:211], v[48:51]\n"
    "v_mfma_f32_16x16x32_bf16 v[52:55], v[124:127], v[208:211], v[52:55]\n"
    "v_mfma_f32_16x16x32_bf16 v[48:51], v[212:215], v[212:215], v[48:51]\n"
    "v_mfma_f32_16x16x32_bf16 v[52:55], v[124:127], v[212:215], v[52:55]\n"
    "s_waitcnt lgkmcnt(2)\n"
    "v_mfma_f32_16x16x32_bf16 v[48:51], v[216:219], v[216:219], v[48:51]\n"
    "v_mfma_f32_16x16x32_bf16 v[52:55], v[124:127], v[216:219], v[52:55]\n"
    "v_mfma_f32_16x16x32_bf16 v[48:51], v[220:223], v[220:223], v[48:51]\n"
    "v_mfma_f32_16x16x32_bf16 v[52:55], v[124:127], v[220:223], v[52:55]\n"
    "s_waitcnt lgkmcnt(0)\n"
    "v_mfma_f32_16x16x32_bf16 v[48:51], v[224:227], v[224:227], v[48:51]\n"
    "v_mfma_f32_16x16x32_bf16 v[52:55], v[124:127], v[224:227], v[52:55]\n"
    "v_mfma_f32_16x16x32_bf16 v[48:51], v[228:231], v[228:231], v[48:51]\n"
    "v_mfma_f32_16x16x32_bf16 v[52:55], v[124:127], v[228:231], v[52:55]\n"
    "v_cmp_eq_u32 vcc, 1, v7\n"
    "v_cmp_eq_u32 s[40:41], 2, v7\n"
    "v_cmp_eq_u32 s[42:43], 3, v7\n"
    "v_cmp_eq_u32 s[84:85], v4, v6\n"
    "s_nop 2\n"
    "v_cndmask_b32 v8, v48, v49, vcc\n"
    "v_cndmask_b32 v8, v8, v50, s[40:41]\n"
    "v_cndmask_b32 v8, v8, v51, s[42:43]\n"
    "v_cndmask_b32 v9, 0, v8, s[84:85]\n"
    "v_cndmask_b32 v8, 0, v8, s[84:85]\n"
    "v_mul_f32 v12, 0x3b124925, v52\n"
    "v_cvt_pk_bf16_f32 v232, -v12, -v12\n"
    "v_permlane32_swap_b32 v8, v9\n"
    "v_add_f32 v8, v8, v9\n"
    "v_mov_b32 v9, v8\n"
    "v_mov_b32 v234, v232\n"
    "s_nop 0\n"
    "v_permlane16_swap_b32 v8, v9\n"
    "v_add_f32 v8, v8, v9\n"
    "v_mul_f32 v8, 0x3b124925, v8\n"
    "v_fma_f32 v8, -v12, v12, v8\n"
    "v_max_f32 v8, 0, v8\n"
    "v_add_f32 v8, 0x3727c5ac, v8\n"
    "v_sqrt_f32 v8, v8\n"
    "s_nop 0\n"
    "v_cvt_pk_bf16_f32 v233, v8, v8\n"
    "v_lshlrev_b32 v8, 16, v233\n"
    "v_mov_b32 v235, v233\n"
    "v_rcp_f32 v236, v8\n"
    "s_cmp_eq_u32 s76, 3\n"
    "s_cbranch_scc0 .Lfb_L43\n"
    "s_waitcnt vmcnt(0)\n"
    "v_lshlrev_b32 v96, 16, v116\n"
    "v_and_b32 v97, 0xffff0000, v116\n"
    "v_lshlrev_b32 v98, 16, v117\n"
    "v_and_b32 v99, 0xffff0000, v117\n"
    "v_lshlrev_b32 v100, 16, v118\n"
    "v_and_b32 v101, 0xffff0000, v118\n"
    "v_lshlrev_b32 v102, 16, v119\n"
    "v_and_b32 v103, 0xffff0000, v119\n"
    "v_mul_f32 v104, 0xbfb8aa3b, v96\n"
    "v_mul_f32 v105, 0xbfb8aa3b, v97\n"
    "v_mul_f32 v106, 0xbfb8aa3b, v98\n"
    "v_mul_f32 v107, 0xbfb8aa3b, v99\n"
    "v_mul_f32 v108, 0xbfb8aa3b, v100\n"
    "v_mul_f32 v109, 0xbfb8aa3b, v101\n"
    "v_mul_f32 v110, 0xbfb8aa3b, v102\n"
    "v_mul_f32 v111, 0xbfb8aa3b, v103\n"
    "v_exp_f32 v104, v104\n"
    "v_exp_f32 v105, v105\n"
    "v_exp_f32 v106, v106\n"
    "v_exp_f32 v107, v107\n"
    "v_exp_f32 v108, v108\n"
    "v_exp_f32 v109, v109\n"
    "v_exp_f32 v110, v110\n"
    "v_exp_f32 v111, v111\n"
    "v_add_f32 v104, 1.0, v104\n"
    "v_add_f32 v105, 1.0, v105\n"
    "v_add_f32 v106, 1.0, v106\n"
    "v_add_f32 v107, 1.0, v107\n"
    "v_add_f32 v108, 1.0, v108\n"
    "v_add_f32 v109, 1.0, v109\n"
    "v_add_f32 v110, 1.0, v110\n"
    "v_add_f32 v111, 1.0, v111\n"
    "v_rcp_f32 v104, v104\n"
    "v_rcp_f32 v105, v105\n"
    "v_rcp_f32 v106, v106\n"
    "v_rcp_f32 v107, v107\n"
    "v_rcp_f32 v108, v108\n"
    "v_rcp_f32 v109, v109\n"
    "v_rcp_f32 v110, v110\n"
    "v_rcp_f32 v111, v111\n"
    "v_mul_f32 v96, v96, v104\n"
    "v_mul_f32 v97, v97, v105\n"
    "v_mul_f32 v98, v98, v106\n"
    "v_mul_f32 v99, v99, v107\n"
    "v_mul_f32 v100, v100, v108\n"
    "v_mul_f32 v101, v101, v109\n"
    "v_mul_f32 v102, v102, v110\n"
    "v_mul_f32 v103, v103, v111\n"
    "v_cvt_pk_bf16_f32 v116, v96, v97\n"
    "v_cvt_pk_bf16_f32 v117, v98, v99\n"
    "v_cvt_pk_bf16_f32 v118, v100, v101\n"
    "v_cvt_pk_bf16_f32 v119, v102, v103\n"
    "s_lshl1_add_u32 s3, s31, 160768\n"
    "v_lshl_add_u32 v120, v1, 2, s3\n"
    ".Lfb_L43:\n"
    "s_cmp_lt_u32 s52, 2\n"
    "s_cbranch_scc0 .Lfb_L44\n"
    "v_mul_u32_u24 v8, 112, v4\n"
    "v_mad_u32_u24 v8, v6, 7, v8\n"
    "v_add_u32 v8, v8, v7\n"
    "s_lshl_b32 s3, s52, 11\n"
    "s_add_u32 s4, s3, 150528\n"
    "v_lshl_add_u32 v8, v8, 1, s4\n"
    "s_add_u32 s4, s3, 154624\n"
    "v_add_u32 v9, s4, v2\n"
    "s_waitcnt vmcnt(0)\n"
    "ds_read_u16 v87, v8 offset:1064\n"
    "ds_read_u16 v86, v8 offset:1008\n"
    "ds_read_u16 v85, v8 offset:952\n"
    "ds_read_u16 v84, v8 offset:896\n"
    "ds_read_u16 v83, v8 offset:168\n"
    "ds_read_u16 v82, v8 offset:112\n"
    "ds_read_u16 v81, v8 offset:56\n"
    "ds_read_u16 v80, v8 offset:0\n"
    "s_waitcnt lgkmcnt(0)\n"
    "v_lshl_or_b32 v96, v81, 16, v80\n"
    "v_lshl_or_b32 v97, v83, 16, v82\n"
    "v_lshl_or_b32 v98, v85, 16, v84\n"
    "v_lshl_or_b32 v99, v87, 16, v86\n"
    "ds_read_u16 v95, v8 offset:1072\n"
    "ds_read_u16 v94, v8 offset:1016\n"
    "ds_read_u16 v93, v8 offset:960\n"
    "ds_read_u16 v92, v8 offset:904\n"
    "ds_read_u16 v91, v8 offset:176\n"
    "ds_read_u16 v90, v8 offset:120\n"
    "ds_read_u16 v89, v8 offset:64\n"
    "ds_read_u16 v88, v8 offset:8\n"
    "s_waitcnt lgkmcnt(0)\n"
    "v_lshl_or_b32 v100, v89, 16, v88\n"
    "v_lshl_or_b32 v101, v91, 16, v90\n"
    "v_lshl_or_b32 v102, v93, 16, v92\n"
    "v_lshl_or_b32 v103, v95, 16, v94\n"
    "v_cmp_ne_u32 vcc, 3, v7\n"
    "v_cndmask_b32 v98, v98, 0, s[82:83]\n"
    "v_cndmask_b32 v99, v99, 0, s[82:83]\n"
    "v_cndmask_b32 v102, v102, 0, s[82:83]\n"
    "v_cndmask_b32 v103, v103, 0, s[82:83]\n"
    "v_cndmask_b32 v100, 0, v100, vcc\n"
    "v_cndmask_b32 v101, 0, v101, vcc\n"
    "v_cndmask_b32 v102, 0, v102, vcc\n"
    "v_cndmask_b32 v103, 0, v103, vcc\n"
    "ds_write_b128 v9, v[96:99]\n"
    "ds_write_b128 v9, v[100:103] offset:1024\n"
    ".Lfb_L44:\n"
    ".Lfb_L42:\n"
    "s_waitcnt lgkmcnt(0)\n"
    "s_barrier\n"
    "s_cmp_lt_u32 s52, 7\n"
    "s_cbranch_scc0 .Lfb_L45\n"
    "s_add_u32 m0, s30, 0\n"
    "s_nop 0\n"
    "buffer_load_dwordx4 v2, s[8:11], s68 offen sc1 lds\n"
    "buffer_load_dwordx4 v2, s[8:11], s68 offen offset:1024 sc1 lds\n"
    "buffer_load_dwordx4 v2, s[8:11], s68 offen offset:2048 sc1 lds\n"
    "buffer_load_dwordx4 v2, s[8:11], s68 offen offset:3072 sc1 lds\n"
    "s_branch .Lfb_L46\n"
    ".Lfb_L45:\n"
    "s_mul_i32 s3, s62, 7\n"
    "s_lshr_b32 s3, s3, 2\n"
    "v_min_u32 v10, 2, v4\n"
    "v_add_u32 v10, s3, v10\n"
    "v_min_u32 v10, 6, v10\n"
    "v_lshl_add_u32 v10, v5, 4, v10\n"
    "v_lshlrev_b32 v10, 7, v10\n"
    "v_add_u32 v10, 81920, v10\n"
    "buffer_load_dword v11, v10, s[8:11], 0 offen sc1\n"
    "v_mov_b32 v8, 0\n"
    "v_mov_b32 v9, 0\n"
    "s_mov_b64 exec, s[82:83]\n"
    "ds_write2st64_b64 v247, v[8:9], v[8:9] offset0:0 offset1:2\n"
    "ds_write2st64_b64 v247, v[8:9], v[8:9] offset0:60 offset1:62\n"
    "ds_write2st64_b64 v247, v[8:9], v[8:9] offset0:120 offset1:122\n"
    "s_mov_b64 exec, -1\n"
    ".Lfb_L46:\n"
    "ds_read_b64 v[20:21], v251 offset:8\n"
    "ds_read_b64 v[16:17], v251\n"
    "ds_read_b64 v[26:27], v251 offset:280\n"
    "ds_read_b64 v[22:23], v251 offset:272\n"
    "ds_read_b64 v[32:33], v251 offset:552\n"
    "ds_read_b64 v[28:29], v251 offset:544\n"
    "ds_read_b64 v[38:39], v251 offset:824\n"
    "ds_read_b64 v[34:35], v251 offset:816\n"
    "v_mov_b32 v18, 0\n"
    "v_mov_b32 v19, 0\n"
    "v_mov_b32 v24, 0\n"
    "v_mov_b32 v25, 0\n"
    "v_mov_b32 v30, 0\n"
    "v_mov_b32 v31, 0\n"
    "v_mov_b32 v36, 0\n"
    "v_mov_b32 v37, 0\n"
    "s_waitcnt lgkmcnt(6)\n"
    "v_mfma_f32_16x16x32_bf16 v[64:67], v[16:19], v[176:179], 0\n"
    "v_mfma_f32_16x16x32_bf16 v[68:71], v[18:21], v[176:179], 0\n"
    "s_waitcnt lgkmcnt(4)\n"
    "v_mfma_f32_16x16x32_bf16 v[72:75], v[22:25], v[180:183], 0\n"
    "ds_read_b64 v[20:21], v251 offset:1096\n"
    "s_nop 3\n"
    "v_cvt_pk_bf16_f32 v176, v64, v65\n"
    "v_cvt_pk_bf16_f32 v177, v66, v67\n"
    "v_mfma_f32_16x16x32_bf16 v[76:79], v[24:27], v[180:183], 0\n"
    "ds_read_b64 v[16:17], v251 offset:1088\n"
    "s_waitcnt lgkmcnt(4)\n"
    "v_cvt_pk_bf16_f32 v178, v68, v69\n"
    "v_cvt_pk_bf16_f32 v179, v70, v71\n"
    "v_mfma_f32_16x16x32_bf16 v[64:67], v[28:31], v[184:187], 0\n"
    "ds_read_b64 v[26:27], v251 offset:1368\n"
    "v_cvt_pk_bf16_f32 v180, v72, v73\n"
    "v_cvt_pk_bf16_f32 v181, v74, v75\n"
    "v_mfma_f32_16x16x32_bf16 v[68:71], v[30:33], v[184:187], 0\n"
    "ds_read_b64 v[22:23], v251 offset:1360\n"
    "s_waitcnt lgkmcnt(4)\n"
    "v_cvt_pk_bf16_f32 v182, v76, v77\n"
    "v_cvt_pk_bf16_f32 v183, v78, v79\n"
    "v_mfma_f32_16x16x32_bf16 v[72:75], v[34:37], v[188:191], 0\n"
    "ds_read_b64 v[32:33], v251 offset:1640\n"
    "v_cvt_pk_bf16_f32 v184, v64, v65\n"
    "v_cvt_pk_bf16_f32 v185, v66, v67\n"
    "v_mfma_f32_16x16x32_bf16 v[76:79], v[36:39], v[188:191], 0\n"
    "ds_read_b64 v[28:29], v251 offset:1632\n"
    "s_waitcnt lgkmcnt(4)\n"
    "v_cvt_pk_bf16_f32 v186, v68, v69\n"
    "v_cvt_pk_bf16_f32 v187, v70, v71\n"
    "s_cmp_lt_u32 s52, 7\n"
    "s_cbranch_scc0 .Lfb_L47\n"
    "s_add_u32 m0, s30, 30720\n"
    "s_add_u32 s68, s68, 28672\n"
    "buffer_load_dwordx4 v2, s[8:11], s68 offen sc1 lds\n"
    "buffer_load_dwordx4 v2, s[8:11], s68 offen offset:1024 sc1 lds\n"
    ".Lfb_L47:\n"
    "v_mfma_f32_16x16x32_bf16 v[64:67], v[16:19], v[192:195], 0\n"
    "ds_read_b64 v[38:39], v251 offset:1912\n"
    "v_cvt_pk_bf16_f32 v188, v72, v73\n"
    "v_cvt_pk_bf16_f32 v189, v74, v75\n"
    "v_mfma_f32_16x16x32_bf16 v[68:71], v[18:21], v[192:195], 0\n"
    "ds_read_b64 v[34:35], v251 offset:1904\n"
    "s_waitcnt lgkmcnt(4)\n"
    "v_cvt_pk_bf16_f32 v190, v76, v77\n"
    "v_cvt_pk_bf16_f32 v191, v78, v79\n"
    "v_mfma_f32_16x16x32_bf16 v[72:75], v[22:25], v[196:199], 0\n"
    "ds_read_b64 v[20:21], v251 offset:2184\n"
    "v_cvt_pk_bf16_f32 v192, v64, v65\n"
    "v_cvt_pk_bf16_f32 v193, v66, v67\n"
    "v_mfma_f32_16x16x32_bf16 v[76:79], v[24:27], v[196:199], 0\n"
    "ds_read_b64 v[16:17], v251 offset:2176\n"
    "s_waitcnt lgkmcnt(4)\n"
    "v_cvt_pk_bf16_f32 v194, v68, v69\n"
    "v_cvt_pk_bf16_f32 v195, v70, v71\n"
    "v_mfma_f32_16x16x32_bf16 v[64:67], v[28:31], v[200:203], 0\n"
    "ds_read_b64 v[26:27], v251 offset:2456\n"
    "v_cvt_pk_bf16_f32 v196, v72, v73\n"
    "v_cvt_pk_bf16_f32 v197, v74, v75\n"
    "v_mfma_f32_16x16x32_bf16 v[68:71], v[30:33], v[200:203], 0\n"
    "ds_read_b64 v[22:23], v251 offset:2448\n"
    "s_waitcnt lgkmcnt(4)\n"
    "v_cvt_pk_bf16_f32 v198, v76, v77\n"
    "v_cvt_pk_bf16_f32 v199, v78, v79\n"
    "s_cmp_lt_u32 s52, 7\n"
    "s_cbranch_scc0 .Lfb_L48\n"
    "buffer_load_dwordx4 v2, s[8:11], s68 offen offset:2048 sc1 lds\n"
    "buffer_load_dwordx4 v2, s[8:11], s68 offen offset:3072 sc1 lds\n"
    ".Lfb_L48:\n"
    "v_mfma_f32_16x16x32_bf16 v[72:75], v[34:37], v[204:207], 0\n"
    "ds_read_b64 v[32:33], v251 offset:2728\n"
    "v_cvt_pk_bf16_f32 v200, v64, v65\n"
    "v_cvt_pk_bf16_f32 v201, v66, v67\n"
    "v_mfma_f32_16x16x32_bf16 v[76:79], v[36:39], v[204:207], 0\n"
    "ds_read_b64 v[28:29], v251 offset:2720\n"
    "s_waitcnt lgkmcnt(4)\n"
    "v_cvt_pk_bf16_f32 v202, v68, v69\n"
    "v_cvt_pk_bf16_f32 v203, v70, v71\n"
    "v_mfma_f32_16x16x32_bf16 v[64:67], v[16:19], v[208:211], 0\n"
    "ds_read_b64 v[38:39], v251 offset:3000\n"
    "v_cvt_pk_bf16_f32 v204, v72, v73\n"
    "v_cvt_pk_bf16_f32 v205, v74, v75\n"
    "v_mfma_f32_16x16x32_bf16 v[68:71], v[18:21], v[208:211], 0\n"
    "ds_read_b64 v[34:35], v251 offset:2992\n"
    "s_waitcnt lgkmcnt(4)\n"
    "v_cvt_pk_bf16_f32 v206, v76, v77\n"
    "v_cvt_pk_bf16_f32 v207, v78, v79\n"
    "v_mfma_f32_16x16x32_bf16 v[72:75], v[22:25], v[212:215], 0\n"
    "ds_read_b64 v[20:21], v251 offset:3272\n"
    "v_cvt_pk_bf16_f32 v208, v64, v65\n"
    "v_cvt_pk_bf16_f32 v209, v66, v67\n"
    "v_mfma_f32_16x16x32_bf16 v[76:79], v[24:27], v[212:215], 0\n"
    "ds_read_b64 v[16:17], v251 offset:3264\n"
    "s_waitcnt lgkmcnt(4)\n"
    "v_cvt_pk_bf16_f32 v210, v68, v69\n"
    "v_cvt_pk_bf16_f32 v211, v70, v71\n"
    "s_cmp_lt_u32 s52, 7\n"
    "s_cbranch_scc0 .Lfb_L49\n"
    "s_add_u32 m0, s30, 61440\n"
    "s_add_u32 s68, s68, 28672\n"
    "buffer_load_dwordx4 v2, s[8:11], s68 offen sc1 lds\n"
    "buffer_load_dwordx4 v2, s[8:11], s68 offen offset:1024 sc1 lds\n"
    ".Lfb_L49:\n"
    "v_mfma_f32_16x16x32_bf16 v[64:67], v[28:31], v[216:219], 0\n"
    "ds_read_b64 v[26:27], v251 offset:3544\n"
    "v_cvt_pk_bf16_f32 v212, v72, v73\n"
    "v_cvt_pk_bf16_f32 v213, v74, v75\n"
    "v_mfma_f32_16x16x32_bf16 v[68:71], v[30:33], v[216:219], 0\n"
    "ds_read_b64 v[22:23], v251 offset:3536\n"
    "s_waitcnt lgkmcnt(4)\n"
    "v_cvt_pk_bf16_f32 v214, v76, v77\n"
    "v_cvt_pk_bf16_f32 v215, v78, v79\n"
    "v_mfma_f32_16x16x32_bf16 v[72:75], v[34:37], v[220:223], 0\n"
    "v_cvt_pk_bf16_f32 v216, v64, v65\n"
    "v_cvt_pk_bf16_f32 v217, v66, v67\n"
    "v_mfma_f32_16x16x32_bf16 v[76:79], v[36:39], v[220:223], 0\n"
    "s_waitcnt lgkmcnt(2)\n"
    "v_cvt_pk_bf16_f32 v218, v68, v69\n"
    "v_cvt_pk_bf16_f32 v219, v70, v71\n"
    "ds_read_b128 v[144:147], v142\n"
    "ds_read_b128 v[148:151], v142 offset:64\n"
    "v_mfma_f32_16x16x32_bf16 v[64:67], v[16:19], v[224:227], 0\n"
    "v_cvt_pk_bf16_f32 v220, v72, v73\n"
    "v_cvt_pk_bf16_f32 v221, v74, v75\n"
    "v_mfma_f32_16x16x32_bf16 v[68:71], v[18:21], v[224:227], 0\n"
    "s_waitcnt lgkmcnt(2)\n"
    "v_cvt_pk_bf16_f32 v222, v76, v77\n"
    "v_cvt_pk_bf16_f32 v223, v78, v79\n"
    "s_cmp_lt_u32 s52, 7\n"
    "s_cbranch_scc0 .Lfb_L50\n"
    "buffer_load_dwordx4 v2, s[8:11], s68 offen offset:2048 sc1 lds\n"
    "buffer_load_dwordx4 v2, s[8:11], s68 offen offset:3072 sc1 lds\n"
    ".Lfb_L50:\n"
    "v_mfma_f32_16x16x32_bf16 v[72:75], v[22:25], v[228:231], 0\n"
    "v_cvt_pk_bf16_f32 v224, v64, v65\n"
    "v_cvt_pk_bf16_f32 v225, v66, v67\n"
    "v_mfma_f32_16x16x32_bf16 v[76:79], v[24:27], v[228:231], 0\n"
    "v_cvt_pk_bf16_f32 v226, v68, v69\n"
    "v_cvt_pk_bf16_f32 v227, v70, v71\n"
    "s_nop 2\n"
    "v_cvt_pk_bf16_f32 v228, v72, v73\n"
    "v_cvt_pk_bf16_f32 v229, v74, v75\n"
    "s_nop 0\n"
    "v_cvt_pk_bf16_f32 v230, v76, v77\n"
    "v_cvt_pk_bf16_f32 v231, v78, v79\n"
    "v_mul_f32 v12, 0x3fb8aa3b, v236\n"
    "s_cmp_eq_u32 s52, 7\n"
    "s_cbranch_scc0 .Lfb_L51\n"
    "s_waitcnt vmcnt(0)\n"
    "v_xor_b32 v13, s53, v11\n"
    "v_cmp_ne_u32 vcc, 0, v13\n"
    "s_cbranch_vccnz .Lfb_S52\n"
    ".Lfb_B53:\n"
    ".Lfb_L51:\n"
    "s_cmp_eq_u32 s52, 7\n"
    "s_cbranch_scc0 .Lfb_L56\n"
    ".Lfb_L56:\n"
    "s_waitcnt vmcnt(8) lgkmcnt(0)\n"
    "s_barrier\n"
    "ds_read_b128 v[16:19], v2\n"
    "ds_read_b128 v[20:23], v2 offset:1024\n"
    "ds_read_b128 v[24:27], v2 offset:2048\n"
    "ds_read_b128 v[28:31], v2 offset:3072\n"
    "ds_read_b128 v[32:35], v2 offset:4096\n"
    "ds_read_b128 v[36:39], v2 offset:5120\n"
    "ds_read_b128 v[40:43], v2 offset:6144\n"
    "ds_read_b128 v[44:47], v2 offset:7168\n"
    "ds_read_b128 v[164:167], v245 offset:3072\n"
    "ds_read_b128 v[160:163], v245 offset:2048\n"
    "ds_read_b128 v[156:159], v245 offset:1024\n"
    "ds_read_b128 v[152:155], v245\n"
    "s_waitcnt lgkmcnt(11)\n"
    "v_mfma_f32_16x16x32_bf16 v[56:59], v[16:19], v[176:179], 0\n"
    "ds_read_b128 v[16:19], v2 offset:8192\n"
    "s_waitcnt lgkmcnt(11)\n"
    "v_mfma_f32_16x16x32_bf16 v[60:63], v[20:23], v[176:179], 0\n"
    "ds_read_b128 v[20:23], v2 offset:9216\n"
    "s_waitcnt lgkmcnt(10)\n"
    "v_mov_b32 v124, -1\n"
    "v_mov_b32 v125, -1\n"
    "v_mov_b32 v126, -1\n"
    "v_mov_b32 v127, -1\n"
    "s_cmp_eq_u32 s52, 7\n"
    "s_cbranch_scc0 .Lfb_L57\n"
    "s_lshl_b32 s3, s2, 7\n"
    "s_add_u32 s3, s3, 180224\n"
    "s_mov_b64 exec, 1\n"
    "v_mov_b32 v14, s53\n"
    "buffer_store_dword v14, off, s[8:11], s3 sc1\n"
    "s_mov_b64 exec, -1\n"
    ".Lfb_L57:\n"
    "buffer_store_dwordx4 v[124:127], v2, s[8:11], s67 offen sc1\n"
    "s_or_b64 exec, s[80:81], 0xffff\n"
    "buffer_store_dwordx4 v[124:127], v2, s[8:11], s69 offen sc1\n"
    "s_mov_b64 exec, -1\n"
    "v_mfma_f32_16x16x32_bf16 v[56:59], v[24:27], v[180:183], v[56:59]\n"
    "ds_read_b128 v[24:27], v2 offset:10240\n"
    "v_mfma_f32_16x16x32_bf16 v[60:63], v[28:31], v[180:183], v[60:63]\n"
    "ds_read_b128 v[28:31], v2 offset:11264\n"
    "s_waitcnt lgkmcnt(10)\n"
    "v_mfma_f32_16x16x32_bf16 v[56:59], v[32:35], v[184:187], v[56:59]\n"
    "ds_read_b128 v[32:35], v2 offset:12288\n"
    "v_mfma_f32_16x16x32_bf16 v[60:63], v[36:39], v[184:187], v[60:63]\n"
    "ds_read_b128 v[36:39], v2 offset:13312\n"
    "s_waitcnt lgkmcnt(6)\n"
    "v_mfma_f32_16x16x32_bf16 v[64:67], v[144:147], v[152:155], 0\n"
    "v_mfma_f32_16x16x32_bf16 v[68:71], v[144:147], v[156:159], 0\n"
    "v_mfma_f32_16x16x32_bf16 v[64:67], v[148:151], v[160:163], v[64:67]\n"
    "v_mfma_f32_16x16x32_bf16 v[68:71], v[148:151], v[164:167], v[68:71]\n"
    "v_mfma_f32_16x16x32_bf16 v[56:59], v[40:43], v[188:191], v[56:59]\n"
    "ds_read_b128 v[40:43], v2 offset:14336\n"
    "v_mfma_f32_16x16x32_bf16 v[60:63], v[44:47], v[188:191], v[60:63]\n"
    "ds_read_b128 v[44:47], v2 offset:15360\n"
    "s_waitcnt lgkmcnt(6)\n"
    "s_mov_b32 s3, s76\n"
    "s_cmp_lg_u32 s3, 0\n"
    "s_cbranch_scc0 .Lfb_L58\n"
    "ds_read_b64_tr_b16 v[80:81], v252\n"
    "ds_read_b64_tr_b16 v[82:83], v252 offset:512\n"
    "ds_read_b64_tr_b16 v[84:85], v252 offset:32\n"
    "ds_read_b64_tr_b16 v[86:87], v252 offset:544\n"
    "ds_read_b64_tr_b16 v[88:89], v252 offset:64\n"
    "ds_read_b64_tr_b16 v[90:91], v252 offset:576\n"
    "ds_read_b64_tr_b16 v[92:93], v252 offset:96\n"
    "ds_read_b64_tr_b16 v[94:95], v252 offset:608\n"
    ".Lfb_L58:\n"
    "v_mfma_f32_16x16x32_bf16 v[56:59], v[16:19], v[192:195], v[56:59]\n"
    "ds_read_b128 v[16:19], v2 offset:16384\n"
    "v_cvt_pk_bf16_f32 v66, 0, v64\n"
    "v_cvt_pk_bf16_f32 v67, 0, v65\n"
    "v_mfma_f32_16x16x32_bf16 v[60:63], v[20:23], v[192:195], v[60:63]\n"
    "s_waitcnt lgkmcnt(12)\n"
    "ds_read_b128 v[20:23], v2 offset:17408\n"
    "s_waitcnt lgkmcnt(6)\n"
    "v_cvt_pk_bf16_f32 v70, 0, v68\n"
    "v_cvt_pk_bf16_f32 v71, 0, v69\n"
    "v_mfma_f32_16x16x32_bf16 v[56:59], v[24:27], v[196:199], v[56:59]\n"
    "ds_read_b128 v[24:27], v2 offset:18432\n"
    "v_sub_f32 v64, v64, v66\n"
    "v_sub_f32 v65, v65, v67\n"
    "v_mfma_f32_16x16x32_bf16 v[60:63], v[28:31], v[196:199], v[60:63]\n"
    "ds_read_b128 v[28:31], v2 offset:19456\n"
    "s_waitcnt lgkmcnt(6)\n"
    "s_mov_b32 s3, s76\n"
    "s_cmp_eq_u32 s3, 1\n"
    "s_cbranch_scc0 .Lfb_L59\n"
    "s_waitcnt lgkmcnt(10)\n"
    "buffer_store_dwordx4 v[80:83], v2, s[8:11], s75 offen sc1\n"
    "s_add_u32 s75, s75, s89\n"
    "s_waitcnt lgkmcnt(8)\n"
    "buffer_store_dwordx4 v[84:87], v2, s[8:11], s75 offen sc1\n"
    "s_add_u32 s75, s75, s89\n"
    "s_waitcnt lgkmcnt(6)\n"
    "buffer_store_dwordx4 v[88:91], v2, s[8:11], s75 offen sc1\n"
    "s_add_u32 s75, s75, s89\n"
    "s_waitcnt lgkmcnt(4)\n"
    "buffer_store_dwordx4 v[92:95], v2, s[8:11], s75 offen sc1\n"
    "s_branch .Lfb_L60\n"
    ".Lfb_L59:\n"
    "s_cmp_eq_u32 s3, 3\n"
    "s_cbranch_scc0 .Lfb_L61\n"
    "v_mfma_f32_16x16x32_bf16 v[96:99], v[116:119], v[80:83], 0\n"
    "v_mfma_f32_16x16x32_bf16 v[100:103], v[116:119], v[84:87], 0\n"
    "v_mfma_f32_16x16x32_bf16 v[104:107], v[116:119], v[88:91], 0\n"
    "s_waitcnt lgkmcnt(4)\n"
    "v_mfma_f32_16x16x32_bf16 v[108:111], v[116:119], v[92:95], 0\n"
    "v_cmp_eq_u32 vcc, 1, v4\n"
    "v_cmp_eq_u32 s[40:41], 2, v4\n"
    "s_nop 2\n"
    "v_cndmask_b32 v96, v96, v100, vcc\n"
    "v_cndmask_b32 v96, v96, v104, s[40:41]\n"
    "s_nop 0\n"
    "v_cndmask_b32 v96, v96, v108, s[82:83]\n"
    "ds_write_b32 v120, v96\n"
    ".Lfb_L61:\n"
    ".Lfb_L60:\n"
    "v_sub_f32 v68, v68, v70\n"
    "v_sub_f32 v69, v69, v71\n"
    "v_mfma_f32_16x16x32_bf16 v[56:59], v[32:35], v[200:203], v[56:59]\n"
    "ds_read_b128 v[32:35], v2 offset:20480\n"
    "v_cvt_pk_bf16_f32 v64, v66, v64\n"
    "v_cvt_pk_bf16_f32 v65, v67, v65\n"
    "v_mfma_f32_16x16x32_bf16 v[60:63], v[36:39], v[200:203], v[60:63]\n"
    "ds_read_b128 v[36:39], v2 offset:21504\n"
    "s_waitcnt lgkmcnt(6)\n"
    "v_cvt_pk_bf16_f32 v68, v70, v68\n"
    "v_cvt_pk_bf16_f32 v69, v71, v69\n"
    "s_mov_b64 exec, s[78:79]\n"
    "ds_write2st64_b64 v247, v[64:65], v[68:69] offset0:0 offset1:2\n"
    "s_mov_b64 exec, -1\n"
    "v_mfma_f32_16x16x32_bf16 v[56:59], v[40:43], v[204:207], v[56:59]\n"
    "ds_read_b128 v[40:43], v2 offset:22528\n"
    "v_mfma_f32_16x16x32_bf16 v[60:63], v[44:47], v[204:207], v[60:63]\n"
    "ds_read_b128 v[44:47], v2 offset:23552\n"
    "s_waitcnt lgkmcnt(6)\n"
    "s_cmp_eq_u32 s76, 1\n"
    "s_cbranch_scc0 .Lfb_L62\n"
    "s_waitcnt vmcnt(10)\n"
    "s_branch .Lfb_L63\n"
    ".Lfb_L62:\n"
    "s_waitcnt vmcnt(6)\n"
    ".Lfb_L63:\n"
    "ds_read_b128 v[164:167], v245 offset:33792\n"
    "ds_read_b128 v[160:163], v245 offset:32768\n"
    "ds_read_b128 v[156:159], v245 offset:31744\n"
    "ds_read_b128 v[152:155], v245 offset:30720\n"
    "v_mfma_f32_16x16x32_bf16 v[56:59], v[16:19], v[208:211], v[56:59]\n"
    "ds_read_b128 v[16:19], v2 offset:24576\n"
    "v_mfma_f32_16x16x32_bf16 v[60:63], v[20:23], v[208:211], v[60:63]\n"
    "ds_read_b128 v[20:23], v2 offset:25600\n"
    "s_waitcnt lgkmcnt(10)\n"
    "v_mfma_f32_16x16x32_bf16 v[56:59], v[24:27], v[212:215], v[56:59]\n"
    "ds_read_b128 v[24:27], v2 offset:26624\n"
    "v_mfma_f32_16x16x32_bf16 v[60:63], v[28:31], v[212:215], v[60:63]\n"
    "ds_read_b128 v[28:31], v2 offset:27648\n"
    "s_waitcnt lgkmcnt(4)\n"
    "v_mfma_f32_16x16x32_bf16 v[72:75], v[144:147], v[152:155], 0\n"
    "v_mfma_f32_16x16x32_bf16 v[76:79], v[144:147], v[156:159], 0\n"
    "v_mfma_f32_16x16x32_bf16 v[72:75], v[148:151], v[160:163], v[72:75]\n"
    "v_mfma_f32_16x16x32_bf16 v[76:79], v[148:151], v[164:167], v[76:79]\n"
    "s_barrier\n"
    "v_mfma_f32_16x16x32_bf16 v[56:59], v[32:35], v[216:219], v[56:59]\n"
    "ds_read_b128 v[32:35], v2 offset:28672\n"
    "v_mfma_f32_16x16x32_bf16 v[60:63], v[36:39], v[216:219], v[60:63]\n"
    "ds_read_b128 v[36:39], v2 offset:29696\n"
    "v_mfma_f32_16x16x32_bf16 v[56:59], v[40:43], v[220:223], v[56:59]\n"
    "ds_read_b128 v[40:43], v2 offset:30720\n"
    "v_cvt_pk_bf16_f32 v74, 0, v72\n"
    "v_cvt_pk_bf16_f32 v75, 0, v73\n"
    "v_mfma_f32_16x16x32_bf16 v[60:63], v[44:47], v[220:223], v[60:63]\n"
    "ds_read_b128 v[44:47], v2 offset:31744\n"
    "s_waitcnt lgkmcnt(6)\n"
    "ds_read_b128 v[168:171], v243\n"
    "ds_read_b128 v[172:175], v243 offset:1024\n"
    "v_cvt_pk_bf16_f32 v78, 0, v76\n"
    "v_cvt_pk_bf16_f32 v79, 0, v77\n"
    "v_mfma_f32_16x16x32_bf16 v[56:59], v[16:19], v[224:227], v[56:59]\n"
    "ds_read_b128 v[16:19], v2 offset:32768\n"
    "v_sub_f32 v72, v72, v74\n"
    "v_sub_f32 v73, v73, v75\n"
    "v_mfma_f32_16x16x32_bf16 v[60:63], v[20:23], v[224:227], v[60:63]\n"
    "ds_read_b128 v[20:23], v2 offset:33792\n"
    "s_waitcnt lgkmcnt(8)\n"
    "v_sub_f32 v76, v76, v78\n"
    "v_sub_f32 v77, v77, v79\n"
    "v_mfma_f32_16x16x32_bf16 v[56:59], v[24:27], v[228:231], v[56:59]\n"
    "ds_read_b128 v[24:27], v2 offset:34816\n"
    "v_cvt_pk_bf16_f32 v72, v74, v72\n"
    "v_cvt_pk_bf16_f32 v73, v75, v73\n"
    "v_mfma_f32_16x16x32_bf16 v[60:63], v[28:31], v[228:231], v[60:63]\n"
    "ds_read_b128 v[28:31], v2 offset:35840\n"
    "s_waitcnt lgkmcnt(8)\n"
    "s_cmp_lt_u32 s52, 7\n"
    "s_cbranch_scc0 .Lfb_L64\n"
    "buffer_load_dwordx4 v[80:83], v2, s[8:11], s16 offen sc1\n"
    ".Lfb_L64:\n"
    "v_cvt_pk_bf16_f32 v76, v78, v76\n"
    "v_cvt_pk_bf16_f32 v77, v79, v77\n"
    "s_mov_b64 exec, s[78:79]\n"
    "ds_write2st64_b64 v247, v[72:73], v[76:77] offset0:60 offset1:62\n"
    "s_mov_b64 exec, -1\n"
    "v_mfma_f32_16x16x32_bf16 v[56:59], v[32:35], v[232:235], v[56:59]\n"
    "ds_read_b128 v[32:35], v2 offset:36864\n"
    "v_mfma_f32_16x16x32_bf16 v[60:63], v[36:39], v[232:235], v[60:63]\n"
    "ds_read_b128 v[36:39], v2 offset:37888\n"
    "s_waitcnt lgkmcnt(8)\n"
    "v_mfma_f32_16x16x32_bf16 v[48:51], v[40:43], v[176:179], 0\n"
    "ds_read_b128 v[40:43], v2 offset:38912\n"
    "v_mfma_f32_16x16x32_bf16 v[52:55], v[44:47], v[176:179], 0\n"
    "ds_read_b128 v[44:47], v2 offset:39936\n"
    "s_waitcnt lgkmcnt(6)\n"
    "v_cvt_pk_bf16_f32 v56, v56, v57\n"
    "v_cvt_pk_bf16_f32 v57, v58, v59\n"
    "v_mfma_f32_16x16x32_bf16 v[48:51], v[16:19], v[180:183], v[48:51]\n"
    "ds_read_b128 v[16:19], v2 offset:40960\n"
    "v_cvt_pk_bf16_f32 v58, v60, v61\n"
    "v_cvt_pk_bf16_f32 v59, v62, v63\n"
    "v_mfma_f32_16x16x32_bf16 v[52:55], v[20:23], v[180:183], v[52:55]\n"
    "ds_read_b128 v[20:23], v2 offset:41984\n"
    "s_waitcnt lgkmcnt(6)\n"
    "v_mfma_f32_16x16x32_bf16 v[64:67], v[168:171], v[56:59], 0\n"
    "v_mfma_f32_16x16x32_bf16 v[68:71], v[172:175], v[56:59], 0\n"
    "s_cmp_lt_u32 s52, 7\n"
    "s_cbranch_scc0 .Lfb_L65\n"
    "buffer_load_dwordx4 v[84:87], v2, s[8:11], s16 offen offset:1024 sc1\n"
    ".Lfb_L65:\n"
    "v_mfma_f32_16x16x32_bf16 v[48:51], v[24:27], v[184:187], v[48:51]\n"
    "ds_read_b128 v[24:27], v2 offset:43008\n"
    "v_mfma_f32_16x16x32_bf16 v[52:55], v[28:31], v[184:187], v[52:55]\n"
    "ds_read_b128 v[28:31], v2 offset:44032\n"
    "s_waitcnt lgkmcnt(6)\n"
    "v_mfma_f32_16x16x32_bf16 v[48:51], v[32:35], v[188:191], v[48:51]\n"
    "ds_read_b128 v[32:35], v2 offset:45056\n"
    "v_max3_f32 v72, v64, v65, v66\n"
    "v_max3_f32 v73, -v64, -v65, -v66\n"
    "v_max3_f32 v72, v67, v68, v72\n"
    "v_max3_f32 v73, -v67, -v68, v73\n"
    "v_mfma_f32_16x16x32_bf16 v[52:55], v[36:39], v[188:191], v[52:55]\n"
    "ds_read_b128 v[36:39], v2 offset:46080\n"
    "s_waitcnt lgkmcnt(6)\n"
    "s_mov_b32 s65, 180224\n"
    "buffer_load_dword v11, v143, s[8:11], s65 offen sc1\n"
    "v_max3_f32 v72, v69, v70, v72\n"
    "v_max3_f32 v73, -v69, -v70, v73\n"
    "v_mfma_f32_16x16x32_bf16 v[48:51], v[40:43], v[192:195], v[48:51]\n"
    "ds_read_b128 v[40:43], v2 offset:47104\n"
    "v_permlane32_swap_b32 v72, v73\n"
    "v_max_f32 v72, v72, v73\n"
    "v_mov_b32 v73, v72\n"
    "v_mfma_f32_16x16x32_bf16 v[52:55], v[44:47], v[192:195], v[52:55]\n"
    "ds_read_b128 v[44:47], v2 offset:48128\n"
    "s_waitcnt lgkmcnt(6)\n"
    "s_cmp_lt_u32 s52, 7\n"
    "s_cbranch_scc0 .Lfb_L66\n"
    "buffer_load_dwordx4 v[88:91], v2, s[8:11], s16 offen offset:2048 sc1\n"
    ".Lfb_L66:\n"
    "v_permlane16_swap_b32 v72, v73\n"
    "v_max_f32 v73, v72, v73\n"
    "v_mul_f32 v72, v12, v73\n"
    "v_mul_f32 v73, v12, v73\n"
    "v_mfma_f32_16x16x32_bf16 v[48:51], v[16:19], v[196:199], v[48:51]\n"
    "ds_read_b128 v[16:19], v2 offset:49152\n"
    "v_permlane32_swap_b32 v72, v73\n"
    "v_fma_f32 v152, v64, v12, -v72\n"
    "v_fma_f32 v160, -v64, v12, -v73\n"
    "v_fma_f32 v153, v65, v12, -v72\n"
    "v_mfma_f32_16x16x32_bf16 v[52:55], v[20:23], v[196:199], v[52:55]\n"
    "ds_read_b128 v[20:23], v2 offset:50176\n"
    "s_waitcnt lgkmcnt(6)\n"
    "v_fma_f32 v161, -v65, v12, -v73\n"
    "v_fma_f32 v154, v66, v12, -v72\n"
    "v_fma_f32 v162, -v66, v12, -v73\n"
    "v_fma_f32 v155, v67, v12, -v72\n"
    "v_mfma_f32_16x16x32_bf16 v[48:51], v[24:27], v[200:203], v[48:51]\n"
    "ds_read_b128 v[24:27], v2 offset:51200\n"
    "v_fma_f32 v163, -v67, v12, -v73\n"
    "v_fma_f32 v156, v68, v12, -v72\n"
    "v_fma_f32 v164, -v68, v12, -v73\n"
    "v_fma_f32 v157, v69, v12, -v72\n"
    "v_fma_f32 v165, -v69, v12, -v73\n"
    "v_mfma_f32_16x16x32_bf16 v[52:55], v[28:31], v[200:203], v[52:55]\n"
    "ds_read_b128 v[28:31], v2 offset:52224\n"
    "s_waitcnt lgkmcnt(6)\n"
    "s_cmp_lt_u32 s52, 7\n"
    "s_cbranch_scc0 .Lfb_L67\n"
    "buffer_load_dwordx4 v[92:95], v2, s[8:11], s16 offen offset:3072 sc1\n"
    "s_xor_b32 s16, s16, 4096\n"
    ".Lfb_L67:\n"
    "v_fma_f32 v158, v70, v12, -v72\n"
    "v_fma_f32 v166, -v70, v12, -v73\n"
    "v_exp_f32 v152, v152\n"
    "v_mfma_f32_16x16x32_bf16 v[48:51], v[32:35], v[204:207], v[48:51]\n"
    "ds_read_b128 v[32:35], v2 offset:53248\n"
    "v_exp_f32 v160, v160\n"
    "v_exp_f32 v153, v153\n"
    "v_mfma_f32_16x16x32_bf16 v[52:55], v[36:39], v[204:207], v[52:55]\n"
    "ds_read_b128 v[36:39], v2 offset:54272\n"
    "s_waitcnt lgkmcnt(6)\n"
    "v_exp_f32 v161, v161\n"
    "v_exp_f32 v154, v154\n"
    "v_mfma_f32_16x16x32_bf16 v[48:51], v[40:43], v[208:211], v[48:51]\n"
    "ds_read_b128 v[40:43], v2 offset:55296\n"
    "v_exp_f32 v162, v162\n"
    "v_add_f32 v72, v152, v153\n"
    "v_add_f32 v73, v160, v161\n"
    "v_mfma_f32_16x16x32_bf16 v[52:55], v[44:47], v[208:211], v[52:55]\n"
    "ds_read_b128 v[44:47], v2 offset:56320\n"
    "s_waitcnt lgkmcnt(6)\n"
    "s_cmp_lt_u32 s52, 7\n"
    "s_cbranch_scc0 .Lfb_L68\n"
    "buffer_load_dwordx4 v[96:99], v2, s[8:11], s16 offen sc1\n"
    ".Lfb_L68:\n"
    "v_exp_f32 v155, v155\n"
    "v_exp_f32 v163, v163\n"
    "v_add_f32 v72, v72, v154\n"
    "v_mfma_f32_16x16x32_bf16 v[48:51], v[16:19], v[212:215], v[48:51]\n"
    "ds_read_b128 v[16:19], v2 offset:57344\n"
    "v_add_f32 v73, v73, v162\n"
    "v_exp_f32 v156, v156\n"
    "v_exp_f32 v164, v164\n"
    "v_mfma_f32_16x16x32_bf16 v[52:55], v[20:23], v[212:215], v[52:55]\n"
    "ds_read_b128 v[20:23], v2 offset:58368\n"
    "s_waitcnt lgkmcnt(6)\n"
    "v_add_f32 v72, v72, v155\n"
    "v_add_f32 v73, v73, v163\n"
    "v_exp_f32 v157, v157\n"
    "s_cmp_eq_u32 s52, 7\n"
    "s_cbranch_scc0 .Lfb_L69\n"
    "s_waitcnt vmcnt(1)\n"
    ".Lfb_L69:\n"
    "s_waitcnt vmcnt(6)\n"
    "s_barrier\n"
    "s_cmp_eq_u32 s52, 7\n"
    "s_cbranch_scc0 .Lfb_L70\n"
    "s_lshl_b32 s3, s2, 7\n"
    "s_add_u32 s3, s3, 114688\n"
    "s_mov_b64 exec, 1\n"
    "v_mov_b32 v14, s53\n"
    "buffer_store_dword v14, off, s[8:11], s3 sc1\n"
    "s_mov_b64 exec, -1\n"
    ".Lfb_L70:\n"
    "s_cmp_lt_u32 s95, 56\n"
    "s_cselect_b32 s3, s52, 0\n"
    "s_cmp_eq_u32 s3, 7\n"
    "s_cbranch_scc0 .Lfb_L71\n"
    "s_lshr_b32 s6, s95, 1\n"
    "s_add_u32 s6, s6, 14\n"
    "s_and_b32 s7, s95, 1\n"
    "v_lshlrev_b32 v13, 2, v1\n"
    "v_add_u32 v13, 160768, v13\n"
    "ds_read_b32 v86, v13 offset:1536\n"
    "ds_read_b32 v85, v13 offset:1280\n"
    "ds_read_b32 v84, v13 offset:1024\n"
    "ds_read_b32 v83, v13 offset:768\n"
    "ds_read_b32 v82, v13 offset:512\n"
    "ds_read_b32 v81, v13 offset:256\n"
    "ds_read_b32 v80, v13\n"
    "s_mul_i32 s3, s7, 21504\n"
    "s_lshl_b32 s4, s6, 9\n"
    "s_add_u32 s3, s3, s4\n"
    "s_add_u32 s3, s3, 4096\n"
    "s_waitcnt lgkmcnt(0)\n"
    "v_add_f32 v80, v80, v81\n"
    "v_add_f32 v80, v80, v82\n"
    "v_add_f32 v80, v80, v83\n"
    "v_add_f32 v80, v80, v84\n"
    "v_add_f32 v80, v80, v85\n"
    "v_add_f32 v80, v80, v86\n"
    "v_mov_b32 v81, s53\n"
    "buffer_store_dwordx2 v[80:81], v249, s[8:11], s3 offen sc1\n"
    ".Lfb_L71:\n"
    "v_mfma_f32_16x16x32_bf16 v[48:51], v[24:27], v[216:219], v[48:51]\n"
    "ds_read_b128 v[24:27], v2 offset:59392\n"
    "v_exp_f32 v165, v165\n"
    "v_add_f32 v72, v72, v156\n"
    "v_add_f32 v73, v73, v164\n"
    "v_mfma_f32_16x16x32_bf16 v[52:55], v[28:31], v[216:219], v[52:55]\n"
    "ds_read_b128 v[28:31], v2 offset:60416\n"
    "s_waitcnt lgkmcnt(6)\n"
    "s_cmp_lt_u32 s52, 7\n"
    "s_cbranch_scc0 .Lfb_L72\n"
    "buffer_load_dwordx4 v[100:103], v2, s[8:11], s16 offen offset:1024 sc1\n"
    ".Lfb_L72:\n"
    "v_exp_f32 v158, v158\n"
    "v_exp_f32 v166, v166\n"
    "v_mfma_f32_16x16x32_bf16 v[48:51], v[32:35], v[220:223], v[48:51]\n"
    "v_add_f32 v72, v72, v157\n"
    "v_add_f32 v73, v73, v165\n"
    "v_add_f32 v72, v72, v158\n"
    "v_add_f32 v73, v73, v166\n"
    "v_mfma_f32_16x16x32_bf16 v[52:55], v[36:39], v[220:223], v[52:55]\n"
    "s_waitcnt lgkmcnt(4)\n"
    "s_cmp_lt_u32 s52, 7\n"
    "s_cbranch_scc0 .Lfb_L73\n"
    "buffer_load_dwordx4 v[104:107], v2, s[8:11], s16 offen offset:2048 sc1\n"
    ".Lfb_L73:\n"
    "v_permlane32_swap_b32 v72, v73\n"
    "v_add_f32 v72, v72, v73\n"
    "v_mov_b32 v73, v72\n"
    "v_mfma_f32_16x16x32_bf16 v[48:51], v[40:43], v[224:227], v[48:51]\n"
    "s_nop 0\n"
    "v_permlane16_swap_b32 v72, v73\n"
    "v_add_f32 v72, v72, v73\n"
    "v_rcp_f32 v73, v72\n"
    "v_mfma_f32_16x16x32_bf16 v[52:55], v[44:47], v[224:227], v[52:55]\n"
    "s_waitcnt lgkmcnt(2)\n"
    "s_cmp_lt_u32 s52, 7\n"
    "s_cbranch_scc0 .Lfb_L74\n"
    "buffer_load_dwordx4 v[108:111], v2, s[8:11], s16 offen offset:3072 sc1\n"
    "s_xor_b32 s16, s16, 12288\n"
    ".Lfb_L74:\n"
    "v_mul_f32 v72, v73, v236\n"
    "v_mul_f32 v73, v73, v236\n"
    "v_mfma_f32_16x16x32_bf16 v[48:51], v[16:19], v[228:231], v[48:51]\n"
    "s_nop 0\n"
    "v_permlane32_swap_b32 v72, v73\n"
    "v_mul_f32 v152, v152, v72\n"
    "v_mul_f32 v153, v153, v72\n"
    "v_mul_f32 v154, v154, v72\n"
    "v_mfma_f32_16x16x32_bf16 v[52:55], v[20:23], v[228:231], v[52:55]\n"
    "s_waitcnt lgkmcnt(0)\n"
    "s_cmp_lt_u32 s52, 7\n"
    "s_cbranch_scc0 .Lfb_L75\n"
    "buffer_load_dwordx4 v[112:115], v2, s[8:11], s16 offen sc1\n"
    ".Lfb_L75:\n"
    "v_mul_f32 v155, v155, v72\n"
    "v_mul_f32 v156, v156, v72\n"
    "v_mul_f32 v157, v157, v72\n"
    "v_mul_f32 v158, v158, v72\n"
    "v_mfma_f32_16x16x32_bf16 v[48:51], v[24:27], v[232:235], v[48:51]\n"
    "v_cvt_pk_bf16_f32 v64, v152, v153\n"
    "v_cvt_pk_bf16_f32 v65, v154, v155\n"
    "v_cvt_pk_bf16_f32 v66, v156, v157\n"
    "v_cvt_pk_bf16_f32 v67, v158, 0\n"
    "ds_write2_b64 v242, v[64:65], v[66:67] offset1:4\n"
    "v_mul_f32 v160, v160, v73\n"
    "v_mfma_f32_16x16x32_bf16 v[52:55], v[28:31], v[232:235], v[52:55]\n"
    "s_cmp_lt_u32 s52, 7\n"
    "s_cbranch_scc0 .Lfb_L76\n"
    "buffer_load_dwordx4 v[116:119], v2, s[8:11], s16 offen offset:1024 sc1\n"
    ".Lfb_L76:\n"
    "v_mul_f32 v161, v161, v73\n"
    "v_mul_f32 v162, v162, v73\n"
    "v_mul_f32 v163, v163, v73\n"
    "v_mul_f32 v164, v164, v73\n"
    "v_mul_f32 v165, v165, v73\n"
    "v_mul_f32 v166, v166, v73\n"
    "v_cvt_pk_bf16_f32 v68, v160, v161\n"
    "v_cvt_pk_bf16_f32 v69, v162, v163\n"
    "v_cvt_pk_bf16_f32 v70, v164, v165\n"
    "v_cvt_pk_bf16_f32 v71, v166, 0\n"
    "ds_write2_b64 v242, v[68:69], v[70:71] offset0:8 offset1:12\n"
    "v_cvt_pk_bf16_f32 v48, v48, v49\n"
    "v_cvt_pk_bf16_f32 v49, v50, v51\n"
    "v_cvt_pk_bf16_f32 v50, v52, v53\n"
    "v_cvt_pk_bf16_f32 v51, v54, v55\n"
    "v_cndmask_b32 v50, v50, v233, s[82:83]\n"
    "v_cndmask_b32 v51, v51, v233, s[82:83]\n"
    "ds_write2_b64 v242, v[48:49], v[50:51] offset0:16 offset1:20\n"
    "s_cmp_eq_u32 s52, 7\n"
    "s_cbranch_scc0 .Lfb_L77\n"
    "s_waitcnt vmcnt(1)\n"
    ".Lfb_L77:\n"
    "s_waitcnt vmcnt(8)\n"
    "v_xor_b32 v13, s53, v11\n"
    "v_cmp_ne_u32 vcc, 0, v13\n"
    "s_cbranch_vccnz .Lfb_S78\n"
    ".Lfb_B79:\n"
    "s_setprio 2\n"
    "s_waitcnt lgkmcnt(0)\n"
    "s_barrier\n"
    "ds_read_b64_tr_b16 v[16:17], v240\n"
    "ds_read_b64_tr_b16 v[18:19], v240 offset:800\n"
    "ds_read_b64_tr_b16 v[20:21], v241\n"
    "ds_read_b64_tr_b16 v[22:23], v241 offset:800\n"
    "ds_read_b64_tr_b16 v[24:25], v240 offset:6400\n"
    "ds_read_b64_tr_b16 v[26:27], v240 offset:7200\n"
    "ds_read_b64_tr_b16 v[28:29], v241 offset:6400\n"
    "ds_read_b64_tr_b16 v[30:31], v241 offset:7200\n"
    "ds_read_b64_tr_b16 v[32:33], v240 offset:12800\n"
    "ds_read_b64_tr_b16 v[34:35], v240 offset:13600\n"
    "ds_read_b64_tr_b16 v[36:37], v241 offset:12800\n"
    "ds_read_b64_tr_b16 v[38:39], v241 offset:13600\n"
    "s_waitcnt lgkmcnt(8)\n"
    "v_mfma_f32_16x16x32_bf16 v[48:51], v[16:19], v[20:23], 0\n"
    "ds_read_b64_tr_b16 v[40:41], v240 offset:19200\n"
    "ds_read_b64_tr_b16 v[42:43], v240 offset:20000\n"
    "ds_read_b64_tr_b16 v[44:45], v241 offset:19200\n"
    "ds_read_b64_tr_b16 v[46:47], v241 offset:20000\n"
    "s_waitcnt lgkmcnt(8)\n"
    "v_mfma_f32_16x16x32_bf16 v[52:55], v[24:27], v[28:31], 0\n"
    "s_waitcnt lgkmcnt(4)\n"
    "v_mfma_f32_16x16x32_bf16 v[48:51], v[32:35], v[36:39], v[48:51]\n"
    "s_waitcnt lgkmcnt(0)\n"
    "v_mfma_f32_16x16x32_bf16 v[52:55], v[40:43], v[44:47], v[52:55]\n"
    "s_nop 7\n"
    "v_add_f32 v48, v48, v52\n"
    "v_add_f32 v49, v49, v53\n"
    "v_add_f32 v50, v50, v54\n"
    "v_add_f32 v51, v51, v55\n"
    "v_cvt_pk_bf16_f32 v48, v48, v49\n"
    "v_cvt_pk_bf16_f32 v49, v50, v51\n"
    "v_min_u32 v48, -2, v48\n"
    "v_min_u32 v49, -2, v49\n"
    "buffer_store_dwordx2 v[48:49], v249, s[8:11], s66 offen sc1\n"
    "s_setprio 0\n"
    "ds_read_b128 v[16:19], v244\n"
    "ds_read_b128 v[20:23], v244 offset:1024\n"
    "ds_read_b128 v[24:27], v244 offset:2048\n"
    "ds_read_b128 v[28:31], v244 offset:3072\n"
    "ds_read_b128 v[32:35], v244 offset:4096\n"
    "ds_read_b128 v[36:39], v244 offset:5120\n"
    "ds_read_b128 v[40:43], v244 offset:6144\n"
    "ds_read_b128 v[44:47], v244 offset:7168\n"
    "ds_read_b128 v[164:167], v246 offset:3072\n"
    "ds_read_b128 v[160:163], v246 offset:2048\n"
    "ds_read_b128 v[156:159], v246 offset:1024\n"
    "ds_read_b128 v[152:155], v246\n"
    "s_waitcnt lgkmcnt(11)\n"
    "v_mfma_f32_16x16x32_bf16 v[48:51], v[16:19], v[176:179], 0\n"
    "ds_read_b128 v[16:19], v244 offset:8192\n"
    "s_waitcnt lgkmcnt(11)\n"
    "v_mfma_f32_16x16x32_bf16 v[52:55], v[20:23], v[176:179], 0\n"
    "ds_read_b128 v[20:23], v244 offset:9216\n"
    "s_waitcnt lgkmcnt(10)\n"
    "s_mov_b32 s65, 114688\n"
    "buffer_load_dword v11, v138, s[8:11], s65 offen sc1\n"
    "v_mfma_f32_16x16x32_bf16 v[48:51], v[24:27], v[180:183], v[48:51]\n"
    "ds_read_b128 v[24:27], v244 offset:10240\n"
    "v_mfma_f32_16x16x32_bf16 v[52:55], v[28:31], v[180:183], v[52:55]\n"
    "ds_read_b128 v[28:31], v244 offset:11264\n"
    "s_waitcnt lgkmcnt(10)\n"
    "v_mfma_f32_16x16x32_bf16 v[48:51], v[32:35], v[184:187], v[48:51]\n"
    "ds_read_b128 v[32:35], v244 offset:12288\n"
    "v_mfma_f32_16x16x32_bf16 v[52:55], v[36:39], v[184:187], v[52:55]\n"
    "ds_read_b128 v[36:39], v244 offset:13312\n"
    "s_waitcnt lgkmcnt(6)\n"
    "v_mfma_f32_16x16x32_bf16 v[64:67], v[144:147], v[152:155], 0\n"
    "v_mfma_f32_16x16x32_bf16 v[68:71], v[144:147], v[156:159], 0\n"
    "v_mfma_f32_16x16x32_bf16 v[64:67], v[148:151], v[160:163], v[64:67]\n"
    "v_mfma_f32_16x16x32_bf16 v[68:71], v[148:151], v[164:167], v[68:71]\n"
    "v_mfma_f32_16x16x32_bf16 v[48:51], v[40:43], v[188:191], v[48:51]\n"
    "ds_read_b128 v[40:43], v244 offset:14336\n"
    "v_mfma_f32_16x16x32_bf16 v[52:55], v[44:47], v[188:191], v[52:55]\n"
    "ds_read_b128 v[44:47], v244 offset:15360\n"
    "s_waitcnt lgkmcnt(6)\n"
    "v_mfma_f32_16x16x32_bf16 v[48:51], v[16:19], v[192:195], v[48:51]\n"
    "ds_read_b128 v[16:19], v244 offset:16384\n"
    "v_cvt_pk_bf16_f32 v66, 0, v64\n"
    "v_cvt_pk_bf16_f32 v67, 0, v65\n"
    "v_mfma_f32_16x16x32_bf16 v[52:55], v[20:23], v[192:195], v[52:55]\n"
    "ds_read_b128 v[20:23], v244 offset:17408\n"
    "s_waitcnt lgkmcnt(6)\n"
    "v_cvt_pk_bf16_f32 v70, 0, v68\n"
    "v_cvt_pk_bf16_f32 v71, 0, v69\n"
    "v_mfma_f32_16x16x32_bf16 v[48:51], v[24:27], v[196:199], v[48:51]\n"
    "ds_read_b128 v[24:27], v244 offset:18432\n"
    "v_sub_f32 v64, v64, v66\n"
    "v_sub_f32 v65, v65, v67\n"
    "v_mfma_f32_16x16x32_bf16 v[52:55], v[28:31], v[196:199], v[52:55]\n"
    "ds_read_b128 v[28:31], v244 offset:19456\n"
    "s_waitcnt lgkmcnt(6)\n"
    "v_sub_f32 v68, v68, v70\n"
    "v_sub_f32 v69, v69, v71\n"
    "v_mfma_f32_16x16x32_bf16 v[48:51], v[32:35], v[200:203], v[48:51]\n"
    "ds_read_b128 v[32:35], v244 offset:20480\n"
    "v_cvt_pk_bf16_f32 v64, v66, v64\n"
    "v_cvt_pk_bf16_f32 v65, v67, v65\n"
    "v_mfma_f32_16x16x32_bf16 v[52:55], v[36:39], v[200:203], v[52:55]\n"
    "ds_read_b128 v[36:39], v244 offset:21504\n"
    "s_waitcnt lgkmcnt(6)\n"
    "v_cvt_pk_bf16_f32 v68, v70, v68\n"
    "v_cvt_pk_bf16_f32 v69, v71, v69\n"
    "s_mov_b64 exec, s[78:79]\n"
    "ds_write2st64_b64 v247, v[64:65], v[68:69] offset0:120 offset1:122\n"
    "s_mov_b64 exec, -1\n"
    "v_mfma_f32_16x16x32_bf16 v[48:51], v[40:43], v[204:207], v[48:51]\n"
    "ds_read_b128 v[40:43], v244 offset:22528\n"
    "v_mfma_f32_16x16x32_bf16 v[52:55], v[44:47], v[204:207], v[52:55]\n"
    "ds_read_b128 v[44:47], v244 offset:23552\n"
    "s_waitcnt lgkmcnt(6)\n"
    "v_mfma_f32_16x16x32_bf16 v[48:51], v[16:19], v[208:211], v[48:51]\n"
    "ds_read_b128 v[16:19], v244 offset:24576\n"
    "v_mfma_f32_16x16x32_bf16 v[52:55], v[20:23], v[208:211], v[52:55]\n"
    "ds_read_b128 v[20:23], v244 offset:25600\n"
    "s_waitcnt lgkmcnt(6)\n"
    "v_mfma_f32_16x16x32_bf16 v[48:51], v[24:27], v[212:215], v[48:51]\n"
    "ds_read_b128 v[24:27], v244 offset:26624\n"
    "v_mfma_f32_16x16x32_bf16 v[52:55], v[28:31], v[212:215], v[52:55]\n"
    "ds_read_b128 v[28:31], v244 offset:27648\n"
    "s_waitcnt lgkmcnt(6)\n"
    "buffer_load_dwordx4 v[144:147], v2, s[8:11], s17 offen sc1\n"
    "s_waitcnt vmcnt(1)\n"
    "v_xor_b32 v13, s53, v11\n"
    "v_cmp_ne_u32 vcc, 0, v13\n"
    "s_cbranch_vccnz .Lfb_S82\n"
    ".Lfb_B83:\n"
    "s_barrier\n"
    "v_mfma_f32_16x16x32_bf16 v[48:51], v[32:35], v[216:219], v[48:51]\n"
    "ds_read_b128 v[32:35], v244 offset:28672\n"
    "v_mfma_f32_16x16x32_bf16 v[52:55], v[36:39], v[216:219], v[52:55]\n"
    "ds_read_b128 v[36:39], v244 offset:29696\n"
    "s_waitcnt lgkmcnt(6)\n"
    "buffer_load_dwordx4 v[148:151], v2, s[8:11], s17 offen offset:1024 sc1\n"
    "v_mfma_f32_16x16x32_bf16 v[48:51], v[40:43], v[220:223], v[48:51]\n"
    "v_mfma_f32_16x16x32_bf16 v[52:55], v[44:47], v[220:223], v[52:55]\n"
    "s_waitcnt lgkmcnt(4)\n"
    "ds_read_b128 v[72:75], v243 offset:2048\n"
    "ds_read_b128 v[76:79], v243 offset:3072\n"
    "v_mfma_f32_16x16x32_bf16 v[48:51], v[16:19], v[224:227], v[48:51]\n"
    "v_mfma_f32_16x16x32_bf16 v[52:55], v[20:23], v[224:227], v[52:55]\n"
    "s_waitcnt lgkmcnt(4)\n"
    "buffer_load_dwordx4 v[152:155], v2, s[8:11], s17 offen offset:2048 sc1\n"
    "v_mfma_f32_16x16x32_bf16 v[48:51], v[24:27], v[228:231], v[48:51]\n"
    "v_mfma_f32_16x16x32_bf16 v[52:55], v[28:31], v[228:231], v[52:55]\n"
    "s_waitcnt lgkmcnt(2)\n"
    "buffer_load_dwordx4 v[156:159], v2, s[8:11], s17 offen offset:3072 sc1\n"
    "v_mfma_f32_16x16x32_bf16 v[48:51], v[32:35], v[232:235], v[48:51]\n"
    "v_mfma_f32_16x16x32_bf16 v[52:55], v[36:39], v[232:235], v[52:55]\n"
    "v_mov_b32 v9, v254\n"
    "s_nop 5\n"
    "v_cvt_pk_bf16_f32 v56, v48, v49\n"
    "v_cvt_pk_bf16_f32 v57, v50, v51\n"
    "v_cvt_pk_bf16_f32 v58, v52, v53\n"
    "v_cvt_pk_bf16_f32 v59, v54, v55\n"
    "s_waitcnt lgkmcnt(1)\n"
    "s_nop 0\n"
    "v_mfma_f32_16x16x32_bf16 v[16:19], v[72:75], v[56:59], 0\n"
    "s_waitcnt lgkmcnt(0)\n"
    "v_mfma_f32_16x16x32_bf16 v[20:23], v[76:79], v[56:59], 0\n"
    "buffer_load_dwordx4 v[160:163], v2, s[8:11], s18 offen sc1\n"
    "s_nop 4\n"
    "v_max3_f32 v40, v16, v17, v18\n"
    "v_max3_f32 v41, -v16, -v17, -v18\n"
    "v_max3_f32 v40, v19, v20, v40\n"
    "v_max3_f32 v41, -v19, -v20, v41\n"
    "v_max3_f32 v40, v21, v22, v40\n"
    "v_max3_f32 v41, -v21, -v22, v41\n"
    "s_nop 1\n"
    "v_permlane32_swap_b32 v40, v41\n"
    "v_max_f32 v40, v40, v41\n"
    "v_mov_b32 v41, v40\n"
    "s_nop 1\n"
    "v_permlane16_swap_b32 v40, v41\n"
    "v_max_f32 v41, v40, v41\n"
    "v_mul_f32 v40, v12, v41\n"
    "v_mul_f32 v41, v12, v41\n"
    "buffer_load_dwordx4 v[164:167], v2, s[8:11], s18 offen offset:1024 sc1\n"
    "s_nop 0\n"
    "v_permlane32_swap_b32 v40, v41\n"
    "v_fma_f32 v24, v16, v12, -v40\n"
    "v_fma_f32 v32, -v16, v12, -v41\n"
    "v_fma_f32 v25, v17, v12, -v40\n"
    "v_fma_f32 v33, -v17, v12, -v41\n"
    "v_fma_f32 v26, v18, v12, -v40\n"
    "v_fma_f32 v34, -v18, v12, -v41\n"
    "v_fma_f32 v27, v19, v12, -v40\n"
    "v_fma_f32 v35, -v19, v12, -v41\n"
    "v_fma_f32 v28, v20, v12, -v40\n"
    "v_fma_f32 v36, -v20, v12, -v41\n"
    "v_fma_f32 v29, v21, v12, -v40\n"
    "v_fma_f32 v37, -v21, v12, -v41\n"
    "v_fma_f32 v30, v22, v12, -v40\n"
    "v_fma_f32 v38, -v22, v12, -v41\n"
    "v_exp_f32 v24, v24\n"
    "v_exp_f32 v32, v32\n"
    "v_exp_f32 v25, v25\n"
    "v_exp_f32 v33, v33\n"
    "v_exp_f32 v26, v26\n"
    "v_exp_f32 v34, v34\n"
    "v_add_f32 v40, v24, v25\n"
    "v_add_f32 v41, v32, v33\n"
    "v_exp_f32 v27, v27\n"
    "v_exp_f32 v35, v35\n"
    "v_add_f32 v40, v40, v26\n"
    "v_add_f32 v41, v41, v34\n"
    "v_exp_f32 v28, v28\n"
    "v_exp_f32 v36, v36\n"
    "v_add_f32 v40, v40, v27\n"
    "v_add_f32 v41, v41, v35\n"
    "v_exp_f32 v29, v29\n"
    "v_exp_f32 v37, v37\n"
    "v_add_f32 v40, v40, v28\n"
    "v_add_f32 v41, v41, v36\n"
    "v_exp_f32 v30, v30\n"
    "v_exp_f32 v38, v38\n"
    "v_add_f32 v40, v40, v29\n"
    "v_add_f32 v41, v41, v37\n"
    "v_add_f32 v40, v40, v30\n"
    "v_add_f32 v41, v41, v38\n"
    "buffer_load_dwordx4 v[168:171], v2, s[8:11], s18 offen offset:2048 sc1\n"
    "s_nop 0\n"
    "v_permlane32_swap_b32 v40, v41\n"
    "v_add_f32 v40, v40, v41\n"
    "v_mov_b32 v41, v40\n"
    "buffer_load_dwordx4 v[172:175], v2, s[8:11], s18 offen offset:3072 sc1\n"
    "s_nop 0\n"
    "v_permlane16_swap_b32 v40, v41\n"
    "v_add_f32 v40, v40, v41\n"
    "v_rcp_f32 v41, v40\n"
    "s_nop 0\n"
    "v_mov_b32 v40, v41\n"
    "s_nop 1\n"
    "v_permlane32_swap_b32 v40, v41\n"
    "v_mul_f32 v24, v24, v40\n"
    "v_mul_f32 v25, v25, v40\n"
    "v_mul_f32 v26, v26, v40\n"
    "v_mul_f32 v27, v27, v40\n"
    "v_mul_f32 v28, v28, v40\n"
    "v_mul_f32 v29, v29, v40\n"
    "v_mul_f32 v30, v30, v40\n"
    "v_cvt_pk_bf16_f32 v64, v24, v25\n"
    "v_cvt_pk_bf16_f32 v65, v26, v27\n"
    "v_cvt_pk_bf16_f32 v66, v28, v29\n"
    "v_cvt_pk_bf16_f32 v67, v30, 0\n"
    "buffer_load_dwordx4 v[176:179], v9, s[8:11], s22 offen sc1\n"
    "v_mul_f32 v32, v32, v41\n"
    "v_mul_f32 v33, v33, v41\n"
    "v_mul_f32 v34, v34, v41\n"
    "v_mul_f32 v35, v35, v41\n"
    "v_mul_f32 v36, v36, v41\n"
    "v_mul_f32 v37, v37, v41\n"
    "v_mul_f32 v38, v38, v41\n"
    "v_cvt_pk_bf16_f32 v68, v32, v33\n"
    "v_cvt_pk_bf16_f32 v69, v34, v35\n"
    "v_cvt_pk_bf16_f32 v70, v36, v37\n"
    "v_cvt_pk_bf16_f32 v71, v38, 0\n"
    "s_setprio 2\n"
    "s_waitcnt vmcnt(8)\n"
    "v_mfma_f32_16x16x32_bf16 v[48:51], v[128:131], v[144:147], 0\n"
    "v_mfma_f32_16x16x32_bf16 v[52:55], v[130:133], v[144:147], 0\n"
    "s_waitcnt vmcnt(7)\n"
    "v_mfma_f32_16x16x32_bf16 v[48:51], v[128:131], v[148:151], v[48:51]\n"
    "v_mfma_f32_16x16x32_bf16 v[52:55], v[130:133], v[148:151], v[52:55]\n"
    "s_waitcnt vmcnt(6)\n"
    "v_mfma_f32_16x16x32_bf16 v[48:51], v[128:131], v[152:155], v[48:51]\n"
    "v_mfma_f32_16x16x32_bf16 v[52:55], v[130:133], v[152:155], v[52:55]\n"
    "s_waitcnt vmcnt(5)\n"
    "v_mfma_f32_16x16x32_bf16 v[48:51], v[128:131], v[156:159], v[48:51]\n"
    "v_mfma_f32_16x16x32_bf16 v[52:55], v[130:133], v[156:159], v[52:55]\n"
    "s_waitcnt vmcnt(4)\n"
    "v_mfma_f32_16x16x32_bf16 v[48:51], v[128:131], v[160:163], v[48:51]\n"
    "v_mfma_f32_16x16x32_bf16 v[52:55], v[130:133], v[160:163], v[52:55]\n"
    "s_waitcnt vmcnt(3)\n"
    "v_mfma_f32_16x16x32_bf16 v[48:51], v[128:131], v[164:167], v[48:51]\n"
    "v_mfma_f32_16x16x32_bf16 v[52:55], v[130:133], v[164:167], v[52:55]\n"
    "s_waitcnt vmcnt(2)\n"
    "v_mfma_f32_16x16x32_bf16 v[48:51], v[128:131], v[168:171], v[48:51]\n"
    "v_mfma_f32_16x16x32_bf16 v[52:55], v[130:133], v[168:171], v[52:55]\n"
    "s_waitcnt vmcnt(1)\n"
    "v_mfma_f32_16x16x32_bf16 v[48:51], v[128:131], v[172:175], v[48:51]\n"
    "v_mfma_f32_16x16x32_bf16 v[52:55], v[130:133], v[172:175], v[52:55]\n"
    "buffer_load_dwordx4 v[180:183], v9, s[8:11], s22 offen offset:1024 sc1\n"
    "s_nop 5\n"
    "v_cmp_u_f32 vcc, v48, v48\n"
    "s_cbranch_vccnz .Lfb_S86\n"
    ".Lfb_B87:\n"
    "v_permlane32_swap_b32 v48, v52\n"
    "v_permlane32_swap_b32 v49, v53\n"
    "v_permlane32_swap_b32 v50, v54\n"
    "v_permlane32_swap_b32 v51, v55\n"
    "v_add_f32 v48, v48, v52\n"
    "v_add_f32 v49, v49, v53\n"
    "v_add_f32 v50, v50, v54\n"
    "v_add_f32 v51, v51, v55\n"
    "v_cvt_pk_bf16_f32 v48, v48, v49\n"
    "v_cvt_pk_bf16_f32 v49, v50, v51\n"
    "ds_write_b64 v134, v[48:49]\n"
    "buffer_load_dwordx4 v[184:187], v9, s[8:11], s22 offen offset:2048 sc1\n"
    "s_waitcnt lgkmcnt(0)\n"
    "s_barrier\n"
    "ds_read_b128 v[32:35], v135 offset:1024\n"
    "ds_read_b128 v[28:31], v135\n"
    "ds_read_b128 v[40:43], v135 offset:3072\n"
    "ds_read_b128 v[36:39], v135 offset:2048\n"
    "buffer_load_dwordx4 v[188:191], v9, s[8:11], s22 offen offset:3072 sc1\n"
    "s_waitcnt lgkmcnt(3)\n"
    "v_mfma_f32_16x16x32_bf16 v[52:55], v[32:35], v[64:67], 0\n"
    "s_waitcnt lgkmcnt(2)\n"
    "v_mfma_f32_16x16x32_bf16 v[48:51], v[28:31], v[64:67], 0\n"
    "s_waitcnt lgkmcnt(1)\n"
    "v_mfma_f32_16x16x32_bf16 v[52:55], v[40:43], v[68:71], v[52:55]\n"
    "s_waitcnt lgkmcnt(0)\n"
    "v_mfma_f32_16x16x32_bf16 v[48:51], v[36:39], v[68:71], v[48:51]\n"
    "s_mov_b32 s90, 4096\n"
    "s_mov_b32 s91, 25600\n"
    "s_add_u32 m0, s30, 0\n"
    "s_nop 2\n"
    "v_add_f32 v10, 0x358637bd, v52\n"
    "v_rcp_f32 v10, v10\n"
    "s_nop 0\n"
    "v_mov_b32 v11, v10\n"
    "s_nop 1\n"
    "v_permlane32_swap_b32 v10, v11\n"
    "v_mov_b32 v10, v11\n"
    "s_nop 1\n"
    "v_permlane16_swap_b32 v10, v11\n"
    "v_mul_f32 v48, v11, v48\n"
    "v_mul_f32 v49, v11, v49\n"
    "v_mul_f32 v50, v11, v50\n"
    "v_mul_f32 v51, v11, v51\n"
    "v_mul_f32 v52, v11, v52\n"
    "v_mul_f32 v53, v11, v53\n"
    "v_mul_f32 v54, v11, v54\n"
    "v_mul_f32 v55, v11, v55\n"
    "v_cvt_pk_bf16_f32 v48, v48, v49\n"
    "v_cvt_pk_bf16_f32 v49, v50, v51\n"
    "v_cvt_pk_bf16_f32 v50, v52, v53\n"
    "v_cvt_pk_bf16_f32 v51, v54, v55\n"
    "v_min_u32 v48, -2, v48\n"
    "v_min_u32 v49, -2, v49\n"
    "v_min_u32 v50, -2, v50\n"
    "v_min_u32 v51, -2, v51\n"
    "v_cndmask_b32 v50, v50, 0, s[82:83]\n"
    "v_cndmask_b32 v51, v51, 0, s[82:83]\n"
    "buffer_store_dwordx4 v[48:51], v2, s[8:11], s67 offen sc1\n"
    "s_setprio 0\n"
    "v_mov_b32 v23, v137\n"
    "buffer_load_dwordx4 v[28:31], v23, s[8:11], s90 offen sc1\n"
    "buffer_load_dwordx4 v[32:35], v23, s[8:11], s91 offen sc1\n"
    "buffer_load_dwordx4 v[76:79], v136, s[12:15], s29 offen\n"
    "s_cmp_lt_u32 s52, 7\n"
    "s_cbranch_scc0 .Lfb_L90\n"
    "buffer_load_dwordx4 v[120:123], v2, s[8:11], s16 offen offset:2048 sc1\n"
    "buffer_load_dwordx4 v[124:127], v2, s[8:11], s16 offen offset:3072 sc1\n"
    "s_xor_b32 s16, s16, 4096\n"
    "buffer_load_dwordx4 v[128:131], v2, s[8:11], s16 offen sc1\n"
    ".Lfb_L90:\n"
    "s_cmp_lt_u32 s52, 7\n"
    "s_cselect_b32 s3, 0, 256\n"
    "s_cselect_b32 s5, 224, 0\n"
    "v_subrev_u32 v10, 224, v0\n"
    "v_add_u32 v11, s5, v0\n"
    "v_cmp_gt_u32 vcc, 224, v10\n"
    "s_add_u32 s3, s3, 32768\n"
    "s_nop 0\n"
    "v_cndmask_b32 v13, 0, 1.0, vcc\n"
    "v_cndmask_b32 v12, v11, v10, vcc\n"
    "v_cmp_le_u32 vcc, 224, v0\n"
    "v_lshl_add_u32 v11, v10, 3, s3\n"
    "s_mov_b64 s[84:85], vcc\n"
    "v_cndmask_b32 v10, v0, v10, vcc\n"
    "s_lshr_b32 s4, s93, 7\n"
    "s_cmp_lt_u32 s52, 7\n"
    "s_cselect_b32 s4, s4, 0\n"
    "v_cmp_gt_u32 vcc, s4, v10\n"
    "v_subrev_u32 v12, s4, v12\n"
    "v_add_u32 v10, 224, v12\n"
    "v_cndmask_b32 v12, v12, v10, vcc\n"
    "v_lshlrev_b32 v12, 2, v12\n"
    "v_add_u32 v10, 158720, v12\n"
    "s_cmp_eq_u32 s52, 7\n"
    "s_cbranch_scc0 .Lfb_L91\n"
    "s_waitcnt vmcnt(1)\n"
    ".Lfb_L91:\n"
    "s_waitcnt vmcnt(6)\n"
    "s_setprio 2\n"
    "buffer_load_dwordx4 v2, s[8:11], s28 offen sc1 lds\n"
    "buffer_load_dwordx4 v2, s[8:11], s28 offen offset:1024 sc1 lds\n"
    "buffer_load_dwordx4 v2, s[8:11], s28 offen offset:2048 sc1 lds\n"
    "buffer_load_dwordx4 v2, s[8:11], s28 offen offset:3072 sc1 lds\n"
    "s_setprio 0\n"
    "s_waitcnt vmcnt(9)\n"
    "v_xor_b32 v26, s53, v29\n"
    "v_bitop3_b32 v26, v31, s53, v26 bitop3:0xbe\n"
    "s_waitcnt vmcnt(8)\n"
    "v_bitop3_b32 v26, v33, s53, v26 bitop3:0xbe\n"
    "v_bitop3_b32 v26, v35, s53, v26 bitop3:0xbe\n"
    "v_cmp_ne_u32 vcc, 0, v26\n"
    "s_cbranch_vccnz .Lfb_S92\n"
    ".Lfb_B93:\n"
    "v_add_f32 v28, v28, v32\n"
    "v_add_f32 v30, v30, v34\n"
    "v_cvt_pk_bf16_f32 v29, v28, v30\n"
    "v_lshlrev_b32 v28, 16, v29\n"
    "v_and_b32 v30, 0xffff0000, v29\n"
    "v_add_f32 v28, v13, v28\n"
    "v_add_f32 v30, v13, v30\n"
    "v_cvt_pk_bf16_f32 v29, v28, v30\n"
    "v_lshlrev_b32 v32, 16, v29\n"
    "v_and_b32 v33, 0xffff0000, v29\n"
    "ds_write_b32 v10, v29\n"
    "s_mov_b64 exec, s[84:85]\n"
    "ds_write_b64 v11, v[32:33]\n"
    "s_mov_b64 exec, -1\n"
    "v_lshrrev_b32 v12, 5, v1\n"
    "s_lshl_b32 s3, s52, 6\n"
    "s_add_u32 s4, s3, 34816\n"
    "v_lshl_add_u32 v10, v12, 5, s4\n"
    "s_mul_i32 s4, s62, 448\n"
    "s_add_u32 s4, s4, s3\n"
    "s_add_u32 s4, s4, 32768\n"
    "v_lshl_add_u32 v11, v12, 5, s4\n"
    "v_and_b32 v13, 31, v1\n"
    "v_lshl_add_u32 v13, v13, 1, v12\n"
    "s_lshl_b32 s4, s52, 8\n"
    "s_add_u32 s4, s4, 35328\n"
    "v_lshlrev_b32 v26, 8, v12\n"
    "v_lshl_add_u32 v12, v13, 2, s4\n"
    "v_and_b32 v13, 1, v4\n"
    "v_lshl_add_u32 v13, v13, 9, v26\n"
    "v_lshl_add_u32 v13, v5, 4, v13\n"
    "s_add_u32 s29, s69, 1835008\n"
    "s_cmp_lt_u32 s52, 7\n"
    "s_cbranch_scc0 .Lfb_L96\n"
    "buffer_load_dwordx4 v[132:135], v2, s[8:11], s16 offen offset:1024 sc1\n"
    "buffer_load_dwordx4 v[136:139], v2, s[8:11], s16 offen offset:2048 sc1\n"
    "buffer_load_dwordx4 v[140:143], v2, s[8:11], s16 offen offset:3072 sc1\n"
    ".Lfb_L96:\n"
    "buffer_load_dwordx4 v[192:195], v9, s[8:11], s23 offen sc1\n"
    "buffer_load_dwordx4 v[196:199], v9, s[8:11], s23 offen offset:1024 sc1\n"
    "s_setprio 2\n"
    "s_cmp_eq_u32 s52, 7\n"
    "s_cbranch_scc0 .Lfb_L97\n"
    "s_waitcnt vmcnt(2)\n"
    ".Lfb_L97:\n"
    "s_waitcnt vmcnt(5)\n"
    "ds_read_b128 v[40:43], v245\n"
    "ds_read_b128 v[44:47], v245 offset:1024\n"
    "ds_read_b128 v[56:59], v245 offset:2048\n"
    "ds_read_b128 v[60:63], v245 offset:3072\n"
    "s_waitcnt lgkmcnt(3)\n"
    "v_max_u32 v26, v40, v41\n"
    "v_max3_u32 v26, v42, v43, v26\n"
    "s_waitcnt lgkmcnt(2)\n"
    "v_max3_u32 v26, v44, v45, v26\n"
    "v_max3_u32 v26, v46, v47, v26\n"
    "s_waitcnt lgkmcnt(1)\n"
    "v_max3_u32 v26, v56, v57, v26\n"
    "v_max3_u32 v26, v58, v59, v26\n"
    "s_waitcnt lgkmcnt(0)\n"
    "v_max3_u32 v26, v60, v61, v26\n"
    "v_max3_u32 v26, v62, v63, v26\n"
    "v_cmp_eq_u32 vcc, -1, v26\n"
    "s_cbranch_vccnz .Lfb_S98\n"
    ".Lfb_B99:\n"
    "s_barrier\n"
    "s_cmp_lt_u32 s52, 7\n"
    "s_cbranch_scc0 .Lfb_L102\n"
    "ds_read_b128 v[16:19], v2\n"
    "ds_read_b128 v[20:23], v2 offset:16384\n"
    "ds_read_b128 v[24:27], v2 offset:1024\n"
    "ds_read_b128 v[28:31], v2 offset:17408\n"
    "ds_read_b128 v[32:35], v2 offset:2048\n"
    "ds_read_b128 v[36:39], v2 offset:18432\n"
    "ds_read_b128 v[40:43], v2 offset:3072\n"
    "ds_read_b128 v[44:47], v2 offset:19456\n"
    "s_waitcnt lgkmcnt(7)\n"
    "v_mfma_f32_16x16x32_bf16 v[48:51], v[80:83], v[16:19], 0\n"
    "ds_read_b128 v[16:19], v2 offset:4096\n"
    "s_waitcnt lgkmcnt(7)\n"
    "v_mfma_f32_16x16x32_bf16 v[52:55], v[80:83], v[20:23], 0\n"
    "ds_read_b128 v[20:23], v2 offset:20480\n"
    "s_waitcnt lgkmcnt(6)\n"
    "buffer_load_dwordx4 v[200:203], v9, s[8:11], s23 offen offset:2048 sc1\n"
    "v_mfma_f32_16x16x32_bf16 v[48:51], v[84:87], v[24:27], v[48:51]\n"
    "ds_read_b128 v[24:27], v2 offset:5120\n"
    "v_mfma_f32_16x16x32_bf16 v[52:55], v[84:87], v[28:31], v[52:55]\n"
    "ds_read_b128 v[28:31], v2 offset:21504\n"
    "s_waitcnt lgkmcnt(6)\n"
    "buffer_load_dwordx4 v[204:207], v9, s[8:11], s23 offen offset:3072 sc1\n"
    "v_mfma_f32_16x16x32_bf16 v[48:51], v[88:91], v[32:35], v[48:51]\n"
    "ds_read_b128 v[32:35], v2 offset:6144\n"
    "v_mfma_f32_16x16x32_bf16 v[52:55], v[88:91], v[36:39], v[52:55]\n"
    "ds_read_b128 v[36:39], v2 offset:22528\n"
    "s_waitcnt lgkmcnt(6)\n"
    "v_mfma_f32_16x16x32_bf16 v[48:51], v[92:95], v[40:43], v[48:51]\n"
    "ds_read_b128 v[40:43], v2 offset:7168\n"
    "v_mfma_f32_16x16x32_bf16 v[52:55], v[92:95], v[44:47], v[52:55]\n"
    "ds_read_b128 v[44:47], v2 offset:23552\n"
    "s_waitcnt lgkmcnt(6)\n"
    "buffer_load_dwordx4 v[208:211], v9, s[8:11], s32 offen sc1\n"
    "v_mfma_f32_16x16x32_bf16 v[48:51], v[96:99], v[16:19], v[48:51]\n"
    "ds_read_b128 v[16:19], v2 offset:8192\n"
    "v_lshlrev_b32 v80, 16, v76\n"
    "v_and_b32 v81, 0xffff0000, v76\n"
    "v_mfma_f32_16x16x32_bf16 v[52:55], v[96:99], v[20:23], v[52:55]\n"
    "ds_read_b128 v[20:23], v2 offset:24576\n"
    "s_waitcnt lgkmcnt(6)\n"
    "v_lshlrev_b32 v82, 16, v77\n"
    "v_and_b32 v83, 0xffff0000, v77\n"
    "buffer_load_dwordx4 v[212:215], v9, s[8:11], s32 offen offset:1024 sc1\n"
    "v_mfma_f32_16x16x32_bf16 v[48:51], v[100:103], v[24:27], v[48:51]\n"
    "ds_read_b128 v[24:27], v2 offset:9216\n"
    "v_lshlrev_b32 v84, 16, v78\n"
    "v_and_b32 v85, 0xffff0000, v78\n"
    "v_mfma_f32_16x16x32_bf16 v[52:55], v[100:103], v[28:31], v[52:55]\n"
    "ds_read_b128 v[28:31], v2 offset:25600\n"
    "s_waitcnt lgkmcnt(6)\n"
    "v_lshlrev_b32 v86, 16, v79\n"
    "v_and_b32 v87, 0xffff0000, v79\n"
    "v_mfma_f32_16x16x32_bf16 v[48:51], v[104:107], v[32:35], v[48:51]\n"
    "ds_read_b128 v[32:35], v2 offset:10240\n"
    "v_mfma_f32_16x16x32_bf16 v[52:55], v[104:107], v[36:39], v[52:55]\n"
    "ds_read_b128 v[36:39], v2 offset:26624\n"
    "s_waitcnt lgkmcnt(6)\n"
    "buffer_load_dwordx4 v[216:219], v9, s[8:11], s32 offen offset:2048 sc1\n"
    "v_mfma_f32_16x16x32_bf16 v[48:51], v[108:111], v[40:43], v[48:51]\n"
    "ds_read_b128 v[40:43], v2 offset:11264\n"
    "v_mfma_f32_16x16x32_bf16 v[52:55], v[108:111], v[44:47], v[52:55]\n"
    "ds_read_b128 v[44:47], v2 offset:27648\n"
    "s_waitcnt lgkmcnt(6)\n"
    "v_mfma_f32_16x16x32_bf16 v[48:51], v[112:115], v[16:19], v[48:51]\n"
    "ds_read_b128 v[16:19], v2 offset:12288\n"
    "v_mfma_f32_16x16x32_bf16 v[52:55], v[112:115], v[20:23], v[52:55]\n"
    "ds_read_b128 v[20:23], v2 offset:28672\n"
    "s_waitcnt lgkmcnt(6)\n"
    "buffer_load_dwordx4 v[220:223], v9, s[8:11], s32 offen offset:3072 sc1\n"
    "v_mfma_f32_16x16x32_bf16 v[48:51], v[116:119], v[24:27], v[48:51]\n"
    "ds_read_b128 v[24:27], v2 offset:13312\n"
    "v_mfma_f32_16x16x32_bf16 v[52:55], v[116:119], v[28:31], v[52:55]\n"
    "ds_read_b128 v[28:31], v2 offset:29696\n"
    "s_waitcnt lgkmcnt(6)\n"
    "buffer_load_dwordx4 v[224:227], v9, s[8:11], s33 offen sc1\n"
    "v_mfma_f32_16x16x32_bf16 v[48:51], v[120:123], v[32:35], v[48:51]\n"
    "ds_read_b128 v[32:35], v2 offset:14336\n"
    "v_mfma_f32_16x16x32_bf16 v[52:55], v[120:123], v[36:39], v[52:55]\n"
    "ds_read_b128 v[36:39], v2 offset:30720\n"
    "s_waitcnt lgkmcnt(6)\n"
    "v_mfma_f32_16x16x32_bf16 v[48:51], v[124:127], v[40:43], v[48:51]\n"
    "ds_read_b128 v[40:43], v2 offset:15360\n"
    "v_mfma_f32_16x16x32_bf16 v[52:55], v[124:127], v[44:47], v[52:55]\n"
    "ds_read_b128 v[44:47], v2 offset:31744\n"
    "s_waitcnt lgkmcnt(6)\n"
    "buffer_load_dwordx4 v[228:231], v9, s[8:11], s33 offen offset:1024 sc1\n"
    "v_mfma_f32_16x16x32_bf16 v[48:51], v[128:131], v[16:19], v[48:51]\n"
    "ds_read_b128 v[56:59], v10\n"
    "v_mfma_f32_16x16x32_bf16 v[52:55], v[128:131], v[20:23], v[52:55]\n"
    "ds_read_b128 v[60:63], v10 offset:16\n"
    "s_waitcnt lgkmcnt(6)\n"
    "buffer_load_dwordx4 v[232:235], v9, s[8:11], s33 offen offset:2048 sc1\n"
    "s_waitcnt vmcnt(13)\n"
    "v_mfma_f32_16x16x32_bf16 v[48:51], v[132:135], v[24:27], v[48:51]\n"
    "ds_read_b128 v[64:67], v11\n"
    "v_mfma_f32_16x16x32_bf16 v[52:55], v[132:135], v[28:31], v[52:55]\n"
    "ds_read_b128 v[68:71], v11 offset:16\n"
    "s_waitcnt vmcnt(12) lgkmcnt(6)\n"
    "v_mfma_f32_16x16x32_bf16 v[48:51], v[136:139], v[32:35], v[48:51]\n"
    "v_mfma_f32_16x16x32_bf16 v[52:55], v[136:139], v[36:39], v[52:55]\n"
    "s_waitcnt lgkmcnt(4)\n"
    "buffer_load_dwordx4 v[144:147], v9, s[8:11], s20 offen sc1\n"
    "s_waitcnt vmcnt(12)\n"
    "v_mfma_f32_16x16x32_bf16 v[48:51], v[140:143], v[40:43], v[48:51]\n"
    "v_mfma_f32_16x16x32_bf16 v[52:55], v[140:143], v[44:47], v[52:55]\n"
    "s_waitcnt lgkmcnt(0)\n"
    "s_nop 6\n"
    "v_permlane16_swap_b32 v48, v52\n"
    "v_permlane16_swap_b32 v49, v53\n"
    "v_permlane16_swap_b32 v50, v54\n"
    "v_permlane16_swap_b32 v51, v55\n"
    "v_fmac_f32 v80, v56, v48\n"
    "v_fmac_f32 v81, v57, v49\n"
    "v_fmac_f32 v82, v58, v50\n"
    "v_fmac_f32 v83, v59, v51\n"
    "v_fmac_f32 v84, v60, v52\n"
    "v_fmac_f32 v85, v61, v53\n"
    "v_fmac_f32 v86, v62, v54\n"
    "v_fmac_f32 v87, v63, v55\n"
    "v_mul_f32 v64, v64, v80\n"
    "v_mul_f32 v65, v65, v81\n"
    "v_mul_f32 v66, v66, v82\n"
    "v_mul_f32 v67, v67, v83\n"
    "v_mul_f32 v68, v68, v84\n"
    "v_mul_f32 v69, v69, v85\n"
    "v_mul_f32 v70, v70, v86\n"
    "v_mul_f32 v71, v71, v87\n"
    "v_cvt_pk_bf16_f32 v64, v64, v65\n"
    "v_cvt_pk_bf16_f32 v65, v66, v67\n"
    "v_cvt_pk_bf16_f32 v66, v68, v69\n"
    "v_cvt_pk_bf16_f32 v67, v70, v71\n"
    "v_min_u32 v64, -2, v64\n"
    "v_min_u32 v65, -2, v65\n"
    "v_min_u32 v66, -2, v66\n"
    "v_min_u32 v67, -2, v67\n"
    "buffer_store_dwordx4 v[64:67], v13, s[8:11], s69 offen sc1\n"
    "s_setprio 0\n"
    "v_cvt_pk_bf16_f32 v56, v80, v81\n"
    "v_cvt_pk_bf16_f32 v57, v82, v83\n"
    "v_cvt_pk_bf16_f32 v58, v84, v85\n"
    "v_cvt_pk_bf16_f32 v59, v86, v87\n"
    "buffer_store_dwordx4 v[56:59], v13, s[8:11], s29 offen sc1\n"
    "v_add_f32 v16, v80, v81\n"
    "v_mul_f32 v17, v80, v80\n"
    "v_fmac_f32 v17, v81, v81\n"
    "v_add_f32 v16, v16, v82\n"
    "v_fmac_f32 v17, v82, v82\n"
    "v_add_f32 v16, v16, v83\n"
    "v_fmac_f32 v17, v83, v83\n"
    "v_add_f32 v16, v16, v84\n"
    "v_fmac_f32 v17, v84, v84\n"
    "v_add_f32 v16, v16, v85\n"
    "v_fmac_f32 v17, v85, v85\n"
    "v_add_f32 v16, v16, v86\n"
    "v_fmac_f32 v17, v86, v86\n"
    "v_add_f32 v16, v16, v87\n"
    "v_fmac_f32 v17, v87, v87\n"
    "s_nop 1\n"
    "v_permlane32_swap_b32 v16, v17\n"
    "v_add_f32 v16, v16, v17\n"
    "ds_write_b32 v12, v16\n"
    "s_branch .Lfb_L103\n"
    ".Lfb_L102:\n"
    "s_setprio 0\n"
    "v_add_u32 v10, 160768, v249\n"
    "v_lshlrev_b32 v11, 2, v1\n"
    "s_lshl_b32 s3, s59, 5\n"
    "v_add_u32 v23, s3, v1\n"
    "v_min_u32 v23, 223, v23\n"
    "v_add_u32 v23, 1120, v23\n"
    "v_lshlrev_b32 v23, 4, v23\n"
    "buffer_load_dwordx4 v[28:31], v23, s[8:11], s90 offen sc1\n"
    "buffer_load_dwordx4 v[32:35], v23, s[8:11], s91 offen sc1\n"
    "buffer_load_dwordx4 v[200:203], v9, s[8:11], s23 offen offset:2048 sc1\n"
    "s_sleep 1\n"
    "buffer_load_dwordx4 v[204:207], v9, s[8:11], s23 offen offset:3072 sc1\n"
    "s_sleep 1\n"
    "buffer_load_dwordx4 v[208:211], v9, s[8:11], s32 offen sc1\n"
    "s_sleep 1\n"
    "buffer_load_dwordx4 v[212:215], v9, s[8:11], s32 offen offset:1024 sc1\n"
    "s_sleep 1\n"
    "buffer_load_dwordx4 v[216:219], v9, s[8:11], s32 offen offset:2048 sc1\n"
    "s_sleep 1\n"
    "buffer_load_dwordx4 v[220:223], v9, s[8:11], s32 offen offset:3072 sc1\n"
    "s_sleep 1\n"
    "buffer_load_dwordx4 v[224:227], v9, s[8:11], s33 offen sc1\n"
    "s_sleep 1\n"
    "buffer_load_dwordx4 v[228:231], v9, s[8:11], s33 offen offset:1024 sc1\n"
    "s_sleep 1\n"
    "buffer_load_dwordx4 v[232:235], v9, s[8:11], s33 offen offset:2048 sc1\n"
    "s_sleep 1\n"
    "buffer_load_dwordx4 v[144:147], v9, s[8:11], s20 offen sc1\n"
    "s_sleep 1\n"
    "s_waitcnt vmcnt(11)\n"
    "v_xor_b32 v26, s53, v29\n"
    "v_bitop3_b32 v26, v31, s53, v26 bitop3:0xbe\n"
    "s_waitcnt vmcnt(10)\n"
    "v_bitop3_b32 v26, v33, s53, v26 bitop3:0xbe\n"
    "v_bitop3_b32 v26, v35, s53, v26 bitop3:0xbe\n"
    "v_cmp_ne_u32 vcc, 0, v26\n"
    "s_cbranch_vccnz .Lfb_S104\n"
    ".Lfb_B105:\n"
    "v_add_f32 v28, v28, v32\n"
    "v_add_f32 v30, v30, v34\n"
    "v_cvt_pk_bf16_f32 v29, v28, v30\n"
    "v_lshlrev_b32 v32, 16, v29\n"
    "v_and_b32 v33, 0xffff0000, v29\n"
    "ds_write_b64 v10, v[32:33]\n"
    ".Lfb_L103:\n"
    "s_cmp_ge_u32 s59, 7\n"
    "s_cbranch_scc0 .Lfb_L108\n"
    "s_waitcnt vmcnt(0) lgkmcnt(0)\n"
    "s_barrier\n"
    "s_cmp_eq_u32 s52, 7\n"
    "s_cbranch_scc0 .Lfb_L109\n"
    "v_lshlrev_b32 v14, 2, v1\n"
    "ds_read_b32 v62, v14 offset:36864\n"
    "ds_read_b32 v61, v14 offset:36608\n"
    "ds_read_b32 v60, v14 offset:36352\n"
    "ds_read_b32 v59, v14 offset:36096\n"
    "ds_read_b32 v58, v14 offset:35840\n"
    "ds_read_b32 v57, v14 offset:35584\n"
    "ds_read_b32 v56, v14 offset:35328\n"
    "s_waitcnt lgkmcnt(0)\n"
    "v_add_f32 v56, v56, v57\n"
    "v_add_f32 v58, v58, v59\n"
    "v_add_f32 v60, v60, v61\n"
    "v_add_f32 v56, v56, v58\n"
    "v_add_f32 v60, v60, v62\n"
    "v_add_f32 v56, v56, v60\n"
    "v_min_u32 v56, -2, v56\n"
    "buffer_store_dword v56, v14, s[8:11], s69 offen sc1\n"
    "s_cmp_eq_u32 s2, 0\n"
    "s_cbranch_scc0 .Lfb_L110\n"
    "s_mov_b64 exec, 1\n"
    "v_mov_b32 v14, s53\n"
    "buffer_store_dword v14, off, s[8:11], 0 sc1\n"
    "s_mov_b64 exec, -1\n"
    ".Lfb_L110:\n"
    ".Lfb_L109:\n"
    "s_branch .Lfb_LEND\n"
    ".Lfb_L108:\n"
    "s_and_b32 s3, s60, 1\n"
    "s_lshl_b32 s3, s3, 1\n"
    "s_lshl2_add_u32 s3, s55, s3\n"
    "s_mul_i32 s7, s52, 3584\n"
    "s_mov_b32 s6, 10006528\n"
    "s_add_u32 s75, s7, 98304\n"
    "s_add_u32 s7, s7, s93\n"
    "v_add_u32 v136, s7, v2\n"
    "v_add_u32 v10, 4096, v136\n"
    "v_subrev_u32 v11, 24576, v136\n"
    "v_cmp_gt_u32 vcc, 25600, v136\n"
    "s_nop 1\n"
    "v_cndmask_b32 v139, v11, v10, vcc\n"
    "v_cmp_gt_u32 vcc, 26624, v136\n"
    "s_nop 1\n"
    "v_cndmask_b32 v138, v11, v10, vcc\n"
    "v_cmp_gt_u32 vcc, 27648, v136\n"
    "s_nop 1\n"
    "v_cndmask_b32 v137, v11, v10, vcc\n"
    "v_cmp_gt_u32 vcc, 28672, v136\n"
    "s_nop 1\n"
    "v_cndmask_b32 v136, v11, v10, vcc\n"
    "s_and_b32 s4, s52, 3\n"
    "s_xor_b32 s4, s4, s3\n"
    "s_lshl_b32 s4, s4, 10\n"
    "s_add_u32 s16, s4, 13680640\n"
    "s_lshl3_add_u32 s65, s60, s52\n"
    "s_lshl_b32 s65, s65, 10\n"
    "s_add_u32 s65, s65, 13746176\n"
    "s_xor_b32 s4, s3, 0\n"
    "s_mul_i32 s5, s4, 28672\n"
    "s_add_u32 s66, s5, s6\n"
    "s_lshl2_add_u32 s71, s5, s65\n"
    "s_xor_b32 s4, s3, 1\n"
    "s_mul_i32 s5, s4, 28672\n"
    "s_add_u32 s67, s5, s6\n"
    "s_lshl2_add_u32 s72, s5, s65\n"
    "s_xor_b32 s4, s3, 2\n"
    "s_mul_i32 s5, s4, 28672\n"
    "s_add_u32 s68, s5, s6\n"
    "s_lshl2_add_u32 s73, s5, s65\n"
    "s_xor_b32 s4, s3, 3\n"
    "s_mul_i32 s5, s4, 28672\n"
    "s_add_u32 s70, s5, s6\n"
    "s_lshl2_add_u32 s74, s5, s65\n"
    "v_and_b32 v10, 1, v1\n"
    "v_mul_u32_u24 v10, 896, v10\n"
    "v_lshl_add_u32 v10, v4, 4, v10\n"
    "v_add_u32 v10, 158720, v10\n"
    "v_and_b32 v7, 1, v4\n"
    "v_lshrrev_b32 v6, 1, v4\n"
    "v_lshlrev_b32 v8, 10, v6\n"
    "v_lshl_add_u32 v8, v7, 8, v8\n"
    "v_lshl_add_u32 v8, v5, 4, v8\n"
    "v_add_u32 v12, 98304, v8\n"
    "v_add_u32 v11, s75, v2\n"
    "v_and_b32 v3, 0x1f0, v2\n"
    "v_add_u32 v3, s75, v3\n"
    "v_lshl_add_u32 v7, v5, 1, v7\n"
    "v_lshl_add_u32 v7, v6, 7, v7\n"
    "s_and_b32 s4, s52, 3\n"
    "s_lshl_b32 s4, s4, 9\n"
    "s_add_u32 s4, s4, 155648\n"
    "v_lshl_add_u32 v7, v7, 4, s4\n"
    "v_lshlrev_b32 v6, 4, v5\n"
    "v_add_u32 v6, 155648, v6\n"
    "s_barrier\n"
    "buffer_load_dwordx4 v[148:151], v9, s[8:11], s20 offen offset:1024 sc1\n"
    "buffer_load_dwordx4 v[152:155], v9, s[8:11], s20 offen offset:2048 sc1\n"
    "buffer_load_dwordx4 v[156:159], v9, s[8:11], s20 offen offset:3072 sc1\n"
    "buffer_load_dwordx4 v[160:163], v9, s[8:11], s21 offen sc1\n"
    "buffer_load_dwordx4 v[164:167], v9, s[8:11], s21 offen offset:1024 sc1\n"
    "buffer_load_dwordx4 v[168:171], v9, s[8:11], s21 offen offset:2048 sc1\n"
    "buffer_load_dwordx4 v[172:175], v9, s[8:11], s21 offen offset:3072 sc1\n"
    "buffer_load_dwordx4 v[236:239], v9, s[8:11], s33 offen offset:3072 sc1\n"
    "buffer_load_dwordx4 v[240:243], v9, s[8:11], s94 offen sc1\n"
    "buffer_load_dwordx4 v[244:247], v9, s[8:11], s94 offen offset:1024 sc1\n"
    "buffer_load_dwordx4 v[248:251], v9, s[8:11], s94 offen offset:2048 sc1\n"
    "buffer_load_dwordx4 v[252:255], v9, s[8:11], s94 offen offset:3072 sc1\n"
    "ds_read_b128 v[16:19], v10\n"
    "ds_read_b128 v[20:23], v10 offset:64\n"
    "ds_read_b128 v[24:27], v10 offset:128\n"
    "ds_read_b128 v[28:31], v10 offset:192\n"
    "s_waitcnt vmcnt(12) lgkmcnt(0)\n"
    "s_barrier\n"
    "s_setprio 2\n"
    "s_add_u32 m0, s75, 0\n"
    "s_mov_b32 exec_hi, 0\n"
    "buffer_load_dwordx4 v139, s[8:11], s66 offen offset:3072 sc1 lds\n"
    "s_mov_b32 exec_hi, -1\n"
    "buffer_load_dwordx4 v136, s[8:11], s66 offen sc1 lds\n"
    "buffer_load_dwordx4 v137, s[8:11], s66 offen offset:1024 sc1 lds\n"
    "buffer_load_dwordx4 v138, s[8:11], s66 offen offset:2048 sc1 lds\n"
    "s_setprio 0\n"
    "s_cmp_eq_u32 s52, 7\n"
    "s_cbranch_scc0 .Lfb_L111\n"
    "v_lshlrev_b32 v14, 2, v1\n"
    "ds_read_b32 v62, v14 offset:36864\n"
    "ds_read_b32 v61, v14 offset:36608\n"
    "ds_read_b32 v60, v14 offset:36352\n"
    "ds_read_b32 v59, v14 offset:36096\n"
    "ds_read_b32 v58, v14 offset:35840\n"
    "ds_read_b32 v57, v14 offset:35584\n"
    "ds_read_b32 v56, v14 offset:35328\n"
    "s_waitcnt lgkmcnt(0)\n"
    "v_add_f32 v56, v56, v57\n"
    "v_add_f32 v58, v58, v59\n"
    "v_add_f32 v60, v60, v61\n"
    "v_add_f32 v56, v56, v58\n"
    "v_add_f32 v60, v60, v62\n"
    "v_add_f32 v56, v56, v60\n"
    "v_min_u32 v56, -2, v56\n"
    "buffer_store_dword v56, v14, s[8:11], s69 offen sc1\n"
    "s_cmp_eq_u32 s2, 0\n"
    "s_cbranch_scc0 .Lfb_L112\n"
    "s_mov_b64 exec, 1\n"
    "v_mov_b32 v14, s53\n"
    "buffer_store_dword v14, off, s[8:11], 0 sc1\n"
    "s_mov_b64 exec, -1\n"
    ".Lfb_L112:\n"
    ".Lfb_L111:\n"
    "v_mov_b32 v96, -1\n"
    "v_mov_b32 v97, -1\n"
    "v_mov_b32 v98, -1\n"
    "v_mov_b32 v99, -1\n"
    "v_mfma_f32_16x16x32_bf16 v[40:43], v[16:19], v[144:147], 0\n"
    "s_and_b32 s4, s54, 3\n"
    "s_lshl2_add_u32 s76, s97, s4\n"
    "s_waitcnt vmcnt(15)\n"
    "v_mfma_f32_16x16x32_bf16 v[44:47], v[16:19], v[148:151], 0\n"
    "s_and_b32 s3, s54, 1\n"
    "s_lshr_b32 s4, s52, 2\n"
    "ds_read_b128 v[16:19], v10 offset:256\n"
    "s_waitcnt vmcnt(14)\n"
    "v_mfma_f32_16x16x32_bf16 v[40:43], v[20:23], v[152:155], v[40:43]\n"
    "s_and_b32 s5, s52, 3\n"
    "s_mul_i32 s6, s59, 56\n"
    "s_waitcnt vmcnt(13)\n"
    "v_mfma_f32_16x16x32_bf16 v[44:47], v[20:23], v[156:159], v[44:47]\n"
    "s_add_u32 s6, s6, s5\n"
    "s_lshl_b32 s6, s6, 12\n"
    "s_add_u32 s6, s6, 5259264\n"
    "ds_read_b128 v[20:23], v10 offset:320\n"
    "s_waitcnt vmcnt(12)\n"
    "v_mfma_f32_16x16x32_bf16 v[40:43], v[24:27], v[160:163], v[40:43]\n"
    "s_mul_i32 s7, s76, 224\n"
    "s_lshl1_add_u32 s7, s5, s7\n"
    "s_waitcnt vmcnt(11)\n"
    "v_mfma_f32_16x16x32_bf16 v[44:47], v[24:27], v[164:167], v[44:47]\n"
    "s_lshl_b32 s7, s7, 10\n"
    "s_add_u32 s7, s7, 13746176\n"
    "ds_read_b128 v[24:27], v10 offset:384\n"
    "s_waitcnt vmcnt(10)\n"
    "v_mfma_f32_16x16x32_bf16 v[40:43], v[28:31], v[168:171], v[40:43]\n"
    "s_xor_b32 s5, s4, s3\n"
    "s_add_u32 s5, s5, 6\n"
    "s_lshl_b32 s65, s5, 13\n"
    "s_waitcnt vmcnt(9)\n"
    "v_mfma_f32_16x16x32_bf16 v[44:47], v[28:31], v[172:175], v[44:47]\n"
    "s_lshl1_add_u32 s37, s65, s6\n"
    "s_add_u32 s43, s65, s7\n"
    "ds_read_b128 v[28:31], v10 offset:448\n"
    "s_waitcnt lgkmcnt(3)\n"
    "v_mfma_f32_16x16x32_bf16 v[40:43], v[16:19], v[176:179], v[40:43]\n"
    "s_lshl_b32 s5, s4, 16\n"
    "s_lshl1_add_u32 s6, s5, s6\n"
    "v_mfma_f32_16x16x32_bf16 v[44:47], v[16:19], v[180:183], v[44:47]\n"
    "s_add_u32 s7, s5, s7\n"
    "s_lshl_b32 s3, s3, 13\n"
    "s_mov_b32 s94, 0x24924924\n"
    "ds_read_b128 v[16:19], v10 offset:512\n"
    "s_waitcnt lgkmcnt(3)\n"
    "v_mfma_f32_16x16x32_bf16 v[40:43], v[20:23], v[184:187], v[40:43]\n"
    "s_bfe_u32 s5, s94, 131072\n"
    "s_lshl_b32 s5, s5, 14\n"
    "v_mfma_f32_16x16x32_bf16 v[44:47], v[20:23], v[188:191], v[44:47]\n"
    "s_add_u32 s5, s5, s3\n"
    "s_lshl1_add_u32 s34, s5, s6\n"
    "ds_read_b128 v[20:23], v10 offset:576\n"
    "s_waitcnt lgkmcnt(3)\n"
    "v_mfma_f32_16x16x32_bf16 v[40:43], v[24:27], v[192:195], v[40:43]\n"
    "s_add_u32 s40, s5, s7\n"
    "s_xor_b32 s5, s5, 8192\n"
    "s_lshl1_add_u32 s38, s5, s6\n"
    "v_mfma_f32_16x16x32_bf16 v[44:47], v[24:27], v[196:199], v[44:47]\n"
    "s_add_u32 s44, s5, s7\n"
    "s_bfe_u32 s5, s94, 131074\n"
    "ds_read_b128 v[24:27], v10 offset:640\n"
    "s_add_u32 m0, s75, 28672\n"
    "s_mov_b32 exec_hi, 0\n"
    "buffer_load_dwordx4 v139, s[8:11], s67 offen offset:3072 sc1 lds\n"
    "s_mov_b32 exec_hi, -1\n"
    "buffer_load_dwordx4 v136, s[8:11], s67 offen sc1 lds\n"
    "buffer_load_dwordx4 v137, s[8:11], s67 offen offset:1024 sc1 lds\n"
    "buffer_load_dwordx4 v138, s[8:11], s67 offen offset:2048 sc1 lds\n"
    "s_waitcnt lgkmcnt(3)\n"
    "v_mfma_f32_16x16x32_bf16 v[40:43], v[28:31], v[200:203], v[40:43]\n"
    "s_lshl_b32 s5, s5, 14\n"
    "s_add_u32 s5, s5, s3\n"
    "v_mfma_f32_16x16x32_bf16 v[44:47], v[28:31], v[204:207], v[44:47]\n"
    "s_lshl1_add_u32 s35, s5, s6\n"
    "s_add_u32 s41, s5, s7\n"
    "s_xor_b32 s5, s5, 8192\n"
    "ds_read_b128 v[28:31], v10 offset:704\n"
    "s_waitcnt lgkmcnt(3)\n"
    "v_mfma_f32_16x16x32_bf16 v[40:43], v[16:19], v[208:211], v[40:43]\n"
    "s_lshl1_add_u32 s39, s5, s6\n"
    "s_add_u32 s45, s5, s7\n"
    "v_mfma_f32_16x16x32_bf16 v[44:47], v[16:19], v[212:215], v[44:47]\n"
    "s_bfe_u32 s5, s94, 131076\n"
    "s_lshl_b32 s5, s5, 14\n"
    "ds_read_b128 v[16:19], v10 offset:768\n"
    "s_waitcnt lgkmcnt(3)\n"
    "v_mfma_f32_16x16x32_bf16 v[40:43], v[20:23], v[216:219], v[40:43]\n"
    "s_add_u32 s5, s5, s3\n"
    "s_lshl1_add_u32 s36, s5, s6\n"
    "s_add_u32 s42, s5, s7\n"
    "v_mfma_f32_16x16x32_bf16 v[44:47], v[20:23], v[220:223], v[44:47]\n"
    "s_xor_b32 s5, s5, 8192\n"
    "s_lshl1_add_u32 s47, s5, s6\n"
    "ds_read_b128 v[20:23], v10 offset:832\n"
    "s_waitcnt lgkmcnt(3)\n"
    "v_mfma_f32_16x16x32_bf16 v[40:43], v[24:27], v[224:227], v[40:43]\n"
    "s_add_u32 s46, s5, s7\n"
    "s_mul_i32 s77, s52, 12288\n"
    "v_mfma_f32_16x16x32_bf16 v[44:47], v[24:27], v[228:231], v[44:47]\n"
    "s_lshr_b32 s3, s76, 1\n"
    "s_min_u32 s5, s52, 6\n"
    "s_lshl3_add_u32 s3, s3, s5\n"
    "s_waitcnt lgkmcnt(2)\n"
    "v_mfma_f32_16x16x32_bf16 v[40:43], v[28:31], v[232:235], v[40:43]\n"
    "s_lshl_b32 s3, s3, 8\n"
    "s_add_u32 s3, s3, 147456\n"
    "s_waitcnt vmcnt(12)\n"
    "v_mfma_f32_16x16x32_bf16 v[44:47], v[28:31], v[236:239], v[44:47]\n"
    "v_lshrrev_b32 v141, 5, v1\n"
    "v_lshl_add_u32 v141, v141, 7, s3\n"
    "s_waitcnt vmcnt(11) lgkmcnt(1)\n"
    "v_mfma_f32_16x16x32_bf16 v[40:43], v[16:19], v[240:243], v[40:43]\n"
    "s_lshl4_add_u32 s3, s55, s60\n"
    "s_lshl_b32 s3, s3, 7\n"
    "s_add_u32 s17, s3, 147456\n"
    "s_waitcnt vmcnt(10)\n"
    "v_mfma_f32_16x16x32_bf16 v[44:47], v[16:19], v[244:247], v[44:47]\n"
    "s_lshl1_add_u32 s4, s76, s4\n"
    "s_mul_i32 s6, s4, 28672\n"
    "s_waitcnt vmcnt(9) lgkmcnt(0)\n"
    "v_mfma_f32_16x16x32_bf16 v[40:43], v[20:23], v[248:251], v[40:43]\n"
    "s_lshl_b32 s5, s59, 12\n"
    "s_add_u32 s5, s5, s6\n"
    "s_waitcnt vmcnt(8)\n"
    "v_mfma_f32_16x16x32_bf16 v[44:47], v[20:23], v[252:255], v[44:47]\n"
    "s_add_u32 s18, s5, 11845632\n"
    "s_lshl_b32 s7, s59, 7\n"
    "s_add_u32 s19, s6, s7\n"
    "v_mov_b32 v9, v8\n"
    "v_add_u32 v10, 114688, v8\n"
    "v_cmp_gt_u32 vcc, 16, v1\n"
    "v_mov_b32 v75, 0\n"
    "v_mov_b32 v79, 0\n"
    "v_cndmask_b32 v40, 0, v40, vcc\n"
    "v_cndmask_b32 v41, 0, v41, vcc\n"
    "v_cndmask_b32 v44, 0, v44, vcc\n"
    "v_cndmask_b32 v45, 0, v45, vcc\n"
    "v_cvt_pk_bf16_f32 v74, v40, v41\n"
    "v_lshlrev_b32 v42, 16, v74\n"
    "v_and_b32 v43, 0xffff0000, v74\n"
    "v_sub_f32 v40, v40, v42\n"
    "v_sub_f32 v41, v41, v43\n"
    "v_cvt_pk_bf16_f32 v72, v42, v40\n"
    "v_cvt_pk_bf16_f32 v73, v43, v41\n"
    "v_cvt_pk_bf16_f32 v78, v44, v45\n"
    "v_lshlrev_b32 v46, 16, v78\n"
    "v_and_b32 v47, 0xffff0000, v78\n"
    "v_sub_f32 v44, v44, v46\n"
    "v_sub_f32 v45, v45, v47\n"
    "v_cvt_pk_bf16_f32 v76, v46, v44\n"
    "v_cvt_pk_bf16_f32 v77, v47, v45\n"
    "s_cmp_eq_u32 s52, 7\n"
    "s_cbranch_scc0 .Lfb_L113\n"
    "s_waitcnt vmcnt(5)\n"
    ".Lfb_L113:\n"
    "s_waitcnt vmcnt(4)\n"
    "ds_read_b128 v[100:103], v11\n"
    "ds_read_b128 v[104:107], v11 offset:1024\n"
    "ds_read_b128 v[108:111], v11 offset:2048\n"
    "ds_read_b128 v[112:115], v3 offset:3072\n"
    "s_waitcnt lgkmcnt(3)\n"
    "v_max_u32 v143, v100, v101\n"
    "v_max3_u32 v143, v102, v103, v143\n"
    "s_waitcnt lgkmcnt(2)\n"
    "v_max3_u32 v143, v104, v105, v143\n"
    "v_max3_u32 v143, v106, v107, v143\n"
    "s_waitcnt lgkmcnt(1)\n"
    "v_max3_u32 v143, v108, v109, v143\n"
    "v_max3_u32 v143, v110, v111, v143\n"
    "s_waitcnt lgkmcnt(0)\n"
    "v_max3_u32 v143, v112, v113, v143\n"
    "v_max3_u32 v143, v114, v115, v143\n"
    "v_cmp_eq_u32 vcc, -1, v143\n"
    "s_cbranch_vccnz .Lfb_S114\n"
    ".Lfb_B115:\n"
    "s_barrier\n"
    "ds_read_b128 v[16:19], v12\n"
    "ds_read_b128 v[20:23], v12 offset:512\n"
    "ds_read_b128 v[24:27], v12 offset:2048\n"
    "ds_read_b128 v[28:31], v12 offset:2560\n"
    "ds_read_b128 v[32:35], v12 offset:4096\n"
    "ds_read_b128 v[36:39], v12 offset:4608\n"
    "s_waitcnt lgkmcnt(5)\n"
    "v_mfma_f32_16x16x32_bf16 v[40:43], v[144:147], v[16:19], 0\n"
    "v_mfma_f32_16x16x32_bf16 v[48:51], v[148:151], v[16:19], 0\n"
    "ds_read_b128 v[16:19], v12 offset:6144\n"
    "s_cmp_ge_u32 s52, 4\n"
    "s_cbranch_scc0 .Lfb_L118\n"
    "buffer_load_dwordx4 v[84:87], v2, s[8:11], s16 offen sc1\n"
    ".Lfb_L118:\n"
    "s_waitcnt lgkmcnt(5)\n"
    "v_mfma_f32_16x16x32_bf16 v[44:47], v[144:147], v[20:23], 0\n"
    "s_waitcnt lgkmcnt(3)\n"
    "v_mfma_f32_16x16x32_bf16 v[52:55], v[148:151], v[20:23], 0\n"
    "ds_read_b128 v[20:23], v12 offset:6656\n"
    "buffer_store_dwordx4 v[96:99], v2, s[8:11], s71 offen sc1\n"
    "v_mfma_f32_16x16x32_bf16 v[40:43], v[152:155], v[24:27], v[40:43]\n"
    "v_mfma_f32_16x16x32_bf16 v[48:51], v[156:159], v[24:27], v[48:51]\n"
    "ds_read_b128 v[24:27], v12 offset:8192\n"
    "buffer_store_dwordx4 v[96:99], v2, s[8:11], s72 offen sc1\n"
    "v_mfma_f32_16x16x32_bf16 v[44:47], v[152:155], v[28:31], v[44:47]\n"
    "s_waitcnt lgkmcnt(3)\n"
    "v_mfma_f32_16x16x32_bf16 v[52:55], v[156:159], v[28:31], v[52:55]\n"
    "ds_read_b128 v[28:31], v12 offset:8704\n"
    "buffer_store_dwordx4 v[96:99], v2, s[8:11], s73 offen sc1\n"
    "v_mfma_f32_16x16x32_bf16 v[40:43], v[160:163], v[32:35], v[40:43]\n"
    "v_mfma_f32_16x16x32_bf16 v[48:51], v[164:167], v[32:35], v[48:51]\n"
    "ds_read_b128 v[32:35], v12 offset:10240\n"
    "buffer_store_dwordx4 v[96:99], v2, s[8:11], s74 offen sc1\n"
    "v_mfma_f32_16x16x32_bf16 v[44:47], v[160:163], v[36:39], v[44:47]\n"
    "s_waitcnt lgkmcnt(3)\n"
    "v_mfma_f32_16x16x32_bf16 v[52:55], v[164:167], v[36:39], v[52:55]\n"
    "ds_read_b128 v[36:39], v12 offset:10752\n"
    "v_mfma_f32_16x16x32_bf16 v[40:43], v[168:171], v[16:19], v[40:43]\n"
    "v_mfma_f32_16x16x32_bf16 v[48:51], v[172:175], v[16:19], v[48:51]\n"
    "ds_read_b128 v[16:19], v12 offset:12288\n"
    "v_mfma_f32_16x16x32_bf16 v[44:47], v[168:171], v[20:23], v[44:47]\n"
    "s_waitcnt lgkmcnt(3)\n"
    "v_mfma_f32_16x16x32_bf16 v[52:55], v[172:175], v[20:23], v[52:55]\n"
    "ds_read_b128 v[20:23], v12 offset:12800\n"
    "v_mfma_f32_16x16x32_bf16 v[40:43], v[176:179], v[24:27], v[40:43]\n"
    "v_mfma_f32_16x16x32_bf16 v[48:51], v[180:183], v[24:27], v[48:51]\n"
    "ds_read_b128 v[24:27], v12 offset:14336\n"
    "s_waitcnt vmcnt(4)\n"
    "ds_read_b128 v[100:103], v11 offset:28672\n"
    "v_mfma_f32_16x16x32_bf16 v[44:47], v[176:179], v[28:31], v[44:47]\n"
    "s_waitcnt lgkmcnt(4)\n"
    "v_mfma_f32_16x16x32_bf16 v[52:55], v[180:183], v[28:31], v[52:55]\n"
    "ds_read_b128 v[28:31], v12 offset:14848\n"
    "ds_read_b128 v[104:107], v11 offset:29696\n"
    "v_mfma_f32_16x16x32_bf16 v[40:43], v[184:187], v[32:35], v[40:43]\n"
    "v_mfma_f32_16x16x32_bf16 v[48:51], v[188:191], v[32:35], v[48:51]\n"
    "ds_read_b128 v[32:35], v12 offset:16384\n"
    "ds_read_b128 v[108:111], v11 offset:30720\n"
    "v_mfma_f32_16x16x32_bf16 v[44:47], v[184:187], v[36:39], v[44:47]\n"
    "s_waitcnt lgkmcnt(6)\n"
    "v_mfma_f32_16x16x32_bf16 v[52:55], v[188:191], v[36:39], v[52:55]\n"
    "ds_read_b128 v[36:39], v12 offset:16896\n"
    "ds_read_b128 v[112:115], v3 offset:31744\n"
    "v_mfma_f32_16x16x32_bf16 v[40:43], v[192:195], v[16:19], v[40:43]\n"
    "v_mfma_f32_16x16x32_bf16 v[48:51], v[196:199], v[16:19], v[48:51]\n"
    "ds_read_b128 v[16:19], v12 offset:18432\n"
    "v_mfma_f32_16x16x32_bf16 v[44:47], v[192:195], v[20:23], v[44:47]\n"
    "s_waitcnt lgkmcnt(6)\n"
    "v_max_u32 v143, v100, v101\n"
    "v_max3_u32 v143, v102, v103, v143\n"
    "v_mfma_f32_16x16x32_bf16 v[52:55], v[196:199], v[20:23], v[52:55]\n"
    "ds_read_b128 v[20:23], v12 offset:18944\n"
    "s_waitcnt lgkmcnt(6)\n"
    "v_max3_u32 v143, v104, v105, v143\n"
    "v_max3_u32 v143, v106, v107, v143\n"
    "v_mfma_f32_16x16x32_bf16 v[40:43], v[200:203], v[24:27], v[40:43]\n"
    "s_waitcnt lgkmcnt(4)\n"
    "v_max3_u32 v143, v108, v109, v143\n"
    "v_mfma_f32_16x16x32_bf16 v[48:51], v[204:207], v[24:27], v[48:51]\n"
    "ds_read_b128 v[24:27], v12 offset:20480\n"
    "v_max3_u32 v143, v110, v111, v143\n"
    "s_waitcnt lgkmcnt(3)\n"
    "v_max3_u32 v143, v112, v113, v143\n"
    "v_mfma_f32_16x16x32_bf16 v[44:47], v[200:203], v[28:31], v[44:47]\n"
    "v_max3_u32 v143, v114, v115, v143\n"
    "v_cmp_eq_u32 vcc, -1, v143\n"
    "s_cbranch_vccnz .Lfb_S119\n"
    ".Lfb_B120:\n"
    "v_mfma_f32_16x16x32_bf16 v[52:55], v[204:207], v[28:31], v[52:55]\n"
    "ds_read_b128 v[28:31], v12 offset:20992\n"
    "s_add_u32 m0, s77, 0\n"
    "s_nop 0\n"
    "buffer_load_dwordx4 v2, s[8:11], s34 offen sc1 lds\n"
    "v_mfma_f32_16x16x32_bf16 v[40:43], v[208:211], v[32:35], v[40:43]\n"
    "v_mfma_f32_16x16x32_bf16 v[48:51], v[212:215], v[32:35], v[48:51]\n"
    "ds_read_b128 v[32:35], v12 offset:22528\n"
    "v_mfma_f32_16x16x32_bf16 v[44:47], v[208:211], v[36:39], v[44:47]\n"
    "s_waitcnt lgkmcnt(3)\n"
    "v_mfma_f32_16x16x32_bf16 v[52:55], v[212:215], v[36:39], v[52:55]\n"
    "ds_read_b128 v[36:39], v12 offset:23040\n"
    "s_cmp_ge_u32 s52, 4\n"
    "s_cbranch_scc0 .Lfb_L123\n"
    "v_max_u32 v143, v84, v85\n"
    "v_max3_u32 v143, v86, v87, v143\n"
    "v_cmp_eq_u32 vcc, -1, v143\n"
    "s_cbranch_vccnz .Lfb_S124\n"
    ".Lfb_B125:\n"
    "v_permlane16_swap_b32 v84, v86\n"
    "v_permlane16_swap_b32 v85, v87\n"
    "v_add_f32 v84, v84, v86\n"
    "v_add_f32 v85, v85, v87\n"
    "v_mov_b32 v86, v84\n"
    "v_mov_b32 v87, v85\n"
    "v_cmp_gt_u32 vcc, 32, v1\n"
    "v_permlane32_swap_b32 v84, v86\n"
    "v_permlane32_swap_b32 v85, v87\n"
    "v_add_f32 v84, v84, v86\n"
    "v_add_f32 v85, v85, v87\n"
    "v_mul_f32 v84, 0x3b124925, v84\n"
    "v_mul_f32 v85, 0x3b124925, v85\n"
    "v_fma_f32 v85, -v84, v84, v85\n"
    "v_max_f32 v85, 0, v85\n"
    "v_add_f32 v85, 0x3727c5ac, v85\n"
    "v_rsq_f32 v86, v85\n"
    "v_mov_b32 v95, 0\n"
    "v_mul_f32 v87, v85, v86\n"
    "v_cvt_pk_bf16_f32 v88, -v84, -v84\n"
    "v_cvt_pk_bf16_f32 v89, v87, v87\n"
    "v_lshlrev_b32 v90, 16, v88\n"
    "v_lshlrev_b32 v91, 16, v89\n"
    "v_add_f32 v90, v90, v84\n"
    "v_sub_f32 v91, v87, v91\n"
    "v_cvt_pk_bf16_f32 v90, -v90, v91\n"
    "v_mul_f32 v92, 0xc0135761, v86\n"
    "v_mul_f32 v93, v86, v86\n"
    "v_mul_f32 v86, 0xbdd2d3e7, v86\n"
    "v_mul_f32 v93, v93, v86\n"
    "v_cndmask_b32 v92, v92, v88, vcc\n"
    "v_cndmask_b32 v93, v93, v89, vcc\n"
    "v_cndmask_b32 v94, v85, v90, vcc\n"
    "ds_write_b128 v7, v[92:95]\n"
    ".Lfb_L123:\n"
    "v_mfma_f32_16x16x32_bf16 v[40:43], v[216:219], v[16:19], v[40:43]\n"
    "v_mfma_f32_16x16x32_bf16 v[48:51], v[220:223], v[16:19], v[48:51]\n"
    "ds_read_b128 v[16:19], v12 offset:24576\n"
    "buffer_load_dwordx4 v2, s[8:11], s34 offen offset:1024 sc1 lds\n"
    "v_mfma_f32_16x16x32_bf16 v[44:47], v[216:219], v[20:23], v[44:47]\n"
    "s_waitcnt lgkmcnt(3)\n"
    "v_mfma_f32_16x16x32_bf16 v[52:55], v[220:223], v[20:23], v[52:55]\n"
    "ds_read_b128 v[20:23], v12 offset:25088\n"
    "v_mfma_f32_16x16x32_bf16 v[40:43], v[224:227], v[24:27], v[40:43]\n"
    "v_mfma_f32_16x16x32_bf16 v[48:51], v[228:231], v[24:27], v[48:51]\n"
    "ds_read_b128 v[24:27], v12 offset:26624\n"
    "buffer_load_dwordx4 v2, s[8:11], s34 offen offset:2048 sc1 lds\n"
    "v_mfma_f32_16x16x32_bf16 v[44:47], v[224:227], v[28:31], v[44:47]\n"
    "s_waitcnt lgkmcnt(3)\n"
    "v_mfma_f32_16x16x32_bf16 v[52:55], v[228:231], v[28:31], v[52:55]\n"
    "ds_read_b128 v[28:31], v12 offset:27136\n"
    "v_mfma_f32_16x16x32_bf16 v[40:43], v[232:235], v[32:35], v[40:43]\n"
    "v_mfma_f32_16x16x32_bf16 v[48:51], v[236:239], v[32:35], v[48:51]\n"
    "buffer_load_dwordx4 v2, s[8:11], s34 offen offset:3072 sc1 lds\n"
    "v_mfma_f32_16x16x32_bf16 v[44:47], v[232:235], v[36:39], v[44:47]\n"
    "s_waitcnt lgkmcnt(2)\n"
    "v_mfma_f32_16x16x32_bf16 v[52:55], v[236:239], v[36:39], v[52:55]\n"
    "s_waitcnt lgkmcnt(0)\n"
    "s_barrier\n"
    "ds_read_b128 v[32:35], v6\n"
    "ds_read_b128 v[36:39], v6 offset:256\n"
    "v_mfma_f32_16x16x32_bf16 v[40:43], v[240:243], v[16:19], v[40:43]\n"
    "v_mfma_f32_16x16x32_bf16 v[48:51], v[244:247], v[16:19], v[48:51]\n"
    "ds_read_b128 v[16:19], v12 offset:28672\n"
    "v_mfma_f32_16x16x32_bf16 v[44:47], v[240:243], v[20:23], v[44:47]\n"
    "v_mfma_f32_16x16x32_bf16 v[52:55], v[244:247], v[20:23], v[52:55]\n"
    "ds_read_b128 v[20:23], v12 offset:29184\n"
    "s_add_u32 m0, s75, 0\n"
    "s_mov_b32 exec_hi, 0\n"
    "buffer_load_dwordx4 v139, s[8:11], s68 offen offset:3072 sc1 lds\n"
    "s_mov_b32 exec_hi, -1\n"
    "v_mfma_f32_16x16x32_bf16 v[40:43], v[248:251], v[24:27], v[40:43]\n"
    "ds_read_b128 v[80:83], v6 offset:2048\n"
    "v_mfma_f32_16x16x32_bf16 v[48:51], v[252:255], v[24:27], v[48:51]\n"
    "ds_read_b128 v[24:27], v12 offset:30720\n"
    "v_mfma_f32_16x16x32_bf16 v[44:47], v[248:251], v[28:31], v[44:47]\n"
    "s_waitcnt lgkmcnt(4)\n"
    "v_mfma_f32_16x16x32_bf16 v[52:55], v[252:255], v[28:31], v[52:55]\n"
    "ds_read_b128 v[28:31], v12 offset:31232\n"
    "buffer_load_dwordx4 v136, s[8:11], s68 offen sc1 lds\n"
    "v_mfma_f32_16x16x32_bf16 v[40:43], v[72:75], v[32:35], v[40:43]\n"
    "ds_read_b128 v[84:87], v6 offset:2304\n"
    "v_mfma_f32_16x16x32_bf16 v[48:51], v[76:79], v[32:35], v[48:51]\n"
    "ds_read_b128 v[32:35], v12 offset:32768\n"
    "v_mfma_f32_16x16x32_bf16 v[44:47], v[72:75], v[36:39], v[44:47]\n"
    "s_waitcnt lgkmcnt(5)\n"
    "v_mfma_f32_16x16x32_bf16 v[52:55], v[76:79], v[36:39], v[52:55]\n"
    "ds_read_b128 v[36:39], v12 offset:33280\n"
    "buffer_load_dwordx4 v137, s[8:11], s68 offen offset:1024 sc1 lds\n"
    "v_mfma_f32_16x16x32_bf16 v[56:59], v[144:147], v[16:19], 0\n"
    "v_mfma_f32_16x16x32_bf16 v[64:67], v[148:151], v[16:19], 0\n"
    "ds_read_b128 v[16:19], v12 offset:34816\n"
    "v_mfma_f32_16x16x32_bf16 v[60:63], v[144:147], v[20:23], 0\n"
    "s_waitcnt lgkmcnt(4)\n"
    "v_mfma_f32_16x16x32_bf16 v[68:71], v[148:151], v[20:23], 0\n"
    "ds_read_b128 v[20:23], v12 offset:35328\n"
    "buffer_load_dwordx4 v138, s[8:11], s68 offen offset:2048 sc1 lds\n"
    "v_mul_f32 v88, v40, v40\n"
    "v_mul_f32 v90, v41, v41\n"
    "v_mfma_f32_16x16x32_bf16 v[56:59], v[152:155], v[24:27], v[56:59]\n"
    "v_fma_f32 v88, v88, v81, v80\n"
    "v_fma_f32 v90, v90, v81, v80\n"
    "v_mfma_f32_16x16x32_bf16 v[64:67], v[156:159], v[24:27], v[64:67]\n"
    "ds_read_b128 v[24:27], v12 offset:36864\n"
    "v_mul_f32 v88, v40, v88\n"
    "v_mfma_f32_16x16x32_bf16 v[60:63], v[152:155], v[28:31], v[60:63]\n"
    "s_waitcnt lgkmcnt(3)\n"
    "v_mul_f32 v90, v41, v90\n"
    "v_exp_f32 v88, v88\n"
    "v_mfma_f32_16x16x32_bf16 v[68:71], v[156:159], v[28:31], v[68:71]\n"
    "ds_read_b128 v[28:31], v12 offset:37376\n"
    "s_add_u32 m0, s77, 4096\n"
    "s_nop 0\n"
    "buffer_load_dwordx4 v2, s[8:11], s35 offen sc1 lds\n"
    "v_mfma_f32_16x16x32_bf16 v[56:59], v[160:163], v[32:35], v[56:59]\n"
    "v_exp_f32 v90, v90\n"
    "v_mul_f32 v89, v40, v48\n"
    "v_mfma_f32_16x16x32_bf16 v[64:67], v[164:167], v[32:35], v[64:67]\n"
    "ds_read_b128 v[32:35], v12 offset:38912\n"
    "v_mul_f32 v91, v41, v49\n"
    "v_fma_f32 v88, v88, v82, v82\n"
    "v_mfma_f32_16x16x32_bf16 v[60:63], v[160:163], v[36:39], v[60:63]\n"
    "s_waitcnt lgkmcnt(3)\n"
    "v_fma_f32 v90, v90, v82, v82\n"
    "v_mfma_f32_16x16x32_bf16 v[68:71], v[164:167], v[36:39], v[68:71]\n"
    "ds_read_b128 v[36:39], v12 offset:39424\n"
    "buffer_load_dwordx4 v2, s[8:11], s35 offen offset:1024 sc1 lds\n"
    "v_rcp_f32 v88, v88\n"
    "v_mfma_f32_16x16x32_bf16 v[56:59], v[168:171], v[16:19], v[56:59]\n"
    "v_rcp_f32 v90, v90\n"
    "v_mfma_f32_16x16x32_bf16 v[64:67], v[172:175], v[16:19], v[64:67]\n"
    "ds_read_b128 v[16:19], v12 offset:40960\n"
    "v_mul_f32 v89, v89, v88\n"
    "v_mul_f32 v91, v91, v90\n"
    "v_mfma_f32_16x16x32_bf16 v[60:63], v[168:171], v[20:23], v[60:63]\n"
    "s_waitcnt lgkmcnt(3)\n"
    "v_cvt_pk_bf16_f32 v96, v89, v91\n"
    "v_mul_f32 v92, v42, v42\n"
    "v_mfma_f32_16x16x32_bf16 v[68:71], v[172:175], v[20:23], v[68:71]\n"
    "ds_read_b128 v[20:23], v12 offset:41472\n"
    "v_mul_f32 v94, v43, v43\n"
    "v_mfma_f32_16x16x32_bf16 v[56:59], v[176:179], v[24:27], v[56:59]\n"
    "v_fma_f32 v92, v92, v81, v80\n"
    "v_fma_f32 v94, v94, v81, v80\n"
    "v_mfma_f32_16x16x32_bf16 v[64:67], v[180:183], v[24:27], v[64:67]\n"
    "ds_read_b128 v[24:27], v12 offset:43008\n"
    "buffer_load_dwordx4 v2, s[8:11], s35 offen offset:2048 sc1 lds\n"
    "v_mul_f32 v92, v42, v92\n"
    "v_mul_f32 v94, v43, v94\n"
    "v_mfma_f32_16x16x32_bf16 v[60:63], v[176:179], v[28:31], v[60:63]\n"
    "s_waitcnt lgkmcnt(3)\n"
    "v_exp_f32 v92, v92\n"
    "v_mfma_f32_16x16x32_bf16 v[68:71], v[180:183], v[28:31], v[68:71]\n"
    "ds_read_b128 v[28:31], v12 offset:43520\n"
    "v_exp_f32 v94, v94\n"
    "v_mfma_f32_16x16x32_bf16 v[56:59], v[184:187], v[32:35], v[56:59]\n"
    "v_mul_f32 v93, v42, v50\n"
    "v_mfma_f32_16x16x32_bf16 v[64:67], v[188:191], v[32:35], v[64:67]\n"
    "ds_read_b128 v[32:35], v12 offset:45056\n"
    "buffer_load_dwordx4 v2, s[8:11], s35 offen offset:3072 sc1 lds\n"
    "v_mul_f32 v95, v43, v51\n"
    "v_fma_f32 v92, v92, v82, v82\n"
    "v_mfma_f32_16x16x32_bf16 v[60:63], v[184:187], v[36:39], v[60:63]\n"
    "s_waitcnt lgkmcnt(3)\n"
    "v_fma_f32 v94, v94, v82, v82\n"
    "v_mfma_f32_16x16x32_bf16 v[68:71], v[188:191], v[36:39], v[68:71]\n"
    "ds_read_b128 v[36:39], v12 offset:45568\n"
    "v_rcp_f32 v92, v92\n"
    "v_mfma_f32_16x16x32_bf16 v[56:59], v[192:195], v[16:19], v[56:59]\n"
    "v_rcp_f32 v94, v94\n"
    "v_mfma_f32_16x16x32_bf16 v[64:67], v[196:199], v[16:19], v[64:67]\n"
    "ds_read_b128 v[16:19], v12 offset:47104\n"
    "v_mul_f32 v93, v93, v92\n"
    "v_mul_f32 v95, v95, v94\n"
    "v_mfma_f32_16x16x32_bf16 v[60:63], v[192:195], v[20:23], v[60:63]\n"
    "s_waitcnt lgkmcnt(3)\n"
    "v_cvt_pk_bf16_f32 v97, v93, v95\n"
    "v_mul_f32 v88, v44, v44\n"
    "v_mfma_f32_16x16x32_bf16 v[68:71], v[196:199], v[20:23], v[68:71]\n"
    "ds_read_b128 v[20:23], v12 offset:47616\n"
    "s_add_u32 m0, s77, 8192\n"
    "s_nop 0\n"
    "buffer_load_dwordx4 v2, s[8:11], s36 offen sc1 lds\n"
    "v_mul_f32 v90, v45, v45\n"
    "v_fma_f32 v88, v88, v85, v84\n"
    "v_mfma_f32_16x16x32_bf16 v[56:59], v[200:203], v[24:27], v[56:59]\n"
    "v_fma_f32 v90, v90, v85, v84\n"
    "v_mul_f32 v88, v44, v88\n"
    "v_mfma_f32_16x16x32_bf16 v[64:67], v[204:207], v[24:27], v[64:67]\n"
    "ds_read_b128 v[24:27], v12 offset:49152\n"
    "v_mul_f32 v90, v45, v90\n"
    "v_mfma_f32_16x16x32_bf16 v[60:63], v[200:203], v[28:31], v[60:63]\n"
    "s_waitcnt lgkmcnt(3)\n"
    "v_exp_f32 v88, v88\n"
    "v_mfma_f32_16x16x32_bf16 v[68:71], v[204:207], v[28:31], v[68:71]\n"
    "ds_read_b128 v[28:31], v12 offset:49664\n"
    "buffer_load_dwordx4 v2, s[8:11], s36 offen offset:1024 sc1 lds\n"
    "v_exp_f32 v90, v90\n"
    "v_mfma_f32_16x16x32_bf16 v[56:59], v[208:211], v[32:35], v[56:59]\n"
    "v_mul_f32 v89, v44, v52\n"
    "v_mul_f32 v91, v45, v53\n"
    "v_mfma_f32_16x16x32_bf16 v[64:67], v[212:215], v[32:35], v[64:67]\n"
    "ds_read_b128 v[32:35], v12 offset:51200\n"
    "v_fma_f32 v88, v88, v86, v86\n"
    "v_fma_f32 v90, v90, v86, v86\n"
    "v_mfma_f32_16x16x32_bf16 v[60:63], v[208:211], v[36:39], v[60:63]\n"
    "s_waitcnt lgkmcnt(3)\n"
    "v_rcp_f32 v88, v88\n"
    "v_mfma_f32_16x16x32_bf16 v[68:71], v[212:215], v[36:39], v[68:71]\n"
    "ds_read_b128 v[36:39], v12 offset:51712\n"
    "v_rcp_f32 v90, v90\n"
    "v_mfma_f32_16x16x32_bf16 v[56:59], v[216:219], v[16:19], v[56:59]\n"
    "v_mul_f32 v89, v89, v88\n"
    "v_mfma_f32_16x16x32_bf16 v[64:67], v[220:223], v[16:19], v[64:67]\n"
    "ds_read_b128 v[16:19], v12 offset:53248\n"
    "buffer_load_dwordx4 v2, s[8:11], s36 offen offset:2048 sc1 lds\n"
    "v_mul_f32 v91, v91, v90\n"
    "v_cvt_pk_bf16_f32 v98, v89, v91\n"
    "v_mfma_f32_16x16x32_bf16 v[60:63], v[216:219], v[20:23], v[60:63]\n"
    "s_waitcnt lgkmcnt(3)\n"
    "v_mul_f32 v92, v46, v46\n"
    "v_mul_f32 v94, v47, v47\n"
    "v_mfma_f32_16x16x32_bf16 v[68:71], v[220:223], v[20:23], v[68:71]\n"
    "ds_read_b128 v[20:23], v12 offset:53760\n"
    "v_fma_f32 v92, v92, v85, v84\n"
    "v_mfma_f32_16x16x32_bf16 v[56:59], v[224:227], v[24:27], v[56:59]\n"
    "v_fma_f32 v94, v94, v85, v84\n"
    "v_mul_f32 v92, v46, v92\n"
    "v_mfma_f32_16x16x32_bf16 v[64:67], v[228:231], v[24:27], v[64:67]\n"
    "ds_read_b128 v[24:27], v12 offset:55296\n"
    "buffer_load_dwordx4 v2, s[8:11], s36 offen offset:3072 sc1 lds\n"
    "v_mul_f32 v94, v47, v94\n"
    "v_mfma_f32_16x16x32_bf16 v[60:63], v[224:227], v[28:31], v[60:63]\n"
    "s_waitcnt lgkmcnt(3)\n"
    "v_exp_f32 v92, v92\n"
    "v_mfma_f32_16x16x32_bf16 v[68:71], v[228:231], v[28:31], v[68:71]\n"
    "ds_read_b128 v[28:31], v12 offset:55808\n"
    "v_exp_f32 v94, v94\n"
    "v_mfma_f32_16x16x32_bf16 v[56:59], v[232:235], v[32:35], v[56:59]\n"
    "v_mul_f32 v93, v46, v54\n"
    "v_mul_f32 v95, v47, v55\n"
    "v_mfma_f32_16x16x32_bf16 v[64:67], v[236:239], v[32:35], v[64:67]\n"
    "v_fma_f32 v92, v92, v86, v86\n"
    "v_fma_f32 v94, v94, v86, v86\n"
    "v_mfma_f32_16x16x32_bf16 v[60:63], v[232:235], v[36:39], v[60:63]\n"
    "s_waitcnt lgkmcnt(2)\n"
    "v_rcp_f32 v92, v92\n"
    "v_mfma_f32_16x16x32_bf16 v[68:71], v[236:239], v[36:39], v[68:71]\n"
    "v_rcp_f32 v94, v94\n"
    "s_waitcnt vmcnt(8) lgkmcnt(0)\n"
    "s_barrier\n"
    "ds_read_b128 v[32:35], v6 offset:512\n"
    "ds_read_b128 v[36:39], v6 offset:768\n"
    "s_cmp_eq_u32 s52, 7\n"
    "s_cbranch_scc0 .Lfb_L128\n"
    "s_mov_b64 exec, 1\n"
    "v_mov_b32 v14, s53\n"
    "buffer_store_dword v14, off, s[8:11], s17 sc1\n"
    "s_mov_b64 exec, -1\n"
    ".Lfb_L128:\n"
    "v_mfma_f32_16x16x32_bf16 v[56:59], v[240:243], v[16:19], v[56:59]\n"
    "v_mul_f32 v93, v93, v92\n"
    "v_mfma_f32_16x16x32_bf16 v[64:67], v[244:247], v[16:19], v[64:67]\n"
    "ds_read_b128 v[16:19], v12\n"
    "v_mul_f32 v95, v95, v94\n"
    "v_cvt_pk_bf16_f32 v99, v93, v95\n"
    "v_mfma_f32_16x16x32_bf16 v[60:63], v[240:243], v[20:23], v[60:63]\n"
    "v_permlane16_swap_b32 v96, v98\n"
    "v_permlane16_swap_b32 v97, v99\n"
    "buffer_store_dwordx4 v[96:99], v13, s[8:11], s71 offen sc1\n"
    "v_mfma_f32_16x16x32_bf16 v[68:71], v[244:247], v[20:23], v[68:71]\n"
    "ds_read_b128 v[20:23], v12 offset:512\n"
    "s_add_u32 m0, s75, 28672\n"
    "s_mov_b32 exec_hi, 0\n"
    "buffer_load_dwordx4 v139, s[8:11], s70 offen offset:3072 sc1 lds\n"
    "s_mov_b32 exec_hi, -1\n"
    "v_mfma_f32_16x16x32_bf16 v[56:59], v[248:251], v[24:27], v[56:59]\n"
    "ds_read_b128 v[80:83], v6 offset:2560\n"
    "v_mfma_f32_16x16x32_bf16 v[64:67], v[252:255], v[24:27], v[64:67]\n"
    "ds_read_b128 v[24:27], v12 offset:2048\n"
    "v_mfma_f32_16x16x32_bf16 v[60:63], v[248:251], v[28:31], v[60:63]\n"
    "s_waitcnt lgkmcnt(4)\n"
    "v_mfma_f32_16x16x32_bf16 v[68:71], v[252:255], v[28:31], v[68:71]\n"
    "ds_read_b128 v[28:31], v12 offset:2560\n"
    "buffer_load_dwordx4 v136, s[8:11], s70 offen sc1 lds\n"
    "v_mfma_f32_16x16x32_bf16 v[56:59], v[72:75], v[32:35], v[56:59]\n"
    "ds_read_b128 v[84:87], v6 offset:2816\n"
    "v_mfma_f32_16x16x32_bf16 v[64:67], v[76:79], v[32:35], v[64:67]\n"
    "ds_read_b128 v[32:35], v12 offset:4096\n"
    "v_mfma_f32_16x16x32_bf16 v[60:63], v[72:75], v[36:39], v[60:63]\n"
    "s_waitcnt lgkmcnt(5)\n"
    "v_mfma_f32_16x16x32_bf16 v[68:71], v[76:79], v[36:39], v[68:71]\n"
    "ds_read_b128 v[36:39], v12 offset:4608\n"
    "buffer_load_dwordx4 v137, s[8:11], s70 offen offset:1024 sc1 lds\n"
    "v_mfma_f32_16x16x32_bf16 v[40:43], v[144:147], v[16:19], 0\n"
    "v_mfma_f32_16x16x32_bf16 v[48:51], v[148:151], v[16:19], 0\n"
    "ds_read_b128 v[16:19], v12 offset:6144\n"
    "v_mfma_f32_16x16x32_bf16 v[44:47], v[144:147], v[20:23], 0\n"
    "s_waitcnt lgkmcnt(4)\n"
    "v_mfma_f32_16x16x32_bf16 v[52:55], v[148:151], v[20:23], 0\n"
    "ds_read_b128 v[20:23], v12 offset:6656\n"
    "buffer_load_dwordx4 v138, s[8:11], s70 offen offset:2048 sc1 lds\n"
    "v_mul_f32 v88, v56, v56\n"
    "v_mul_f32 v90, v57, v57\n"
    "v_mfma_f32_16x16x32_bf16 v[40:43], v[152:155], v[24:27], v[40:43]\n"
    "v_fma_f32 v88, v88, v81, v80\n"
    "v_fma_f32 v90, v90, v81, v80\n"
    "v_mfma_f32_16x16x32_bf16 v[48:51], v[156:159], v[24:27], v[48:51]\n"
    "ds_read_b128 v[24:27], v12 offset:8192\n"
    "v_mul_f32 v88, v56, v88\n"
    "v_mfma_f32_16x16x32_bf16 v[44:47], v[152:155], v[28:31], v[44:47]\n"
    "s_waitcnt lgkmcnt(3)\n"
    "v_mul_f32 v90, v57, v90\n"
    "v_exp_f32 v88, v88\n"
    "v_mfma_f32_16x16x32_bf16 v[52:55], v[156:159], v[28:31], v[52:55]\n"
    "ds_read_b128 v[28:31], v12 offset:8704\n"
    "buffer_load_dwordx4 v[100:103], v2, s[8:11], s37 offen sc1\n"
    "v_mfma_f32_16x16x32_bf16 v[40:43], v[160:163], v[32:35], v[40:43]\n"
    "v_exp_f32 v90, v90\n"
    "v_mul_f32 v89, v56, v64\n"
    "v_mfma_f32_16x16x32_bf16 v[48:51], v[164:167], v[32:35], v[48:51]\n"
    "ds_read_b128 v[32:35], v12 offset:10240\n"
    "v_mul_f32 v91, v57, v65\n"
    "v_fma_f32 v88, v88, v82, v82\n"
    "v_mfma_f32_16x16x32_bf16 v[44:47], v[160:163], v[36:39], v[44:47]\n"
    "s_waitcnt lgkmcnt(3)\n"
    "v_fma_f32 v90, v90, v82, v82\n"
    "v_mfma_f32_16x16x32_bf16 v[52:55], v[164:167], v[36:39], v[52:55]\n"
    "ds_read_b128 v[36:39], v12 offset:10752\n"
    "buffer_load_dwordx4 v[104:107], v2, s[8:11], s37 offen offset:1024 sc1\n"
    "v_rcp_f32 v88, v88\n"
    "v_mfma_f32_16x16x32_bf16 v[40:43], v[168:171], v[16:19], v[40:43]\n"
    "v_rcp_f32 v90, v90\n"
    "v_mfma_f32_16x16x32_bf16 v[48:51], v[172:175], v[16:19], v[48:51]\n"
    "ds_read_b128 v[16:19], v12 offset:12288\n"
    "v_mul_f32 v89, v89, v88\n"
    "v_mul_f32 v91, v91, v90\n"
    "v_mfma_f32_16x16x32_bf16 v[44:47], v[168:171], v[20:23], v[44:47]\n"
    "s_waitcnt lgkmcnt(3)\n"
    "v_cvt_pk_bf16_f32 v96, v89, v91\n"
    "v_mul_f32 v92, v58, v58\n"
    "v_mfma_f32_16x16x32_bf16 v[52:55], v[172:175], v[20:23], v[52:55]\n"
    "ds_read_b128 v[20:23], v12 offset:12800\n"
    "v_mul_f32 v94, v59, v59\n"
    "v_mfma_f32_16x16x32_bf16 v[40:43], v[176:179], v[24:27], v[40:43]\n"
    "v_fma_f32 v92, v92, v81, v80\n"
    "v_fma_f32 v94, v94, v81, v80\n"
    "v_mfma_f32_16x16x32_bf16 v[48:51], v[180:183], v[24:27], v[48:51]\n"
    "ds_read_b128 v[24:27], v12 offset:14336\n"
    "buffer_load_dwordx4 v[108:111], v2, s[8:11], s37 offen offset:2048 sc1\n"
    "v_mul_f32 v92, v58, v92\n"
    "v_mul_f32 v94, v59, v94\n"
    "v_mfma_f32_16x16x32_bf16 v[44:47], v[176:179], v[28:31], v[44:47]\n"
    "s_waitcnt lgkmcnt(3)\n"
    "v_exp_f32 v92, v92\n"
    "v_mfma_f32_16x16x32_bf16 v[52:55], v[180:183], v[28:31], v[52:55]\n"
    "ds_read_b128 v[28:31], v12 offset:14848\n"
    "v_exp_f32 v94, v94\n"
    "v_mfma_f32_16x16x32_bf16 v[40:43], v[184:187], v[32:35], v[40:43]\n"
    "v_mul_f32 v93, v58, v66\n"
    "v_mfma_f32_16x16x32_bf16 v[48:51], v[188:191], v[32:35], v[48:51]\n"
    "ds_read_b128 v[32:35], v12 offset:16384\n"
    "buffer_load_dwordx4 v[112:115], v2, s[8:11], s37 offen offset:3072 sc1\n"
    "v_mul_f32 v95, v59, v67\n"
    "v_fma_f32 v92, v92, v82, v82\n"
    "v_mfma_f32_16x16x32_bf16 v[44:47], v[184:187], v[36:39], v[44:47]\n"
    "s_waitcnt lgkmcnt(3)\n"
    "v_fma_f32 v94, v94, v82, v82\n"
    "v_mfma_f32_16x16x32_bf16 v[52:55], v[188:191], v[36:39], v[52:55]\n"
    "ds_read_b128 v[36:39], v12 offset:16896\n"
    "v_rcp_f32 v92, v92\n"
    "v_mfma_f32_16x16x32_bf16 v[40:43], v[192:195], v[16:19], v[40:43]\n"
    "v_rcp_f32 v94, v94\n"
    "v_mfma_f32_16x16x32_bf16 v[48:51], v[196:199], v[16:19], v[48:51]\n"
    "ds_read_b128 v[16:19], v12 offset:18432\n"
    "buffer_load_dword v142, v141, s[8:11], 0 offen sc1\n"
    "v_mul_f32 v93, v93, v92\n"
    "v_mul_f32 v95, v95, v94\n"
    "v_mfma_f32_16x16x32_bf16 v[44:47], v[192:195], v[20:23], v[44:47]\n"
    "s_waitcnt lgkmcnt(3)\n"
    "v_cvt_pk_bf16_f32 v97, v93, v95\n"
    "v_mul_f32 v88, v60, v60\n"
    "v_mfma_f32_16x16x32_bf16 v[52:55], v[196:199], v[20:23], v[52:55]\n"
    "ds_read_b128 v[20:23], v12 offset:18944\n"
    "buffer_load_dwordx4 v[116:119], v2, s[8:11], s38 offen sc1\n"
    "v_mul_f32 v90, v61, v61\n"
    "v_fma_f32 v88, v88, v85, v84\n"
    "v_mfma_f32_16x16x32_bf16 v[40:43], v[200:203], v[24:27], v[40:43]\n"
    "v_fma_f32 v90, v90, v85, v84\n"
    "v_mul_f32 v88, v60, v88\n"
    "v_mfma_f32_16x16x32_bf16 v[48:51], v[204:207], v[24:27], v[48:51]\n"
    "ds_read_b128 v[24:27], v12 offset:20480\n"
    "v_mul_f32 v90, v61, v90\n"
    "v_mfma_f32_16x16x32_bf16 v[44:47], v[200:203], v[28:31], v[44:47]\n"
    "s_waitcnt lgkmcnt(3)\n"
    "v_exp_f32 v88, v88\n"
    "v_mfma_f32_16x16x32_bf16 v[52:55], v[204:207], v[28:31], v[52:55]\n"
    "ds_read_b128 v[28:31], v12 offset:20992\n"
    "buffer_load_dwordx4 v[120:123], v2, s[8:11], s38 offen offset:1024 sc1\n"
    "v_exp_f32 v90, v90\n"
    "v_mfma_f32_16x16x32_bf16 v[40:43], v[208:211], v[32:35], v[40:43]\n"
    "v_mul_f32 v89, v60, v68\n"
    "v_mul_f32 v91, v61, v69\n"
    "v_mfma_f32_16x16x32_bf16 v[48:51], v[212:215], v[32:35], v[48:51]\n"
    "ds_read_b128 v[32:35], v12 offset:22528\n"
    "v_fma_f32 v88, v88, v86, v86\n"
    "v_fma_f32 v90, v90, v86, v86\n"
    "v_mfma_f32_16x16x32_bf16 v[44:47], v[208:211], v[36:39], v[44:47]\n"
    "s_waitcnt lgkmcnt(3)\n"
    "v_rcp_f32 v88, v88\n"
    "v_mfma_f32_16x16x32_bf16 v[52:55], v[212:215], v[36:39], v[52:55]\n"
    "ds_read_b128 v[36:39], v12 offset:23040\n"
    "v_rcp_f32 v90, v90\n"
    "v_mfma_f32_16x16x32_bf16 v[40:43], v[216:219], v[16:19], v[40:43]\n"
    "v_mul_f32 v89, v89, v88\n"
    "v_mfma_f32_16x16x32_bf16 v[48:51], v[220:223], v[16:19], v[48:51]\n"
    "ds_read_b128 v[16:19], v12 offset:24576\n"
    "buffer_load_dwordx4 v[124:127], v2, s[8:11], s38 offen offset:2048 sc1\n"
    "v_mul_f32 v91, v91, v90\n"
    "v_cvt_pk_bf16_f32 v98, v89, v91\n"
    "v_mfma_f32_16x16x32_bf16 v[44:47], v[216:219], v[20:23], v[44:47]\n"
    "s_waitcnt lgkmcnt(3)\n"
    "v_mul_f32 v92, v62, v62\n"
    "v_mul_f32 v94, v63, v63\n"
    "v_mfma_f32_16x16x32_bf16 v[52:55], v[220:223], v[20:23], v[52:55]\n"
    "ds_read_b128 v[20:23], v12 offset:25088\n"
    "v_fma_f32 v92, v92, v85, v84\n"
    "v_mfma_f32_16x16x32_bf16 v[40:43], v[224:227], v[24:27], v[40:43]\n"
    "v_fma_f32 v94, v94, v85, v84\n"
    "v_mul_f32 v92, v62, v92\n"
    "v_mfma_f32_16x16x32_bf16 v[48:51], v[228:231], v[24:27], v[48:51]\n"
    "ds_read_b128 v[24:27], v12 offset:26624\n"
    "buffer_load_dwordx4 v[128:131], v2, s[8:11], s38 offen offset:3072 sc1\n"
    "v_mul_f32 v94, v63, v94\n"
    "v_mfma_f32_16x16x32_bf16 v[44:47], v[224:227], v[28:31], v[44:47]\n"
    "s_waitcnt lgkmcnt(3)\n"
    "v_exp_f32 v92, v92\n"
    "v_mfma_f32_16x16x32_bf16 v[52:55], v[228:231], v[28:31], v[52:55]\n"
    "ds_read_b128 v[28:31], v12 offset:27136\n"
    "v_exp_f32 v94, v94\n"
    "v_mfma_f32_16x16x32_bf16 v[40:43], v[232:235], v[32:35], v[40:43]\n"
    "v_mul_f32 v93, v62, v70\n"
    "v_mul_f32 v95, v63, v71\n"
    "v_mfma_f32_16x16x32_bf16 v[48:51], v[236:239], v[32:35], v[48:51]\n"
    "v_fma_f32 v92, v92, v86, v86\n"
    "v_fma_f32 v94, v94, v86, v86\n"
    "v_mfma_f32_16x16x32_bf16 v[44:47], v[232:235], v[36:39], v[44:47]\n"
    "s_waitcnt lgkmcnt(2)\n"
    "v_rcp_f32 v92, v92\n"
    "v_mfma_f32_16x16x32_bf16 v[52:55], v[236:239], v[36:39], v[52:55]\n"
    "v_rcp_f32 v94, v94\n"
    "s_waitcnt vmcnt(4)\n"
    "v_xor_b32 v143, s53, v142\n"
    "v_cmp_ne_u32 vcc, 0, v143\n"
    "s_cbranch_vccnz .Lfb_S129\n"
    ".Lfb_B130:\n"
    "s_waitcnt lgkmcnt(0)\n"
    "s_barrier\n"
    "ds_read_b128 v[32:35], v6 offset:1024\n"
    "ds_read_b128 v[36:39], v6 offset:1280\n"
    "v_mfma_f32_16x16x32_bf16 v[40:43], v[240:243], v[16:19], v[40:43]\n"
    "v_mul_f32 v93, v93, v92\n"
    "v_mfma_f32_16x16x32_bf16 v[48:51], v[244:247], v[16:19], v[48:51]\n"
    "ds_read_b128 v[16:19], v12 offset:28672\n"
    "v_mul_f32 v95, v95, v94\n"
    "v_cvt_pk_bf16_f32 v99, v93, v95\n"
    "v_mfma_f32_16x16x32_bf16 v[44:47], v[240:243], v[20:23], v[44:47]\n"
    "v_permlane16_swap_b32 v96, v98\n"
    "v_permlane16_swap_b32 v97, v99\n"
    "buffer_store_dwordx4 v[96:99], v13, s[8:11], s72 offen sc1\n"
    "v_mfma_f32_16x16x32_bf16 v[52:55], v[244:247], v[20:23], v[52:55]\n"
    "ds_read_b128 v[20:23], v12 offset:29184\n"
    "v_mfma_f32_16x16x32_bf16 v[40:43], v[248:251], v[24:27], v[40:43]\n"
    "ds_read_b128 v[80:83], v6 offset:3072\n"
    "v_mfma_f32_16x16x32_bf16 v[48:51], v[252:255], v[24:27], v[48:51]\n"
    "ds_read_b128 v[24:27], v12 offset:30720\n"
    "buffer_load_dwordx4 v[132:135], v2, s[8:11], s39 offen sc1\n"
    "v_mfma_f32_16x16x32_bf16 v[44:47], v[248:251], v[28:31], v[44:47]\n"
    "s_waitcnt lgkmcnt(4)\n"
    "v_mfma_f32_16x16x32_bf16 v[52:55], v[252:255], v[28:31], v[52:55]\n"
    "ds_read_b128 v[28:31], v12 offset:31232\n"
    "v_mfma_f32_16x16x32_bf16 v[40:43], v[72:75], v[32:35], v[40:43]\n"
    "ds_read_b128 v[84:87], v6 offset:3328\n"
    "v_mfma_f32_16x16x32_bf16 v[48:51], v[76:79], v[32:35], v[48:51]\n"
    "ds_read_b128 v[32:35], v12 offset:32768\n"
    "buffer_load_dwordx4 v[136:139], v2, s[8:11], s39 offen offset:1024 sc1\n"
    "v_mfma_f32_16x16x32_bf16 v[44:47], v[72:75], v[36:39], v[44:47]\n"
    "s_waitcnt lgkmcnt(5)\n"
    "v_mfma_f32_16x16x32_bf16 v[52:55], v[76:79], v[36:39], v[52:55]\n"
    "ds_read_b128 v[36:39], v12 offset:33280\n"
    "v_mfma_f32_16x16x32_bf16 v[56:59], v[144:147], v[16:19], 0\n"
    "v_mfma_f32_16x16x32_bf16 v[64:67], v[148:151], v[16:19], 0\n"
    "ds_read_b128 v[16:19], v12 offset:34816\n"
    "v_mfma_f32_16x16x32_bf16 v[60:63], v[144:147], v[20:23], 0\n"
    "s_waitcnt lgkmcnt(4)\n"
    "v_mfma_f32_16x16x32_bf16 v[68:71], v[148:151], v[20:23], 0\n"
    "ds_read_b128 v[20:23], v12 offset:35328\n"
    "buffer_load_dwordx4 v[144:147], v9, s[8:11], s40 offen sc1\n"
    "v_mul_f32 v88, v40, v40\n"
    "v_mul_f32 v90, v41, v41\n"
    "v_mfma_f32_16x16x32_bf16 v[56:59], v[152:155], v[24:27], v[56:59]\n"
    "v_fma_f32 v88, v88, v81, v80\n"
    "v_fma_f32 v90, v90, v81, v80\n"
    "v_mfma_f32_16x16x32_bf16 v[64:67], v[156:159], v[24:27], v[64:67]\n"
    "ds_read_b128 v[24:27], v12 offset:36864\n"
    "v_mul_f32 v88, v40, v88\n"
    "v_mfma_f32_16x16x32_bf16 v[60:63], v[152:155], v[28:31], v[60:63]\n"
    "s_waitcnt lgkmcnt(3)\n"
    "v_mul_f32 v90, v41, v90\n"
    "v_exp_f32 v88, v88\n"
    "v_mfma_f32_16x16x32_bf16 v[68:71], v[156:159], v[28:31], v[68:71]\n"
    "ds_read_b128 v[28:31], v12 offset:37376\n"
    "buffer_load_dwordx4 v[148:151], v9, s[8:11], s40 offen offset:512 sc1\n"
    "v_mfma_f32_16x16x32_bf16 v[56:59], v[160:163], v[32:35], v[56:59]\n"
    "v_exp_f32 v90, v90\n"
    "v_mul_f32 v89, v40, v48\n"
    "v_mfma_f32_16x16x32_bf16 v[64:67], v[164:167], v[32:35], v[64:67]\n"
    "ds_read_b128 v[32:35], v12 offset:38912\n"
    "v_mul_f32 v91, v41, v49\n"
    "v_fma_f32 v88, v88, v82, v82\n"
    "v_mfma_f32_16x16x32_bf16 v[60:63], v[160:163], v[36:39], v[60:63]\n"
    "s_waitcnt lgkmcnt(3)\n"
    "v_fma_f32 v90, v90, v82, v82\n"
    "v_mfma_f32_16x16x32_bf16 v[68:71], v[164:167], v[36:39], v[68:71]\n"
    "ds_read_b128 v[36:39], v12 offset:39424\n"
    "buffer_load_dwordx4 v[152:155], v9, s[8:11], s41 offen sc1\n"
    "v_rcp_f32 v88, v88\n"
    "v_mfma_f32_16x16x32_bf16 v[56:59], v[168:171], v[16:19], v[56:59]\n"
    "v_rcp_f32 v90, v90\n"
    "v_mfma_f32_16x16x32_bf16 v[64:67], v[172:175], v[16:19], v[64:67]\n"
    "ds_read_b128 v[16:19], v12 offset:40960\n"
    "v_mul_f32 v89, v89, v88\n"
    "v_mul_f32 v91, v91, v90\n"
    "v_mfma_f32_16x16x32_bf16 v[60:63], v[168:171], v[20:23], v[60:63]\n"
    "s_waitcnt lgkmcnt(3)\n"
    "v_cvt_pk_bf16_f32 v96, v89, v91\n"
    "v_mul_f32 v92, v42, v42\n"
    "v_mfma_f32_16x16x32_bf16 v[68:71], v[172:175], v[20:23], v[68:71]\n"
    "ds_read_b128 v[20:23], v12 offset:41472\n"
    "v_mul_f32 v94, v43, v43\n"
    "v_mfma_f32_16x16x32_bf16 v[56:59], v[176:179], v[24:27], v[56:59]\n"
    "v_fma_f32 v92, v92, v81, v80\n"
    "v_fma_f32 v94, v94, v81, v80\n"
    "v_mfma_f32_16x16x32_bf16 v[64:67], v[180:183], v[24:27], v[64:67]\n"
    "ds_read_b128 v[24:27], v12 offset:43008\n"
    "buffer_load_dwordx4 v[156:159], v9, s[8:11], s41 offen offset:512 sc1\n"
    "v_mul_f32 v92, v42, v92\n"
    "v_mul_f32 v94, v43, v94\n"
    "v_mfma_f32_16x16x32_bf16 v[60:63], v[176:179], v[28:31], v[60:63]\n"
    "s_waitcnt lgkmcnt(3)\n"
    "v_exp_f32 v92, v92\n"
    "v_mfma_f32_16x16x32_bf16 v[68:71], v[180:183], v[28:31], v[68:71]\n"
    "ds_read_b128 v[28:31], v12 offset:43520\n"
    "v_exp_f32 v94, v94\n"
    "v_mfma_f32_16x16x32_bf16 v[56:59], v[184:187], v[32:35], v[56:59]\n"
    "v_mul_f32 v93, v42, v50\n"
    "v_mfma_f32_16x16x32_bf16 v[64:67], v[188:191], v[32:35], v[64:67]\n"
    "ds_read_b128 v[32:35], v12 offset:45056\n"
    "buffer_load_dwordx4 v[160:163], v9, s[8:11], s42 offen sc1\n"
    "v_mul_f32 v95, v43, v51\n"
    "v_fma_f32 v92, v92, v82, v82\n"
    "v_mfma_f32_16x16x32_bf16 v[60:63], v[184:187], v[36:39], v[60:63]\n"
    "s_waitcnt lgkmcnt(3)\n"
    "v_fma_f32 v94, v94, v82, v82\n"
    "v_mfma_f32_16x16x32_bf16 v[68:71], v[188:191], v[36:39], v[68:71]\n"
    "ds_read_b128 v[36:39], v12 offset:45568\n"
    "v_rcp_f32 v92, v92\n"
    "v_mfma_f32_16x16x32_bf16 v[56:59], v[192:195], v[16:19], v[56:59]\n"
    "v_rcp_f32 v94, v94\n"
    "v_mfma_f32_16x16x32_bf16 v[64:67], v[196:199], v[16:19], v[64:67]\n"
    "ds_read_b128 v[16:19], v12 offset:47104\n"
    "buffer_load_dwordx4 v[164:167], v9, s[8:11], s42 offen offset:512 sc1\n"
    "v_mul_f32 v93, v93, v92\n"
    "v_mul_f32 v95, v95, v94\n"
    "v_mfma_f32_16x16x32_bf16 v[60:63], v[192:195], v[20:23], v[60:63]\n"
    "s_waitcnt lgkmcnt(3)\n"
    "v_cvt_pk_bf16_f32 v97, v93, v95\n"
    "v_mul_f32 v88, v44, v44\n"
    "v_mfma_f32_16x16x32_bf16 v[68:71], v[196:199], v[20:23], v[68:71]\n"
    "ds_read_b128 v[20:23], v12 offset:47616\n"
    "v_mul_f32 v90, v45, v45\n"
    "v_fma_f32 v88, v88, v85, v84\n"
    "v_mfma_f32_16x16x32_bf16 v[56:59], v[200:203], v[24:27], v[56:59]\n"
    "v_fma_f32 v90, v90, v85, v84\n"
    "v_mul_f32 v88, v44, v88\n"
    "v_mfma_f32_16x16x32_bf16 v[64:67], v[204:207], v[24:27], v[64:67]\n"
    "ds_read_b128 v[24:27], v12 offset:49152\n"
    "buffer_load_dwordx4 v[192:195], v10, s[8:11], s40 offen sc1\n"
    "v_mul_f32 v90, v45, v90\n"
    "v_mfma_f32_16x16x32_bf16 v[60:63], v[200:203], v[28:31], v[60:63]\n"
    "s_waitcnt lgkmcnt(3)\n"
    "v_exp_f32 v88, v88\n"
    "v_mfma_f32_16x16x32_bf16 v[68:71], v[204:207], v[28:31], v[68:71]\n"
    "ds_read_b128 v[28:31], v12 offset:49664\n"
    "v_exp_f32 v90, v90\n"
    "v_mfma_f32_16x16x32_bf16 v[56:59], v[208:211], v[32:35], v[56:59]\n"
    "v_mul_f32 v89, v44, v52\n"
    "v_mul_f32 v91, v45, v53\n"
    "v_mfma_f32_16x16x32_bf16 v[64:67], v[212:215], v[32:35], v[64:67]\n"
    "ds_read_b128 v[32:35], v12 offset:51200\n"
    "buffer_load_dwordx4 v[196:199], v10, s[8:11], s40 offen offset:512 sc1\n"
    "v_fma_f32 v88, v88, v86, v86\n"
    "v_fma_f32 v90, v90, v86, v86\n"
    "v_mfma_f32_16x16x32_bf16 v[60:63], v[208:211], v[36:39], v[60:63]\n"
    "s_waitcnt lgkmcnt(3)\n"
    "v_rcp_f32 v88, v88\n"
    "v_mfma_f32_16x16x32_bf16 v[68:71], v[212:215], v[36:39], v[68:71]\n"
    "ds_read_b128 v[36:39], v12 offset:51712\n"
    "v_rcp_f32 v90, v90\n"
    "v_mfma_f32_16x16x32_bf16 v[56:59], v[216:219], v[16:19], v[56:59]\n"
    "v_mul_f32 v89, v89, v88\n"
    "v_mfma_f32_16x16x32_bf16 v[64:67], v[220:223], v[16:19], v[64:67]\n"
    "ds_read_b128 v[16:19], v12 offset:53248\n"
    "buffer_load_dwordx4 v[200:203], v10, s[8:11], s41 offen sc1\n"
    "v_mul_f32 v91, v91, v90\n"
    "v_cvt_pk_bf16_f32 v98, v89, v91\n"
    "v_mfma_f32_16x16x32_bf16 v[60:63], v[216:219], v[20:23], v[60:63]\n"
    "s_waitcnt lgkmcnt(3)\n"
    "v_mul_f32 v92, v46, v46\n"
    "v_mul_f32 v94, v47, v47\n"
    "v_mfma_f32_16x16x32_bf16 v[68:71], v[220:223], v[20:23], v[68:71]\n"
    "ds_read_b128 v[20:23], v12 offset:53760\n"
    "v_fma_f32 v92, v92, v85, v84\n"
    "v_mfma_f32_16x16x32_bf16 v[56:59], v[224:227], v[24:27], v[56:59]\n"
    "v_fma_f32 v94, v94, v85, v84\n"
    "v_mul_f32 v92, v46, v92\n"
    "v_mfma_f32_16x16x32_bf16 v[64:67], v[228:231], v[24:27], v[64:67]\n"
    "ds_read_b128 v[24:27], v12 offset:55296\n"
    "buffer_load_dwordx4 v[204:207], v10, s[8:11], s41 offen offset:512 sc1\n"
    "v_mul_f32 v94, v47, v94\n"
    "v_mfma_f32_16x16x32_bf16 v[60:63], v[224:227], v[28:31], v[60:63]\n"
    "s_waitcnt lgkmcnt(3)\n"
    "v_exp_f32 v92, v92\n"
    "v_mfma_f32_16x16x32_bf16 v[68:71], v[228:231], v[28:31], v[68:71]\n"
    "ds_read_b128 v[28:31], v12 offset:55808\n"
    "v_exp_f32 v94, v94\n"
    "v_mfma_f32_16x16x32_bf16 v[56:59], v[232:235], v[32:35], v[56:59]\n"
    "v_mul_f32 v93, v46, v54\n"
    "v_mul_f32 v95, v47, v55\n"
    "v_mfma_f32_16x16x32_bf16 v[64:67], v[236:239], v[32:35], v[64:67]\n"
    "buffer_load_dwordx4 v[208:211], v10, s[8:11], s42 offen sc1\n"
    "v_fma_f32 v92, v92, v86, v86\n"
    "v_fma_f32 v94, v94, v86, v86\n"
    "v_mfma_f32_16x16x32_bf16 v[60:63], v[232:235], v[36:39], v[60:63]\n"
    "s_waitcnt lgkmcnt(2)\n"
    "v_rcp_f32 v92, v92\n"
    "v_mfma_f32_16x16x32_bf16 v[68:71], v[236:239], v[36:39], v[68:71]\n"
    "v_rcp_f32 v94, v94\n"
    "s_waitcnt lgkmcnt(0)\n"
    "s_barrier\n"
    "ds_read_b128 v[32:35], v6 offset:1536\n"
    "ds_read_b128 v[36:39], v6 offset:1792\n"
    "v_mfma_f32_16x16x32_bf16 v[56:59], v[240:243], v[16:19], v[56:59]\n"
    "v_mul_f32 v93, v93, v92\n"
    "v_mfma_f32_16x16x32_bf16 v[64:67], v[244:247], v[16:19], v[64:67]\n"
    "v_mul_f32 v95, v95, v94\n"
    "v_cvt_pk_bf16_f32 v99, v93, v95\n"
    "v_mfma_f32_16x16x32_bf16 v[60:63], v[240:243], v[20:23], v[60:63]\n"
    "v_permlane16_swap_b32 v96, v98\n"
    "v_permlane16_swap_b32 v97, v99\n"
    "buffer_store_dwordx4 v[96:99], v13, s[8:11], s73 offen sc1\n"
    "v_mfma_f32_16x16x32_bf16 v[68:71], v[244:247], v[20:23], v[68:71]\n"
    "v_mfma_f32_16x16x32_bf16 v[56:59], v[248:251], v[24:27], v[56:59]\n"
    "ds_read_b128 v[80:83], v6 offset:3584\n"
    "v_mfma_f32_16x16x32_bf16 v[64:67], v[252:255], v[24:27], v[64:67]\n"
    "buffer_load_dwordx4 v[212:215], v10, s[8:11], s42 offen offset:512 sc1\n"
    "v_mfma_f32_16x16x32_bf16 v[60:63], v[248:251], v[28:31], v[60:63]\n"
    "s_waitcnt lgkmcnt(1)\n"
    "v_mfma_f32_16x16x32_bf16 v[68:71], v[252:255], v[28:31], v[68:71]\n"
    "v_mfma_f32_16x16x32_bf16 v[56:59], v[72:75], v[32:35], v[56:59]\n"
    "ds_read_b128 v[84:87], v6 offset:3840\n"
    "v_mfma_f32_16x16x32_bf16 v[64:67], v[76:79], v[32:35], v[64:67]\n"
    "buffer_load_dwordx4 v[168:171], v2, s[8:11], s39 offen offset:2048 sc1\n"
    "v_mfma_f32_16x16x32_bf16 v[60:63], v[72:75], v[36:39], v[60:63]\n"
    "v_mfma_f32_16x16x32_bf16 v[68:71], v[76:79], v[36:39], v[68:71]\n"
    "v_add_u32 v11, s77, v2\n"
    "ds_read_b128 v[48:51], v11\n"
    "ds_read_b128 v[52:55], v11 offset:1024\n"
    "s_waitcnt vmcnt(13)\n"
    "v_max_u32 v143, v144, v145\n"
    "v_max3_u32 v143, v146, v147, v143\n"
    "s_waitcnt vmcnt(12)\n"
    "v_max3_u32 v143, v148, v149, v143\n"
    "v_max3_u32 v143, v150, v151, v143\n"
    "v_cmp_eq_u32 vcc, -1, v143\n"
    "s_cbranch_vccnz .Lfb_S133\n"
    ".Lfb_B134:\n"
    "ds_read_b128 v[72:75], v11 offset:2048\n"
    "v_mul_f32 v88, v56, v56\n"
    "v_mul_f32 v90, v57, v57\n"
    "s_waitcnt lgkmcnt(4)\n"
    "v_fma_f32 v88, v88, v81, v80\n"
    "v_fma_f32 v90, v90, v81, v80\n"
    "v_mul_f32 v88, v56, v88\n"
    "v_mul_f32 v90, v57, v90\n"
    "v_exp_f32 v88, v88\n"
    "v_exp_f32 v90, v90\n"
    "v_mul_f32 v89, v56, v64\n"
    "v_mul_f32 v91, v57, v65\n"
    "v_fma_f32 v88, v88, v82, v82\n"
    "v_fma_f32 v90, v90, v82, v82\n"
    "v_rcp_f32 v88, v88\n"
    "v_rcp_f32 v90, v90\n"
    "v_mul_f32 v89, v89, v88\n"
    "v_mul_f32 v91, v91, v90\n"
    "v_cvt_pk_bf16_f32 v96, v89, v91\n"
    "v_mul_f32 v92, v58, v58\n"
    "v_mul_f32 v94, v59, v59\n"
    "v_fma_f32 v92, v92, v81, v80\n"
    "v_fma_f32 v94, v94, v81, v80\n"
    "v_mul_f32 v92, v58, v92\n"
    "v_mul_f32 v94, v59, v94\n"
    "v_exp_f32 v92, v92\n"
    "v_exp_f32 v94, v94\n"
    "v_mul_f32 v93, v58, v66\n"
    "v_mul_f32 v95, v59, v67\n"
    "v_fma_f32 v92, v92, v82, v82\n"
    "v_fma_f32 v94, v94, v82, v82\n"
    "v_rcp_f32 v92, v92\n"
    "v_rcp_f32 v94, v94\n"
    "v_mul_f32 v93, v93, v92\n"
    "v_mul_f32 v95, v95, v94\n"
    "v_cvt_pk_bf16_f32 v97, v93, v95\n"
    "v_mul_f32 v88, v60, v60\n"
    "v_mul_f32 v90, v61, v61\n"
    "s_waitcnt lgkmcnt(3)\n"
    "v_fma_f32 v88, v88, v85, v84\n"
    "v_fma_f32 v90, v90, v85, v84\n"
    "v_mul_f32 v88, v60, v88\n"
    "v_mul_f32 v90, v61, v90\n"
    "v_exp_f32 v88, v88\n"
    "v_exp_f32 v90, v90\n"
    "v_mul_f32 v89, v60, v68\n"
    "v_mul_f32 v91, v61, v69\n"
    "v_fma_f32 v88, v88, v86, v86\n"
    "v_fma_f32 v90, v90, v86, v86\n"
    "v_rcp_f32 v88, v88\n"
    "v_rcp_f32 v90, v90\n"
    "v_mul_f32 v89, v89, v88\n"
    "v_mul_f32 v91, v91, v90\n"
    "v_cvt_pk_bf16_f32 v98, v89, v91\n"
    "v_mul_f32 v92, v62, v62\n"
    "v_mul_f32 v94, v63, v63\n"
    "v_fma_f32 v92, v92, v85, v84\n"
    "v_fma_f32 v94, v94, v85, v84\n"
    "v_mul_f32 v92, v62, v92\n"
    "v_mul_f32 v94, v63, v94\n"
    "v_exp_f32 v92, v92\n"
    "v_exp_f32 v94, v94\n"
    "v_mul_f32 v93, v62, v70\n"
    "v_mul_f32 v95, v63, v71\n"
    "v_fma_f32 v92, v92, v86, v86\n"
    "v_fma_f32 v94, v94, v86, v86\n"
    "v_rcp_f32 v92, v92\n"
    "v_rcp_f32 v94, v94\n"
    "v_mul_f32 v93, v93, v92\n"
    "v_mul_f32 v95, v95, v94\n"
    "v_cvt_pk_bf16_f32 v99, v93, v95\n"
    "s_waitcnt lgkmcnt(2)\n"
    "v_mfma_f32_16x16x32_bf16 v[16:19], v[48:51], v[144:147], 0\n"
    "ds_read_b128 v[76:79], v11 offset:3072\n"
    "buffer_load_dwordx4 v[172:175], v2, s[8:11], s39 offen offset:3072 sc1\n"
    "v_mfma_f32_16x16x32_bf16 v[32:35], v[48:51], v[148:151], 0\n"
    "s_waitcnt lgkmcnt(2)\n"
    "v_permlane16_swap_b32 v96, v98\n"
    "v_permlane16_swap_b32 v97, v99\n"
    "buffer_store_dwordx4 v[96:99], v13, s[8:11], s74 offen sc1\n"
    "v_mfma_f32_16x16x32_bf16 v[20:23], v[52:55], v[144:147], 0\n"
    "ds_read_b128 v[48:51], v11 offset:4096\n"
    "buffer_load_dwordx4 v[176:179], v2, s[8:11], s47 offen sc1\n"
    "v_mfma_f32_16x16x32_bf16 v[36:39], v[52:55], v[148:151], 0\n"
    "s_waitcnt lgkmcnt(2)\n"
    "v_mfma_f32_16x16x32_bf16 v[24:27], v[72:75], v[144:147], 0\n"
    "ds_read_b128 v[52:55], v11 offset:5120\n"
    "buffer_load_dwordx4 v[180:183], v2, s[8:11], s47 offen offset:1024 sc1\n"
    "v_mfma_f32_16x16x32_bf16 v[40:43], v[72:75], v[148:151], 0\n"
    "s_waitcnt vmcnt(15) lgkmcnt(2)\n"
    "v_max_u32 v143, v152, v153\n"
    "v_max3_u32 v143, v154, v155, v143\n"
    "v_mfma_f32_16x16x32_bf16 v[28:31], v[76:79], v[144:147], 0\n"
    "ds_read_b128 v[72:75], v11 offset:6144\n"
    "buffer_load_dwordx4 v[184:187], v2, s[8:11], s47 offen offset:2048 sc1\n"
    "s_waitcnt vmcnt(15)\n"
    "v_max3_u32 v143, v156, v157, v143\n"
    "v_mfma_f32_16x16x32_bf16 v[44:47], v[76:79], v[148:151], 0\n"
    "s_waitcnt lgkmcnt(2)\n"
    "v_max3_u32 v143, v158, v159, v143\n"
    "v_cmp_eq_u32 vcc, -1, v143\n"
    "s_cbranch_vccnz .Lfb_S137\n"
    ".Lfb_B138:\n"
    "v_mfma_f32_16x16x32_bf16 v[16:19], v[48:51], v[152:155], v[16:19]\n"
    "ds_read_b128 v[76:79], v11 offset:7168\n"
    "buffer_load_dwordx4 v[188:191], v2, s[8:11], s47 offen offset:3072 sc1\n"
    "v_mfma_f32_16x16x32_bf16 v[32:35], v[48:51], v[156:159], v[32:35]\n"
    "s_waitcnt lgkmcnt(2)\n"
    "v_mfma_f32_16x16x32_bf16 v[20:23], v[52:55], v[152:155], v[20:23]\n"
    "ds_read_b128 v[48:51], v11 offset:8192\n"
    "v_mfma_f32_16x16x32_bf16 v[36:39], v[52:55], v[156:159], v[36:39]\n"
    "s_waitcnt lgkmcnt(2)\n"
    "v_mfma_f32_16x16x32_bf16 v[24:27], v[72:75], v[152:155], v[24:27]\n"
    "ds_read_b128 v[52:55], v11 offset:9216\n"
    "v_mfma_f32_16x16x32_bf16 v[40:43], v[72:75], v[156:159], v[40:43]\n"
    "s_waitcnt vmcnt(15) lgkmcnt(2)\n"
    "v_max_u32 v143, v160, v161\n"
    "v_max3_u32 v143, v162, v163, v143\n"
    "v_mfma_f32_16x16x32_bf16 v[28:31], v[76:79], v[152:155], v[28:31]\n"
    "ds_read_b128 v[72:75], v11 offset:10240\n"
    "buffer_load_dwordx4 v[224:227], v9, s[8:11], s43 offen sc1\n"
    "s_waitcnt vmcnt(15)\n"
    "v_max3_u32 v143, v164, v165, v143\n"
    "v_mfma_f32_16x16x32_bf16 v[44:47], v[76:79], v[156:159], v[44:47]\n"
    "s_waitcnt lgkmcnt(2)\n"
    "v_max3_u32 v143, v166, v167, v143\n"
    "v_cmp_eq_u32 vcc, -1, v143\n"
    "s_cbranch_vccnz .Lfb_S141\n"
    ".Lfb_B142:\n"
    "v_mfma_f32_16x16x32_bf16 v[16:19], v[48:51], v[160:163], v[16:19]\n"
    "ds_read_b128 v[76:79], v11 offset:11264\n"
    "v_mfma_f32_16x16x32_bf16 v[32:35], v[48:51], v[164:167], v[32:35]\n"
    "s_waitcnt lgkmcnt(2)\n"
    "v_mfma_f32_16x16x32_bf16 v[20:23], v[52:55], v[160:163], v[20:23]\n"
    "ds_read_b128 v[48:51], v11\n"
    "buffer_load_dwordx4 v[228:231], v9, s[8:11], s43 offen offset:512 sc1\n"
    "v_mfma_f32_16x16x32_bf16 v[36:39], v[52:55], v[164:167], v[36:39]\n"
    "s_waitcnt lgkmcnt(2)\n"
    "v_mfma_f32_16x16x32_bf16 v[24:27], v[72:75], v[160:163], v[24:27]\n"
    "ds_read_b128 v[52:55], v11 offset:1024\n"
    "v_mfma_f32_16x16x32_bf16 v[40:43], v[72:75], v[164:167], v[40:43]\n"
    "s_waitcnt vmcnt(15) lgkmcnt(2)\n"
    "v_max_u32 v143, v192, v193\n"
    "v_max3_u32 v143, v194, v195, v143\n"
    "v_mfma_f32_16x16x32_bf16 v[28:31], v[76:79], v[160:163], v[28:31]\n"
    "ds_read_b128 v[72:75], v11 offset:2048\n"
    "buffer_load_dwordx4 v[232:235], v9, s[8:11], s44 offen sc1\n"
    "s_waitcnt vmcnt(15)\n"
    "v_max3_u32 v143, v196, v197, v143\n"
    "v_mfma_f32_16x16x32_bf16 v[44:47], v[76:79], v[164:167], v[44:47]\n"
    "s_waitcnt lgkmcnt(2)\n"
    "v_max3_u32 v143, v198, v199, v143\n"
    "v_cmp_eq_u32 vcc, -1, v143\n"
    "s_cbranch_vccnz .Lfb_S145\n"
    ".Lfb_B146:\n"
    "v_mfma_f32_16x16x32_bf16 v[56:59], v[48:51], v[192:195], 0\n"
    "ds_read_b128 v[76:79], v11 offset:3072\n"
    "v_mfma_f32_16x16x32_bf16 v[80:83], v[48:51], v[196:199], 0\n"
    "s_waitcnt lgkmcnt(2)\n"
    "v_mfma_f32_16x16x32_bf16 v[60:63], v[52:55], v[192:195], 0\n"
    "ds_read_b128 v[48:51], v11 offset:4096\n"
    "buffer_load_dwordx4 v[236:239], v9, s[8:11], s44 offen offset:512 sc1\n"
    "v_mfma_f32_16x16x32_bf16 v[84:87], v[52:55], v[196:199], 0\n"
    "s_waitcnt vmcnt(15) lgkmcnt(2)\n"
    "v_max_u32 v143, v200, v201\n"
    "v_max3_u32 v143, v202, v203, v143\n"
    "v_mfma_f32_16x16x32_bf16 v[64:67], v[72:75], v[192:195], 0\n"
    "ds_read_b128 v[52:55], v11 offset:5120\n"
    "s_waitcnt vmcnt(14)\n"
    "v_max3_u32 v143, v204, v205, v143\n"
    "v_max3_u32 v143, v206, v207, v143\n"
    "v_mfma_f32_16x16x32_bf16 v[88:91], v[72:75], v[196:199], 0\n"
    "s_waitcnt vmcnt(13) lgkmcnt(2)\n"
    "v_max3_u32 v143, v208, v209, v143\n"
    "v_mfma_f32_16x16x32_bf16 v[68:71], v[76:79], v[192:195], 0\n"
    "ds_read_b128 v[72:75], v11 offset:6144\n"
    "buffer_load_dwordx4 v[240:243], v9, s[8:11], s45 offen sc1\n"
    "v_max3_u32 v143, v210, v211, v143\n"
    "s_waitcnt vmcnt(12)\n"
    "v_max3_u32 v143, v212, v213, v143\n"
    "v_mfma_f32_16x16x32_bf16 v[92:95], v[76:79], v[196:199], 0\n"
    "s_waitcnt lgkmcnt(2)\n"
    "v_max3_u32 v143, v214, v215, v143\n"
    "v_cmp_eq_u32 vcc, -1, v143\n"
    "s_cbranch_vccnz .Lfb_S149\n"
    ".Lfb_B150:\n"
    "v_mfma_f32_16x16x32_bf16 v[56:59], v[48:51], v[200:203], v[56:59]\n"
    "ds_read_b128 v[76:79], v11 offset:7168\n"
    "v_and_b32 v14, 1, v1\n"
    "v_lshlrev_b32 v14, 4, v14\n"
    "v_mfma_f32_16x16x32_bf16 v[80:83], v[48:51], v[204:207], v[80:83]\n"
    "s_waitcnt lgkmcnt(2)\n"
    "v_mov_b32 v140, 0x3f80\n"
    "v_mfma_f32_16x16x32_bf16 v[60:63], v[52:55], v[200:203], v[60:63]\n"
    "ds_read_b128 v[48:51], v11 offset:8192\n"
    "buffer_load_dwordx4 v[244:247], v9, s[8:11], s45 offen offset:512 sc1\n"
    "v_lshlrev_b32 v14, v14, v140\n"
    "v_lshrrev_b32 v140, 1, v5\n"
    "v_mfma_f32_16x16x32_bf16 v[84:87], v[52:55], v[204:207], v[84:87]\n"
    "s_waitcnt lgkmcnt(2)\n"
    "v_cmp_eq_u32 s[20:21], 0, v140\n"
    "v_cmp_eq_u32 s[22:23], 1, v140\n"
    "v_mfma_f32_16x16x32_bf16 v[64:67], v[72:75], v[200:203], v[64:67]\n"
    "ds_read_b128 v[52:55], v11 offset:9216\n"
    "v_cndmask_b32 v216, 0, v14, s[20:21]\n"
    "v_cndmask_b32 v217, 0, v14, s[22:23]\n"
    "v_mfma_f32_16x16x32_bf16 v[88:91], v[72:75], v[204:207], v[88:91]\n"
    "s_waitcnt lgkmcnt(2)\n"
    "v_cmp_eq_u32 s[20:21], 2, v140\n"
    "v_cmp_eq_u32 s[22:23], 3, v140\n"
    "v_mfma_f32_16x16x32_bf16 v[68:71], v[76:79], v[200:203], v[68:71]\n"
    "ds_read_b128 v[72:75], v11 offset:10240\n"
    "buffer_load_dwordx4 v[248:251], v9, s[8:11], s46 offen sc1\n"
    "v_cndmask_b32 v218, 0, v14, s[20:21]\n"
    "v_cndmask_b32 v219, 0, v14, s[22:23]\n"
    "v_mfma_f32_16x16x32_bf16 v[92:95], v[76:79], v[204:207], v[92:95]\n"
    "s_waitcnt lgkmcnt(2)\n"
    "v_cmp_eq_u32 s[20:21], 4, v140\n"
    "v_cmp_eq_u32 s[22:23], 5, v140\n"
    "v_mfma_f32_16x16x32_bf16 v[56:59], v[48:51], v[208:211], v[56:59]\n"
    "ds_read_b128 v[76:79], v11 offset:11264\n"
    "v_cndmask_b32 v220, 0, v14, s[20:21]\n"
    "v_cndmask_b32 v221, 0, v14, s[22:23]\n"
    "v_mfma_f32_16x16x32_bf16 v[80:83], v[48:51], v[212:215], v[80:83]\n"
    "s_waitcnt lgkmcnt(2)\n"
    "v_cmp_eq_u32 s[20:21], 6, v140\n"
    "v_cmp_eq_u32 s[22:23], 7, v140\n"
    "v_mfma_f32_16x16x32_bf16 v[60:63], v[52:55], v[208:211], v[60:63]\n"
    "buffer_load_dwordx4 v[252:255], v9, s[8:11], s46 offen offset:512 sc1\n"
    "v_cndmask_b32 v222, 0, v14, s[20:21]\n"
    "v_cndmask_b32 v223, 0, v14, s[22:23]\n"
    "v_mfma_f32_16x16x32_bf16 v[84:87], v[52:55], v[212:215], v[84:87]\n"
    "s_waitcnt lgkmcnt(1)\n"
    "v_mfma_f32_16x16x32_bf16 v[64:67], v[72:75], v[208:211], v[64:67]\n"
    "v_mfma_f32_16x16x32_bf16 v[88:91], v[72:75], v[212:215], v[88:91]\n"
    "s_waitcnt vmcnt(7) lgkmcnt(0)\n"
    "v_max_u32 v143, v224, v225\n"
    "v_max3_u32 v143, v226, v227, v143\n"
    "v_mfma_f32_16x16x32_bf16 v[68:71], v[76:79], v[208:211], v[68:71]\n"
    "s_waitcnt vmcnt(6)\n"
    "v_max3_u32 v143, v228, v229, v143\n"
    "v_mfma_f32_16x16x32_bf16 v[92:95], v[76:79], v[212:215], v[92:95]\n"
    "v_max3_u32 v143, v230, v231, v143\n"
    "v_cmp_eq_u32 vcc, -1, v143\n"
    "s_cbranch_vccnz .Lfb_S153\n"
    ".Lfb_B154:\n"
    "v_mfma_f32_16x16x32_bf16 v[16:19], v[100:103], v[224:227], v[16:19]\n"
    "buffer_load_dwordx4 v[144:147], v10, s[8:11], s43 offen sc1\n"
    "v_and_b32 v14, 7, v1\n"
    "v_bfe_u32 v8, v1, 3, 1\n"
    "s_and_b32 s3, s52, 3\n"
    "s_lshl_b32 s3, s3, 2\n"
    "v_mfma_f32_16x16x32_bf16 v[20:23], v[104:107], v[224:227], v[20:23]\n"
    "v_add_u32 v8, s3, v8\n"
    "v_and_b32 v7, 2, v4\n"
    "v_mfma_f32_16x16x32_bf16 v[24:27], v[108:111], v[224:227], v[24:27]\n"
    "v_add_u32 v7, v7, v8\n"
    "v_mfma_f32_16x16x32_bf16 v[28:31], v[112:115], v[224:227], v[28:31]\n"
    "v_and_b32 v140, 1, v4\n"
    "v_lshl_add_u32 v6, v140, 4, v7\n"
    "v_mfma_f32_16x16x32_bf16 v[32:35], v[100:103], v[228:231], v[32:35]\n"
    "buffer_load_dwordx4 v[148:151], v10, s[8:11], s43 offen offset:512 sc1\n"
    "v_mul_u32_u24 v6, 896, v6\n"
    "v_lshl_add_u32 v6, v14, 4, v6\n"
    "v_mfma_f32_16x16x32_bf16 v[36:39], v[104:107], v[228:231], v[36:39]\n"
    "s_waitcnt vmcnt(7)\n"
    "v_max_u32 v143, v232, v233\n"
    "v_max3_u32 v143, v234, v235, v143\n"
    "v_mfma_f32_16x16x32_bf16 v[40:43], v[108:111], v[228:231], v[40:43]\n"
    "s_waitcnt vmcnt(6)\n"
    "v_max3_u32 v143, v236, v237, v143\n"
    "v_mfma_f32_16x16x32_bf16 v[44:47], v[112:115], v[228:231], v[44:47]\n"
    "v_max3_u32 v143, v238, v239, v143\n"
    "v_cmp_eq_u32 vcc, -1, v143\n"
    "s_cbranch_vccnz .Lfb_S157\n"
    ".Lfb_B158:\n"
    "v_mfma_f32_16x16x32_bf16 v[16:19], v[116:119], v[232:235], v[16:19]\n"
    "buffer_load_dwordx4 v[152:155], v10, s[8:11], s44 offen sc1\n"
    "v_and_b32 v3, 6, v14\n"
    "v_add_u32 v3, v3, v140\n"
    "v_mfma_f32_16x16x32_bf16 v[20:23], v[120:123], v[232:235], v[20:23]\n"
    "v_and_b32 v140, 1, v14\n"
    "v_lshl_add_u32 v3, v3, 1, v140\n"
    "v_mfma_f32_16x16x32_bf16 v[24:27], v[124:127], v[232:235], v[24:27]\n"
    "v_lshl_add_u32 v3, v3, 4, v7\n"
    "v_mfma_f32_16x16x32_bf16 v[28:31], v[128:131], v[232:235], v[28:31]\n"
    "v_lshlrev_b32 v3, 4, v3\n"
    "v_lshlrev_b32 v7, 1, v14\n"
    "v_mfma_f32_16x16x32_bf16 v[32:35], v[116:119], v[236:239], v[32:35]\n"
    "buffer_load_dwordx4 v[156:159], v10, s[8:11], s44 offen offset:512 sc1\n"
    "v_xor_b32 v7, v7, v8\n"
    "v_lshl_add_u32 v7, v14, 5, v7\n"
    "v_mfma_f32_16x16x32_bf16 v[36:39], v[120:123], v[236:239], v[36:39]\n"
    "s_waitcnt vmcnt(7)\n"
    "v_max_u32 v143, v240, v241\n"
    "v_max3_u32 v143, v242, v243, v143\n"
    "v_mfma_f32_16x16x32_bf16 v[40:43], v[124:127], v[236:239], v[40:43]\n"
    "s_waitcnt vmcnt(6)\n"
    "v_max3_u32 v143, v244, v245, v143\n"
    "v_mfma_f32_16x16x32_bf16 v[44:47], v[128:131], v[236:239], v[44:47]\n"
    "v_max3_u32 v143, v246, v247, v143\n"
    "v_cmp_eq_u32 vcc, -1, v143\n"
    "s_cbranch_vccnz .Lfb_S161\n"
    ".Lfb_B162:\n"
    "v_mfma_f32_16x16x32_bf16 v[16:19], v[132:135], v[240:243], v[16:19]\n"
    "buffer_load_dwordx4 v[160:163], v10, s[8:11], s45 offen sc1\n"
    "v_mul_u32_u24 v140, 768, v4\n"
    "v_add_u32 v7, v7, v140\n"
    "s_lshr_b32 s3, s52, 2\n"
    "s_lshl_b32 s4, s3, 12\n"
    "v_mfma_f32_16x16x32_bf16 v[20:23], v[136:139], v[240:243], v[20:23]\n"
    "v_lshl_add_u32 v7, v7, 4, s4\n"
    "v_mfma_f32_16x16x32_bf16 v[24:27], v[168:171], v[240:243], v[24:27]\n"
    "v_xor_b32 v8, 32, v7\n"
    "v_lshlrev_b32 v12, 5, v14\n"
    "v_mfma_f32_16x16x32_bf16 v[28:31], v[172:175], v[240:243], v[28:31]\n"
    "v_add_u32 v12, 160768, v12\n"
    "v_mfma_f32_16x16x32_bf16 v[32:35], v[132:135], v[244:247], v[32:35]\n"
    "buffer_load_dwordx4 v[164:167], v10, s[8:11], s45 offen offset:512 sc1\n"
    "v_and_b32 v96, 2, v4\n"
    "v_xor_b32 v96, v96, v1\n"
    "v_mfma_f32_16x16x32_bf16 v[36:39], v[136:139], v[244:247], v[36:39]\n"
    "s_waitcnt vmcnt(7)\n"
    "v_max_u32 v143, v248, v249\n"
    "v_max3_u32 v143, v250, v251, v143\n"
    "v_mfma_f32_16x16x32_bf16 v[40:43], v[168:171], v[244:247], v[40:43]\n"
    "s_waitcnt vmcnt(6)\n"
    "v_max3_u32 v143, v252, v253, v143\n"
    "v_mfma_f32_16x16x32_bf16 v[44:47], v[172:175], v[244:247], v[44:47]\n"
    "v_max3_u32 v143, v254, v255, v143\n"
    "v_cmp_eq_u32 vcc, -1, v143\n"
    "s_cbranch_vccnz .Lfb_S165\n"
    ".Lfb_B166:\n"
    "v_mfma_f32_16x16x32_bf16 v[16:19], v[176:179], v[248:251], v[16:19]\n"
    "buffer_load_dwordx4 v[192:195], v10, s[8:11], s46 offen sc1\n"
    "v_lshl_add_u32 v96, v96, 4, s77\n"
    "v_mfma_f32_16x16x32_bf16 v[20:23], v[180:183], v[248:251], v[20:23]\n"
    "v_xor_b32 v97, 64, v96\n"
    "v_mfma_f32_16x16x32_bf16 v[24:27], v[184:187], v[248:251], v[24:27]\n"
    "v_xor_b32 v98, 0x80, v96\n"
    "v_mfma_f32_16x16x32_bf16 v[28:31], v[188:191], v[248:251], v[28:31]\n"
    "v_xor_b32 v99, 0xc0, v96\n"
    "v_mfma_f32_16x16x32_bf16 v[32:35], v[176:179], v[252:255], v[32:35]\n"
    "buffer_load_dwordx4 v[196:199], v10, s[8:11], s46 offen offset:512 sc1\n"
    "v_mfma_f32_16x16x32_bf16 v[36:39], v[180:183], v[252:255], v[36:39]\n"
    "s_waitcnt vmcnt(7)\n"
    "v_max_u32 v143, v144, v145\n"
    "v_max3_u32 v143, v146, v147, v143\n"
    "v_mfma_f32_16x16x32_bf16 v[40:43], v[184:187], v[252:255], v[40:43]\n"
    "s_waitcnt vmcnt(6)\n"
    "v_max3_u32 v143, v148, v149, v143\n"
    "v_mfma_f32_16x16x32_bf16 v[44:47], v[188:191], v[252:255], v[44:47]\n"
    "v_max3_u32 v143, v150, v151, v143\n"
    "v_cmp_eq_u32 vcc, -1, v143\n"
    "s_cbranch_vccnz .Lfb_S169\n"
    ".Lfb_B170:\n"
    "v_mfma_f32_16x16x32_bf16 v[56:59], v[100:103], v[144:147], v[56:59]\n"
    "v_mfma_f32_16x16x32_bf16 v[60:63], v[104:107], v[144:147], v[60:63]\n"
    "v_mfma_f32_16x16x32_bf16 v[64:67], v[108:111], v[144:147], v[64:67]\n"
    "buffer_load_dwordx4 v[232:235], v3, s[8:11], s18 offen sc1\n"
    "v_cvt_pk_bf16_f32 v16, v16, v17\n"
    "v_mfma_f32_16x16x32_bf16 v[68:71], v[112:115], v[144:147], v[68:71]\n"
    "v_cvt_pk_bf16_f32 v17, v18, v19\n"
    "v_cvt_pk_bf16_f32 v18, v32, v33\n"
    "v_mfma_f32_16x16x32_bf16 v[80:83], v[100:103], v[148:151], v[80:83]\n"
    "v_cvt_pk_bf16_f32 v19, v34, v35\n"
    "ds_write_b128 v96, v[16:19] offset:0\n"
    "v_mfma_f32_16x16x32_bf16 v[84:87], v[104:107], v[148:151], v[84:87]\n"
    "s_waitcnt vmcnt(6)\n"
    "v_max_u32 v143, v152, v153\n"
    "v_max3_u32 v143, v154, v155, v143\n"
    "v_mfma_f32_16x16x32_bf16 v[88:91], v[108:111], v[148:151], v[88:91]\n"
    "s_waitcnt vmcnt(5)\n"
    "v_max3_u32 v143, v156, v157, v143\n"
    "v_mfma_f32_16x16x32_bf16 v[92:95], v[112:115], v[148:151], v[92:95]\n"
    "v_max3_u32 v143, v158, v159, v143\n"
    "v_cmp_eq_u32 vcc, -1, v143\n"
    "s_cbranch_vccnz .Lfb_S173\n"
    ".Lfb_B174:\n"
    "v_mfma_f32_16x16x32_bf16 v[56:59], v[116:119], v[152:155], v[56:59]\n"
    "v_cvt_pk_bf16_f32 v20, v20, v21\n"
    "v_mfma_f32_16x16x32_bf16 v[60:63], v[120:123], v[152:155], v[60:63]\n"
    "v_cvt_pk_bf16_f32 v21, v22, v23\n"
    "v_cvt_pk_bf16_f32 v22, v36, v37\n"
    "v_mfma_f32_16x16x32_bf16 v[64:67], v[124:127], v[152:155], v[64:67]\n"
    "v_cvt_pk_bf16_f32 v23, v38, v39\n"
    "ds_write_b128 v97, v[20:23] offset:1024\n"
    "v_mfma_f32_16x16x32_bf16 v[68:71], v[128:131], v[152:155], v[68:71]\n"
    "s_waitcnt vmcnt(4)\n"
    "v_max_u32 v143, v160, v161\n"
    "v_max3_u32 v143, v162, v163, v143\n"
    "v_mfma_f32_16x16x32_bf16 v[80:83], v[116:119], v[156:159], v[80:83]\n"
    "s_waitcnt vmcnt(3)\n"
    "v_max3_u32 v143, v164, v165, v143\n"
    "v_max3_u32 v143, v166, v167, v143\n"
    "v_mfma_f32_16x16x32_bf16 v[84:87], v[120:123], v[156:159], v[84:87]\n"
    "s_waitcnt vmcnt(2)\n"
    "v_max3_u32 v143, v192, v193, v143\n"
    "v_mfma_f32_16x16x32_bf16 v[88:91], v[124:127], v[156:159], v[88:91]\n"
    "v_max3_u32 v143, v194, v195, v143\n"
    "s_waitcnt vmcnt(1)\n"
    "v_max3_u32 v143, v196, v197, v143\n"
    "v_mfma_f32_16x16x32_bf16 v[92:95], v[128:131], v[156:159], v[92:95]\n"
    "v_max3_u32 v143, v198, v199, v143\n"
    "v_cmp_eq_u32 vcc, -1, v143\n"
    "s_cbranch_vccnz .Lfb_S177\n"
    ".Lfb_B178:\n"
    "v_mfma_f32_16x16x32_bf16 v[56:59], v[132:135], v[160:163], v[56:59]\n"
    "v_cvt_pk_bf16_f32 v24, v24, v25\n"
    "v_cvt_pk_bf16_f32 v25, v26, v27\n"
    "v_mfma_f32_16x16x32_bf16 v[80:83], v[132:135], v[164:167], v[80:83]\n"
    "v_cvt_pk_bf16_f32 v26, v40, v41\n"
    "v_cvt_pk_bf16_f32 v27, v42, v43\n"
    "ds_write_b128 v98, v[24:27] offset:2048\n"
    "v_mfma_f32_16x16x32_bf16 v[56:59], v[176:179], v[192:195], v[56:59]\n"
    "v_cvt_pk_bf16_f32 v28, v28, v29\n"
    "v_cvt_pk_bf16_f32 v29, v30, v31\n"
    "v_mfma_f32_16x16x32_bf16 v[80:83], v[176:179], v[196:199], v[80:83]\n"
    "v_cvt_pk_bf16_f32 v30, v44, v45\n"
    "v_cvt_pk_bf16_f32 v31, v46, v47\n"
    "ds_write_b128 v99, v[28:31] offset:3072\n"
    "ds_read_b128 v[236:239], v12\n"
    "ds_read_b128 v[240:243], v12 offset:16\n"
    "v_mfma_f32_16x16x32_bf16 v[60:63], v[136:139], v[160:163], v[60:63]\n"
    "s_waitcnt vmcnt(0)\n"
    "v_lshlrev_b32 v244, 16, v232\n"
    "v_and_b32 v245, 0xffff0000, v232\n"
    "v_mfma_f32_16x16x32_bf16 v[84:87], v[136:139], v[164:167], v[84:87]\n"
    "v_lshlrev_b32 v246, 16, v233\n"
    "v_and_b32 v247, 0xffff0000, v233\n"
    "v_mfma_f32_16x16x32_bf16 v[60:63], v[180:183], v[192:195], v[60:63]\n"
    "v_lshlrev_b32 v248, 16, v234\n"
    "v_and_b32 v249, 0xffff0000, v234\n"
    "v_mfma_f32_16x16x32_bf16 v[84:87], v[180:183], v[196:199], v[84:87]\n"
    "v_lshlrev_b32 v250, 16, v235\n"
    "v_and_b32 v251, 0xffff0000, v235\n"
    "v_mfma_f32_16x16x32_bf16 v[64:67], v[168:171], v[160:163], v[64:67]\n"
    "v_cvt_pk_bf16_f32 v56, v56, v57\n"
    "v_mfma_f32_16x16x32_bf16 v[88:91], v[168:171], v[164:167], v[88:91]\n"
    "v_cvt_pk_bf16_f32 v57, v58, v59\n"
    "v_cvt_pk_bf16_f32 v58, v80, v81\n"
    "v_mfma_f32_16x16x32_bf16 v[64:67], v[184:187], v[192:195], v[64:67]\n"
    "v_cvt_pk_bf16_f32 v59, v82, v83\n"
    "ds_write_b128 v96, v[56:59] offset:4096\n"
    "v_mfma_f32_16x16x32_bf16 v[88:91], v[184:187], v[196:199], v[88:91]\n"
    "v_mfma_f32_16x16x32_bf16 v[68:71], v[172:175], v[160:163], v[68:71]\n"
    "v_cvt_pk_bf16_f32 v60, v60, v61\n"
    "v_mfma_f32_16x16x32_bf16 v[92:95], v[172:175], v[164:167], v[92:95]\n"
    "v_cvt_pk_bf16_f32 v61, v62, v63\n"
    "v_cvt_pk_bf16_f32 v62, v84, v85\n"
    "v_mfma_f32_16x16x32_bf16 v[68:71], v[188:191], v[192:195], v[68:71]\n"
    "v_cvt_pk_bf16_f32 v63, v86, v87\n"
    "ds_write_b128 v97, v[60:63] offset:5120\n"
    "v_mfma_f32_16x16x32_bf16 v[92:95], v[188:191], v[196:199], v[92:95]\n"
    "v_cvt_pk_bf16_f32 v64, v64, v65\n"
    "v_cvt_pk_bf16_f32 v65, v66, v67\n"
    "v_cvt_pk_bf16_f32 v66, v88, v89\n"
    "v_cvt_pk_bf16_f32 v67, v90, v91\n"
    "ds_write_b128 v98, v[64:67] offset:6144\n"
    "v_cvt_pk_bf16_f32 v68, v68, v69\n"
    "v_cvt_pk_bf16_f32 v69, v70, v71\n"
    "s_nop 0\n"
    "v_cvt_pk_bf16_f32 v70, v92, v93\n"
    "v_cvt_pk_bf16_f32 v71, v94, v95\n"
    "ds_write_b128 v99, v[68:71] offset:7168\n"
    "s_waitcnt lgkmcnt(0)\n"
    "s_barrier\n"
    "ds_read_b128 v[100:103], v7\n"
    "ds_read_b128 v[104:107], v7 offset:256\n"
    "ds_read_b128 v[108:111], v8\n"
    "ds_read_b128 v[112:115], v8 offset:256\n"
    "ds_read_b128 v[116:119], v7 offset:49152\n"
    "ds_read_b128 v[120:123], v7 offset:49408\n"
    "ds_read_b128 v[124:127], v8 offset:49152\n"
    "ds_read_b128 v[128:131], v8 offset:49408\n"
    "s_waitcnt lgkmcnt(7)\n"
    "v_mfma_f32_16x16x32_bf16 v[16:19], v[216:219], v[100:103], 0\n"
    "s_waitcnt lgkmcnt(6)\n"
    "v_mfma_f32_16x16x32_bf16 v[20:23], v[216:219], v[104:107], 0\n"
    "s_waitcnt lgkmcnt(5)\n"
    "v_mfma_f32_16x16x32_bf16 v[16:19], v[220:223], v[108:111], v[16:19]\n"
    "s_waitcnt lgkmcnt(4)\n"
    "v_mfma_f32_16x16x32_bf16 v[20:23], v[220:223], v[112:115], v[20:23]\n"
    "s_waitcnt lgkmcnt(3)\n"
    "v_mfma_f32_16x16x32_bf16 v[16:19], v[216:219], v[116:119], v[16:19]\n"
    "s_waitcnt lgkmcnt(2)\n"
    "v_mfma_f32_16x16x32_bf16 v[20:23], v[216:219], v[120:123], v[20:23]\n"
    "s_waitcnt lgkmcnt(1)\n"
    "v_mfma_f32_16x16x32_bf16 v[16:19], v[220:223], v[124:127], v[16:19]\n"
    "s_waitcnt lgkmcnt(0)\n"
    "v_mfma_f32_16x16x32_bf16 v[20:23], v[220:223], v[128:131], v[20:23]\n"
    "s_nop 5\n"
    "v_fmac_f32 v244, v236, v16\n"
    "v_fmac_f32 v245, v237, v17\n"
    "v_fmac_f32 v246, v238, v18\n"
    "v_fmac_f32 v247, v239, v19\n"
    "v_fmac_f32 v248, v240, v20\n"
    "v_fmac_f32 v249, v241, v21\n"
    "v_fmac_f32 v250, v242, v22\n"
    "v_fmac_f32 v251, v243, v23\n"
    "v_cvt_pk_bf16_f32 v244, v244, v245\n"
    "v_cvt_pk_bf16_f32 v245, v246, v247\n"
    "v_cvt_pk_bf16_f32 v246, v248, v249\n"
    "v_cvt_pk_bf16_f32 v247, v250, v251\n"
    "buffer_store_dwordx4 v[244:247], v6, s[48:51], s19 offen\n"
    ".Lfb_LEND:\n"
    "s_endpgm\n"
    ".Lfb_S37:\n"
    "s_movk_i32 s64, 2048\n"
    ".Lfb_R39:\n"
    "s_sleep 4\n"
    "buffer_load_dwordx4 v[16:19], v112, s[8:11], s90 offen sc1\n"
    "buffer_load_dwordx4 v[20:23], v112, s[8:11], s90 offen offset:16 sc1\n"
    "buffer_load_dwordx4 v[24:27], v112, s[8:11], s91 offen sc1\n"
    "buffer_load_dwordx4 v[28:31], v112, s[8:11], s91 offen offset:16 sc1\n"
    "buffer_load_dword v32, v113, s[8:11], 0 offen sc1\n"
    "s_waitcnt vmcnt(4)\n"
    "v_xor_b32 v12, s53, v17\n"
    "v_bitop3_b32 v12, v19, s53, v12 bitop3:0xbe\n"
    "s_waitcnt vmcnt(3)\n"
    "v_bitop3_b32 v12, v21, s53, v12 bitop3:0xbe\n"
    "v_bitop3_b32 v12, v23, s53, v12 bitop3:0xbe\n"
    "s_waitcnt vmcnt(2)\n"
    "v_bitop3_b32 v12, v25, s53, v12 bitop3:0xbe\n"
    "v_bitop3_b32 v12, v27, s53, v12 bitop3:0xbe\n"
    "s_waitcnt vmcnt(1)\n"
    "v_bitop3_b32 v12, v29, s53, v12 bitop3:0xbe\n"
    "v_bitop3_b32 v12, v31, s53, v12 bitop3:0xbe\n"
    "s_waitcnt vmcnt(0)\n"
    "v_bitop3_b32 v12, v32, s53, v12 bitop3:0xbe\n"
    "v_cmp_ne_u32 vcc, 0, v12\n"
    "s_cbranch_vccz .Lfb_D40\n"
    "s_sub_u32 s64, s64, 1\n"
    "s_cmp_lg_u32 s64, 0\n"
    "s_cbranch_scc1 .Lfb_R39\n"
    ".Lfb_D40:\n"
    "s_branch .Lfb_B38\n"
    ".Lfb_S52:\n"
    "s_movk_i32 s64, 2048\n"
    ".Lfb_R54:\n"
    "s_sleep 8\n"
    "buffer_load_dword v11, v10, s[8:11], 0 offen sc1\n"
    "s_waitcnt vmcnt(0)\n"
    "v_xor_b32 v13, s53, v11\n"
    "v_cmp_ne_u32 vcc, 0, v13\n"
    "s_cbranch_vccz .Lfb_D55\n"
    "s_sub_u32 s64, s64, 1\n"
    "s_cmp_lg_u32 s64, 0\n"
    "s_cbranch_scc1 .Lfb_R54\n"
    ".Lfb_D55:\n"
    "s_branch .Lfb_B53\n"
    ".Lfb_S78:\n"
    "s_movk_i32 s64, 2048\n"
    ".Lfb_R80:\n"
    "s_sleep 2\n"
    "buffer_load_dword v11, v143, s[8:11], s65 offen sc1\n"
    "s_waitcnt vmcnt(0)\n"
    "v_xor_b32 v13, s53, v11\n"
    "v_cmp_ne_u32 vcc, 0, v13\n"
    "s_cbranch_vccz .Lfb_D81\n"
    "s_sub_u32 s64, s64, 1\n"
    "s_cmp_lg_u32 s64, 0\n"
    "s_cbranch_scc1 .Lfb_R80\n"
    ".Lfb_D81:\n"
    "s_branch .Lfb_B79\n"
    ".Lfb_S82:\n"
    "s_movk_i32 s64, 2048\n"
    ".Lfb_R84:\n"
    "s_sleep 2\n"
    "buffer_load_dword v11, v138, s[8:11], s65 offen sc1\n"
    "s_waitcnt vmcnt(0)\n"
    "v_xor_b32 v13, s53, v11\n"
    "v_cmp_ne_u32 vcc, 0, v13\n"
    "s_cbranch_vccz .Lfb_D85\n"
    "s_sub_u32 s64, s64, 1\n"
    "s_cmp_lg_u32 s64, 0\n"
    "s_cbranch_scc1 .Lfb_R84\n"
    ".Lfb_D85:\n"
    "s_branch .Lfb_B83\n"
    ".Lfb_S86:\n"
    "s_movk_i32 s64, 2048\n"
    ".Lfb_R88:\n"
    "s_sleep 1\n"
    "buffer_load_dwordx4 v[144:147], v2, s[8:11], s17 offen sc1\n"
    "buffer_load_dwordx4 v[148:151], v2, s[8:11], s17 offen offset:1024 sc1\n"
    "buffer_load_dwordx4 v[152:155], v2, s[8:11], s17 offen offset:2048 sc1\n"
    "buffer_load_dwordx4 v[156:159], v2, s[8:11], s17 offen offset:3072 sc1\n"
    "buffer_load_dwordx4 v[160:163], v2, s[8:11], s18 offen sc1\n"
    "buffer_load_dwordx4 v[164:167], v2, s[8:11], s18 offen offset:1024 sc1\n"
    "buffer_load_dwordx4 v[168:171], v2, s[8:11], s18 offen offset:2048 sc1\n"
    "buffer_load_dwordx4 v[172:175], v2, s[8:11], s18 offen offset:3072 sc1\n"
    "s_waitcnt vmcnt(7)\n"
    "v_max_u32 v26, v144, v145\n"
    "v_max3_u32 v26, v146, v147, v26\n"
    "s_waitcnt vmcnt(6)\n"
    "v_max3_u32 v26, v148, v149, v26\n"
    "v_max3_u32 v26, v150, v151, v26\n"
    "s_waitcnt vmcnt(5)\n"
    "v_max3_u32 v26, v152, v153, v26\n"
    "v_max3_u32 v26, v154, v155, v26\n"
    "s_waitcnt vmcnt(4)\n"
    "v_max3_u32 v26, v156, v157, v26\n"
    "v_max3_u32 v26, v158, v159, v26\n"
    "s_waitcnt vmcnt(3)\n"
    "v_max3_u32 v26, v160, v161, v26\n"
    "v_max3_u32 v26, v162, v163, v26\n"
    "s_waitcnt vmcnt(2)\n"
    "v_max3_u32 v26, v164, v165, v26\n"
    "v_max3_u32 v26, v166, v167, v26\n"
    "s_waitcnt vmcnt(1)\n"
    "v_max3_u32 v26, v168, v169, v26\n"
    "v_max3_u32 v26, v170, v171, v26\n"
    "s_waitcnt vmcnt(0)\n"
    "v_max3_u32 v26, v172, v173, v26\n"
    "v_max3_u32 v26, v174, v175, v26\n"
    "v_cmp_eq_u32 vcc, -1, v26\n"
    "s_cbranch_vccz .Lfb_D89\n"
    "s_sub_u32 s64, s64, 1\n"
    "s_cmp_lg_u32 s64, 0\n"
    "s_cbranch_scc1 .Lfb_R88\n"
    ".Lfb_D89:\n"
    "v_mfma_f32_16x16x32_bf16 v[48:51], v[128:131], v[144:147], 0\n"
    "v_mfma_f32_16x16x32_bf16 v[52:55], v[130:133], v[144:147], 0\n"
    "v_mfma_f32_16x16x32_bf16 v[48:51], v[128:131], v[148:151], v[48:51]\n"
    "v_mfma_f32_16x16x32_bf16 v[52:55], v[130:133], v[148:151], v[52:55]\n"
    "v_mfma_f32_16x16x32_bf16 v[48:51], v[128:131], v[152:155], v[48:51]\n"
    "v_mfma_f32_16x16x32_bf16 v[52:55], v[130:133], v[152:155], v[52:55]\n"
    "v_mfma_f32_16x16x32_bf16 v[48:51], v[128:131], v[156:159], v[48:51]\n"
    "v_mfma_f32_16x16x32_bf16 v[52:55], v[130:133], v[156:159], v[52:55]\n"
    "v_mfma_f32_16x16x32_bf16 v[48:51], v[128:131], v[160:163], v[48:51]\n"
    "v_mfma_f32_16x16x32_bf16 v[52:55], v[130:133], v[160:163], v[52:55]\n"
    "v_mfma_f32_16x16x32_bf16 v[48:51], v[128:131], v[164:167], v[48:51]\n"
    "v_mfma_f32_16x16x32_bf16 v[52:55], v[130:133], v[164:167], v[52:55]\n"
    "v_mfma_f32_16x16x32_bf16 v[48:51], v[128:131], v[168:171], v[48:51]\n"
    "v_mfma_f32_16x16x32_bf16 v[52:55], v[130:133], v[168:171], v[52:55]\n"
    "v_mfma_f32_16x16x32_bf16 v[48:51], v[128:131], v[172:175], v[48:51]\n"
    "v_mfma_f32_16x16x32_bf16 v[52:55], v[130:133], v[172:175], v[52:55]\n"
    "s_nop 7\n"
    "s_branch .Lfb_B87\n"
    ".Lfb_S92:\n"
    "s_movk_i32 s64, 2048\n"
    ".Lfb_R94:\n"
    "s_sleep 4\n"
    "buffer_load_dwordx4 v[28:31], v23, s[8:11], s90 offen sc1\n"
    "buffer_load_dwordx4 v[32:35], v23, s[8:11], s91 offen sc1\n"
    "s_waitcnt vmcnt(1)\n"
    "v_xor_b32 v26, s53, v29\n"
    "v_bitop3_b32 v26, v31, s53, v26 bitop3:0xbe\n"
    "s_waitcnt vmcnt(0)\n"
    "v_bitop3_b32 v26, v33, s53, v26 bitop3:0xbe\n"
    "v_bitop3_b32 v26, v35, s53, v26 bitop3:0xbe\n"
    "v_cmp_ne_u32 vcc, 0, v26\n"
    "s_cbranch_vccz .Lfb_D95\n"
    "s_sub_u32 s64, s64, 1\n"
    "s_cmp_lg_u32 s64, 0\n"
    "s_cbranch_scc1 .Lfb_R94\n"
    ".Lfb_D95:\n"
    "s_branch .Lfb_B93\n"
    ".Lfb_S98:\n"
    "s_movk_i32 s64, 2048\n"
    ".Lfb_R100:\n"
    "s_sleep 1\n"
    "buffer_load_dwordx4 v2, s[8:11], s28 offen sc1 lds\n"
    "buffer_load_dwordx4 v2, s[8:11], s28 offen offset:1024 sc1 lds\n"
    "buffer_load_dwordx4 v2, s[8:11], s28 offen offset:2048 sc1 lds\n"
    "buffer_load_dwordx4 v2, s[8:11], s28 offen offset:3072 sc1 lds\n"
    "s_waitcnt vmcnt(0)\n"
    "ds_read_b128 v[40:43], v245\n"
    "ds_read_b128 v[44:47], v245 offset:1024\n"
    "ds_read_b128 v[56:59], v245 offset:2048\n"
    "ds_read_b128 v[60:63], v245 offset:3072\n"
    "s_waitcnt lgkmcnt(3)\n"
    "v_max_u32 v26, v40, v41\n"
    "v_max3_u32 v26, v42, v43, v26\n"
    "s_waitcnt lgkmcnt(2)\n"
    "v_max3_u32 v26, v44, v45, v26\n"
    "v_max3_u32 v26, v46, v47, v26\n"
    "s_waitcnt lgkmcnt(1)\n"
    "v_max3_u32 v26, v56, v57, v26\n"
    "v_max3_u32 v26, v58, v59, v26\n"
    "s_waitcnt lgkmcnt(0)\n"
    "v_max3_u32 v26, v60, v61, v26\n"
    "v_max3_u32 v26, v62, v63, v26\n"
    "v_cmp_eq_u32 vcc, -1, v26\n"
    "s_cbranch_vccz .Lfb_D101\n"
    "s_sub_u32 s64, s64, 1\n"
    "s_cmp_lg_u32 s64, 0\n"
    "s_cbranch_scc1 .Lfb_R100\n"
    ".Lfb_D101:\n"
    "s_branch .Lfb_B99\n"
    ".Lfb_S104:\n"
    "s_movk_i32 s64, 2048\n"
    ".Lfb_R106:\n"
    "s_sleep 8\n"
    "buffer_load_dwordx4 v[28:31], v23, s[8:11], s90 offen sc1\n"
    "buffer_load_dwordx4 v[32:35], v23, s[8:11], s91 offen sc1\n"
    "s_waitcnt vmcnt(1)\n"
    "v_xor_b32 v26, s53, v29\n"
    "v_bitop3_b32 v26, v31, s53, v26 bitop3:0xbe\n"
    "s_waitcnt vmcnt(0)\n"
    "v_bitop3_b32 v26, v33, s53, v26 bitop3:0xbe\n"
    "v_bitop3_b32 v26, v35, s53, v26 bitop3:0xbe\n"
    "v_cmp_ne_u32 vcc, 0, v26\n"
    "s_cbranch_vccz .Lfb_D107\n"
    "s_sub_u32 s64, s64, 1\n"
    "s_cmp_lg_u32 s64, 0\n"
    "s_cbranch_scc1 .Lfb_R106\n"
    ".Lfb_D107:\n"
    "s_branch .Lfb_B105\n"
    ".Lfb_S114:\n"
    "s_movk_i32 s64, 2048\n"
    ".Lfb_R116:\n"
    "s_sleep 1\n"
    "s_add_u32 m0, s75, 0\n"
    "s_mov_b32 exec_hi, 0\n"
    "buffer_load_dwordx4 v139, s[8:11], s66 offen offset:3072 sc1 lds\n"
    "s_mov_b32 exec_hi, -1\n"
    "buffer_load_dwordx4 v136, s[8:11], s66 offen sc1 lds\n"
    "buffer_load_dwordx4 v137, s[8:11], s66 offen offset:1024 sc1 lds\n"
    "buffer_load_dwordx4 v138, s[8:11], s66 offen offset:2048 sc1 lds\n"
    "s_waitcnt vmcnt(0)\n"
    "ds_read_b128 v[100:103], v11\n"
    "ds_read_b128 v[104:107], v11 offset:1024\n"
    "ds_read_b128 v[108:111], v11 offset:2048\n"
    "ds_read_b128 v[112:115], v3 offset:3072\n"
    "s_waitcnt lgkmcnt(3)\n"
    "v_max_u32 v143, v100, v101\n"
    "v_max3_u32 v143, v102, v103, v143\n"
    "s_waitcnt lgkmcnt(2)\n"
    "v_max3_u32 v143, v104, v105, v143\n"
    "v_max3_u32 v143, v106, v107, v143\n"
    "s_waitcnt lgkmcnt(1)\n"
    "v_max3_u32 v143, v108, v109, v143\n"
    "v_max3_u32 v143, v110, v111, v143\n"
    "s_waitcnt lgkmcnt(0)\n"
    "v_max3_u32 v143, v112, v113, v143\n"
    "v_max3_u32 v143, v114, v115, v143\n"
    "v_cmp_eq_u32 vcc, -1, v143\n"
    "s_cbranch_vccz .Lfb_D117\n"
    "s_sub_u32 s64, s64, 1\n"
    "s_cmp_lg_u32 s64, 0\n"
    "s_cbranch_scc1 .Lfb_R116\n"
    ".Lfb_D117:\n"
    "s_branch .Lfb_B115\n"
    ".Lfb_S119:\n"
    "s_movk_i32 s64, 2048\n"
    ".Lfb_R121:\n"
    "s_sleep 1\n"
    "s_add_u32 m0, s75, 28672\n"
    "s_mov_b32 exec_hi, 0\n"
    "buffer_load_dwordx4 v139, s[8:11], s67 offen offset:3072 sc1 lds\n"
    "s_mov_b32 exec_hi, -1\n"
    "buffer_load_dwordx4 v136, s[8:11], s67 offen sc1 lds\n"
    "buffer_load_dwordx4 v137, s[8:11], s67 offen offset:1024 sc1 lds\n"
    "buffer_load_dwordx4 v138, s[8:11], s67 offen offset:2048 sc1 lds\n"
    "s_waitcnt vmcnt(0)\n"
    "ds_read_b128 v[100:103], v11 offset:28672\n"
    "ds_read_b128 v[104:107], v11 offset:29696\n"
    "ds_read_b128 v[108:111], v11 offset:30720\n"
    "ds_read_b128 v[112:115], v3 offset:31744\n"
    "s_waitcnt lgkmcnt(3)\n"
    "v_max_u32 v143, v100, v101\n"
    "v_max3_u32 v143, v102, v103, v143\n"
    "s_waitcnt lgkmcnt(2)\n"
    "v_max3_u32 v143, v104, v105, v143\n"
    "v_max3_u32 v143, v106, v107, v143\n"
    "s_waitcnt lgkmcnt(1)\n"
    "v_max3_u32 v143, v108, v109, v143\n"
    "v_max3_u32 v143, v110, v111, v143\n"
    "s_waitcnt lgkmcnt(0)\n"
    "v_max3_u32 v143, v112, v113, v143\n"
    "v_max3_u32 v143, v114, v115, v143\n"
    "v_cmp_eq_u32 vcc, -1, v143\n"
    "s_cbranch_vccz .Lfb_D122\n"
    "s_sub_u32 s64, s64, 1\n"
    "s_cmp_lg_u32 s64, 0\n"
    "s_cbranch_scc1 .Lfb_R121\n"
    ".Lfb_D122:\n"
    "s_branch .Lfb_B120\n"
    ".Lfb_S124:\n"
    "s_movk_i32 s64, 2048\n"
    ".Lfb_R126:\n"
    "s_sleep 2\n"
    "buffer_load_dwordx4 v[84:87], v2, s[8:11], s16 offen sc1\n"
    "s_waitcnt vmcnt(0)\n"
    "v_max_u32 v143, v84, v85\n"
    "v_max3_u32 v143, v86, v87, v143\n"
    "v_cmp_eq_u32 vcc, -1, v143\n"
    "s_cbranch_vccz .Lfb_D127\n"
    "s_sub_u32 s64, s64, 1\n"
    "s_cmp_lg_u32 s64, 0\n"
    "s_cbranch_scc1 .Lfb_R126\n"
    ".Lfb_D127:\n"
    "s_branch .Lfb_B125\n"
    ".Lfb_S129:\n"
    "s_movk_i32 s64, 2048\n"
    ".Lfb_R131:\n"
    "s_sleep 4\n"
    "buffer_load_dword v142, v141, s[8:11], 0 offen sc1\n"
    "s_waitcnt vmcnt(0)\n"
    "v_xor_b32 v143, s53, v142\n"
    "v_cmp_ne_u32 vcc, 0, v143\n"
    "s_cbranch_vccz .Lfb_D132\n"
    "s_sub_u32 s64, s64, 1\n"
    "s_cmp_lg_u32 s64, 0\n"
    "s_cbranch_scc1 .Lfb_R131\n"
    ".Lfb_D132:\n"
    "s_branch .Lfb_B130\n"
    ".Lfb_S133:\n"
    "s_movk_i32 s64, 2048\n"
    ".Lfb_R135:\n"
    "s_sleep 1\n"
    "buffer_load_dwordx4 v[144:147], v9, s[8:11], s40 offen sc1\n"
    "buffer_load_dwordx4 v[148:151], v9, s[8:11], s40 offen offset:512 sc1\n"
    "s_waitcnt vmcnt(1)\n"
    "v_max_u32 v143, v144, v145\n"
    "v_max3_u32 v143, v146, v147, v143\n"
    "s_waitcnt vmcnt(0)\n"
    "v_max3_u32 v143, v148, v149, v143\n"
    "v_max3_u32 v143, v150, v151, v143\n"
    "v_cmp_eq_u32 vcc, -1, v143\n"
    "s_cbranch_vccz .Lfb_D136\n"
    "s_sub_u32 s64, s64, 1\n"
    "s_cmp_lg_u32 s64, 0\n"
    "s_cbranch_scc1 .Lfb_R135\n"
    ".Lfb_D136:\n"
    "s_branch .Lfb_B134\n"
    ".Lfb_S137:\n"
    "s_movk_i32 s64, 2048\n"
    ".Lfb_R139:\n"
    "s_sleep 1\n"
    "buffer_load_dwordx4 v[152:155], v9, s[8:11], s41 offen sc1\n"
    "buffer_load_dwordx4 v[156:159], v9, s[8:11], s41 offen offset:512 sc1\n"
    "s_waitcnt vmcnt(1)\n"
    "v_max_u32 v143, v152, v153\n"
    "v_max3_u32 v143, v154, v155, v143\n"
    "s_waitcnt vmcnt(0)\n"
    "v_max3_u32 v143, v156, v157, v143\n"
    "v_max3_u32 v143, v158, v159, v143\n"
    "v_cmp_eq_u32 vcc, -1, v143\n"
    "s_cbranch_vccz .Lfb_D140\n"
    "s_sub_u32 s64, s64, 1\n"
    "s_cmp_lg_u32 s64, 0\n"
    "s_cbranch_scc1 .Lfb_R139\n"
    ".Lfb_D140:\n"
    "s_branch .Lfb_B138\n"
    ".Lfb_S141:\n"
    "s_movk_i32 s64, 2048\n"
    ".Lfb_R143:\n"
    "s_sleep 1\n"
    "buffer_load_dwordx4 v[160:163], v9, s[8:11], s42 offen sc1\n"
    "buffer_load_dwordx4 v[164:167], v9, s[8:11], s42 offen offset:512 sc1\n"
    "s_waitcnt vmcnt(1)\n"
    "v_max_u32 v143, v160, v161\n"
    "v_max3_u32 v143, v162, v163, v143\n"
    "s_waitcnt vmcnt(0)\n"
    "v_max3_u32 v143, v164, v165, v143\n"
    "v_max3_u32 v143, v166, v167, v143\n"
    "v_cmp_eq_u32 vcc, -1, v143\n"
    "s_cbranch_vccz .Lfb_D144\n"
    "s_sub_u32 s64, s64, 1\n"
    "s_cmp_lg_u32 s64, 0\n"
    "s_cbranch_scc1 .Lfb_R143\n"
    ".Lfb_D144:\n"
    "s_branch .Lfb_B142\n"
    ".Lfb_S145:\n"
    "s_movk_i32 s64, 2048\n"
    ".Lfb_R147:\n"
    "s_sleep 1\n"
    "buffer_load_dwordx4 v[192:195], v10, s[8:11], s40 offen sc1\n"
    "buffer_load_dwordx4 v[196:199], v10, s[8:11], s40 offen offset:512 sc1\n"
    "s_waitcnt vmcnt(1)\n"
    "v_max_u32 v143, v192, v193\n"
    "v_max3_u32 v143, v194, v195, v143\n"
    "s_waitcnt vmcnt(0)\n"
    "v_max3_u32 v143, v196, v197, v143\n"
    "v_max3_u32 v143, v198, v199, v143\n"
    "v_cmp_eq_u32 vcc, -1, v143\n"
    "s_cbranch_vccz .Lfb_D148\n"
    "s_sub_u32 s64, s64, 1\n"
    "s_cmp_lg_u32 s64, 0\n"
    "s_cbranch_scc1 .Lfb_R147\n"
    ".Lfb_D148:\n"
    "s_branch .Lfb_B146\n"
    ".Lfb_S149:\n"
    "s_movk_i32 s64, 2048\n"
    ".Lfb_R151:\n"
    "s_sleep 1\n"
    "buffer_load_dwordx4 v[200:203], v10, s[8:11], s41 offen sc1\n"
    "buffer_load_dwordx4 v[204:207], v10, s[8:11], s41 offen offset:512 sc1\n"
    "buffer_load_dwordx4 v[208:211], v10, s[8:11], s42 offen sc1\n"
    "buffer_load_dwordx4 v[212:215], v10, s[8:11], s42 offen offset:512 sc1\n"
    "s_waitcnt vmcnt(3)\n"
    "v_max_u32 v143, v200, v201\n"
    "v_max3_u32 v143, v202, v203, v143\n"
    "s_waitcnt vmcnt(2)\n"
    "v_max3_u32 v143, v204, v205, v143\n"
    "v_max3_u32 v143, v206, v207, v143\n"
    "s_waitcnt vmcnt(1)\n"
    "v_max3_u32 v143, v208, v209, v143\n"
    "v_max3_u32 v143, v210, v211, v143\n"
    "s_waitcnt vmcnt(0)\n"
    "v_max3_u32 v143, v212, v213, v143\n"
    "v_max3_u32 v143, v214, v215, v143\n"
    "v_cmp_eq_u32 vcc, -1, v143\n"
    "s_cbranch_vccz .Lfb_D152\n"
    "s_sub_u32 s64, s64, 1\n"
    "s_cmp_lg_u32 s64, 0\n"
    "s_cbranch_scc1 .Lfb_R151\n"
    ".Lfb_D152:\n"
    "s_branch .Lfb_B150\n"
    ".Lfb_S153:\n"
    "s_movk_i32 s64, 2048\n"
    ".Lfb_R155:\n"
    "s_sleep 1\n"
    "buffer_load_dwordx4 v[224:227], v9, s[8:11], s43 offen sc1\n"
    "buffer_load_dwordx4 v[228:231], v9, s[8:11], s43 offen offset:512 sc1\n"
    "s_waitcnt vmcnt(1)\n"
    "v_max_u32 v143, v224, v225\n"
    "v_max3_u32 v143, v226, v227, v143\n"
    "s_waitcnt vmcnt(0)\n"
    "v_max3_u32 v143, v228, v229, v143\n"
    "v_max3_u32 v143, v230, v231, v143\n"
    "v_cmp_eq_u32 vcc, -1, v143\n"
    "s_cbranch_vccz .Lfb_D156\n"
    "s_sub_u32 s64, s64, 1\n"
    "s_cmp_lg_u32 s64, 0\n"
    "s_cbranch_scc1 .Lfb_R155\n"
    ".Lfb_D156:\n"
    "s_branch .Lfb_B154\n"
    ".Lfb_S157:\n"
    "s_movk_i32 s64, 2048\n"
    ".Lfb_R159:\n"
    "s_sleep 1\n"
    "buffer_load_dwordx4 v[232:235], v9, s[8:11], s44 offen sc1\n"
    "buffer_load_dwordx4 v[236:239], v9, s[8:11], s44 offen offset:512 sc1\n"
    "s_waitcnt vmcnt(1)\n"
    "v_max_u32 v143, v232, v233\n"
    "v_max3_u32 v143, v234, v235, v143\n"
    "s_waitcnt vmcnt(0)\n"
    "v_max3_u32 v143, v236, v237, v143\n"
    "v_max3_u32 v143, v238, v239, v143\n"
    "v_cmp_eq_u32 vcc, -1, v143\n"
    "s_cbranch_vccz .Lfb_D160\n"
    "s_sub_u32 s64, s64, 1\n"
    "s_cmp_lg_u32 s64, 0\n"
    "s_cbranch_scc1 .Lfb_R159\n"
    ".Lfb_D160:\n"
    "s_branch .Lfb_B158\n"
    ".Lfb_S161:\n"
    "s_movk_i32 s64, 2048\n"
    ".Lfb_R163:\n"
    "s_sleep 1\n"
    "buffer_load_dwordx4 v[240:243], v9, s[8:11], s45 offen sc1\n"
    "buffer_load_dwordx4 v[244:247], v9, s[8:11], s45 offen offset:512 sc1\n"
    "s_waitcnt vmcnt(1)\n"
    "v_max_u32 v143, v240, v241\n"
    "v_max3_u32 v143, v242, v243, v143\n"
    "s_waitcnt vmcnt(0)\n"
    "v_max3_u32 v143, v244, v245, v143\n"
    "v_max3_u32 v143, v246, v247, v143\n"
    "v_cmp_eq_u32 vcc, -1, v143\n"
    "s_cbranch_vccz .Lfb_D164\n"
    "s_sub_u32 s64, s64, 1\n"
    "s_cmp_lg_u32 s64, 0\n"
    "s_cbranch_scc1 .Lfb_R163\n"
    ".Lfb_D164:\n"
    "s_branch .Lfb_B162\n"
    ".Lfb_S165:\n"
    "s_movk_i32 s64, 2048\n"
    ".Lfb_R167:\n"
    "s_sleep 1\n"
    "buffer_load_dwordx4 v[248:251], v9, s[8:11], s46 offen sc1\n"
    "buffer_load_dwordx4 v[252:255], v9, s[8:11], s46 offen offset:512 sc1\n"
    "s_waitcnt vmcnt(1)\n"
    "v_max_u32 v143, v248, v249\n"
    "v_max3_u32 v143, v250, v251, v143\n"
    "s_waitcnt vmcnt(0)\n"
    "v_max3_u32 v143, v252, v253, v143\n"
    "v_max3_u32 v143, v254, v255, v143\n"
    "v_cmp_eq_u32 vcc, -1, v143\n"
    "s_cbranch_vccz .Lfb_D168\n"
    "s_sub_u32 s64, s64, 1\n"
    "s_cmp_lg_u32 s64, 0\n"
    "s_cbranch_scc1 .Lfb_R167\n"
    ".Lfb_D168:\n"
    "s_branch .Lfb_B166\n"
    ".Lfb_S169:\n"
    "s_movk_i32 s64, 2048\n"
    ".Lfb_R171:\n"
    "s_sleep 1\n"
    "buffer_load_dwordx4 v[144:147], v10, s[8:11], s43 offen sc1\n"
    "buffer_load_dwordx4 v[148:151], v10, s[8:11], s43 offen offset:512 sc1\n"
    "s_waitcnt vmcnt(1)\n"
    "v_max_u32 v143, v144, v145\n"
    "v_max3_u32 v143, v146, v147, v143\n"
    "s_waitcnt vmcnt(0)\n"
    "v_max3_u32 v143, v148, v149, v143\n"
    "v_max3_u32 v143, v150, v151, v143\n"
    "v_cmp_eq_u32 vcc, -1, v143\n"
    "s_cbranch_vccz .Lfb_D172\n"
    "s_sub_u32 s64, s64, 1\n"
    "s_cmp_lg_u32 s64, 0\n"
    "s_cbranch_scc1 .Lfb_R171\n"
    ".Lfb_D172:\n"
    "s_branch .Lfb_B170\n"
    ".Lfb_S173:\n"
    "s_movk_i32 s64, 2048\n"
    ".Lfb_R175:\n"
    "s_sleep 1\n"
    "buffer_load_dwordx4 v[152:155], v10, s[8:11], s44 offen sc1\n"
    "buffer_load_dwordx4 v[156:159], v10, s[8:11], s44 offen offset:512 sc1\n"
    "s_waitcnt vmcnt(1)\n"
    "v_max_u32 v143, v152, v153\n"
    "v_max3_u32 v143, v154, v155, v143\n"
    "s_waitcnt vmcnt(0)\n"
    "v_max3_u32 v143, v156, v157, v143\n"
    "v_max3_u32 v143, v158, v159, v143\n"
    "v_cmp_eq_u32 vcc, -1, v143\n"
    "s_cbranch_vccz .Lfb_D176\n"
    "s_sub_u32 s64, s64, 1\n"
    "s_cmp_lg_u32 s64, 0\n"
    "s_cbranch_scc1 .Lfb_R175\n"
    ".Lfb_D176:\n"
    "s_branch .Lfb_B174\n"
    ".Lfb_S177:\n"
    "s_movk_i32 s64, 2048\n"
    ".Lfb_R179:\n"
    "s_sleep 1\n"
    "buffer_load_dwordx4 v[160:163], v10, s[8:11], s45 offen sc1\n"
    "buffer_load_dwordx4 v[164:167], v10, s[8:11], s45 offen offset:512 sc1\n"
    "buffer_load_dwordx4 v[192:195], v10, s[8:11], s46 offen sc1\n"
    "buffer_load_dwordx4 v[196:199], v10, s[8:11], s46 offen offset:512 sc1\n"
    "s_waitcnt vmcnt(3)\n"
    "v_max_u32 v143, v160, v161\n"
    "v_max3_u32 v143, v162, v163, v143\n"
    "s_waitcnt vmcnt(2)\n"
    "v_max3_u32 v143, v164, v165, v143\n"
    "v_max3_u32 v143, v166, v167, v143\n"
    "s_waitcnt vmcnt(1)\n"
    "v_max3_u32 v143, v192, v193, v143\n"
    "v_max3_u32 v143, v194, v195, v143\n"
    "s_waitcnt vmcnt(0)\n"
    "v_max3_u32 v143, v196, v197, v143\n"
    "v_max3_u32 v143, v198, v199, v143\n"
    "v_cmp_eq_u32 vcc, -1, v143\n"
    "s_cbranch_vccz .Lfb_D180\n"
    "s_sub_u32 s64, s64, 1\n"
    "s_cmp_lg_u32 s64, 0\n"
    "s_cbranch_scc1 .Lfb_R179\n"
    ".Lfb_D180:\n"
    "s_branch .Lfb_B178\n"
    : : "{v0}"(tid), "{s2}"(bid), "{s[0:1]}"(ka)
    : "v1", "v2", "v3", "v4", "v5", "v6", "v7", "v8", "v9", "v10", "v11", "v12", "v13", "v14", "v15", "v16", "v17", "v18", "v19", "v20", "v21", "v22", "v23", "v24", "v25", "v26", "v27", "v28", "v29", "v30", "v31", "v32", "v33", "v34", "v35", "v36", "v37", "v38", "v39", "v40", "v41", "v42", "v43", "v44", "v45", "v46", "v47", "v48", "v49", "v50", "v51", "v52", "v53", "v54", "v55", "v56", "v57", "v58", "v59", "v60", "v61", "v62", "v63", "v64", "v65", "v66", "v67", "v68", "v69", "v70", "v71", "v72", "v73", "v74", "v75", "v76", "v77", "v78", "v79", "v80", "v81", "v82", "v83", "v84", "v85", "v86", "v87", "v88", "v89", "v90", "v91", "v92", "v93", "v94", "v95", "v96", "v97", "v98", "v99", "v100", "v101", "v102", "v103", "v104", "v105", "v106", "v107", "v108", "v109", "v110", "v111", "v112", "v113", "v114", "v115", "v116", "v117", "v118", "v119", "v120", "v121", "v122", "v123", "v124", "v125", "v126", "v127", "v128", "v129", "v130", "v131", "v132", "v133", "v134", "v135", "v136", "v137", "v138", "v139", "v140", "v141", "v142", "v143", "v144", "v145", "v146", "v147", "v148", "v149", "v150", "v151", "v152", "v153", "v154", "v155", "v156", "v157", "v158", "v159", "v160", "v161", "v162", "v163", "v164", "v165", "v166", "v167", "v168", "v169", "v170", "v171", "v172", "v173", "v174", "v175", "v176", "v177", "v178", "v179", "v180", "v181", "v182", "v183", "v184", "v185", "v186", "v187", "v188", "v189", "v190", "v191", "v192", "v193", "v194", "v195", "v196", "v197", "v198", "v199", "v200", "v201", "v202", "v203", "v204", "v205", "v206", "v207", "v208", "v209", "v210", "v211", "v212", "v213", "v214", "v215", "v216", "v217", "v218", "v219", "v220", "v221", "v222", "v223", "v224", "v225", "v226", "v227", "v228", "v229", "v230", "v231", "v232", "v233", "v234", "v235", "v236", "v237", "v238", "v239", "v240", "v241", "v242", "v243", "v244", "v245", "v246", "v247", "v248", "v249", "v250", "v251", "v252", "v253", "v254", "v255", "s3", "s4", "s5", "s6", "s7", "s8", "s9", "s10", "s11", "s12", "s13", "s14", "s15", "s16", "s17", "s18", "s19", "s20", "s21", "s22", "s23", "s24", "s25", "s26", "s27", "s28", "s29", "s30", "s31", "s33", "s34", "s35", "s36", "s37", "s38", "s39", "s40", "s41", "s42", "s43", "s44", "s45", "s46", "s47", "s48", "s49", "s50", "s51", "s52", "s53", "s54", "s55", "s56", "s57", "s58", "s59", "s60", "s61", "s62", "s63", "s64", "s65", "s66", "s67", "s68", "s69", "s70", "s71", "s72", "s73", "s74", "s75", "s76", "s77", "s78", "s79", "s80", "s81", "s82", "s83", "s84", "s85", "s86", "s87", "s88", "s89", "s90", "s91", "s92", "s93", "s94", "s95", "s96", "s97", "s98", "s99", "vcc", "scc", "memory");
#endif
}

extern "C" void kernel_launch(void* const* d_in, const int* in_sizes, int n_in, void* d_out, int out_size, void* d_ws, size_t ws_size,
                              hipStream_t stream) {
  Args a;
  const int order[9] = {0, 1, 9, 8, 2, 3, 5, 6, 7};
  a.d[0] = Desc{d_ws, (unsigned)WS_END, 0x00020000u};
  for (int i = 0; i < 9; i++) a.d[1 + i] = Desc{d_in[order[i]], (unsigned)in_sizes[order[i]] * 2u, 0x00020000u};
  a.d[10] = Desc{d_out, (unsigned)out_size * 2u, 0x00020000u};
  (void)hipFuncSetAttribute((const void*)fused_block, hipFuncAttributeMaxDynamicSharedMemorySize, L_TOTAL);
  fused_block<<<256, 512, L_TOTAL, stream>>>(a);
}
```
